```python
import torch

def _softmax(x, axis=-1):
    return torch.softmax(x, dim=axis)

def _gelu(x):
    return torch.nn.functional.gelu(x, approximate='tanh')

def _silu(x):
    return torch.nn.functional.silu(x)

def _sigmoid(x):
    return torch.sigmoid(x)

def _layernorm(x, axis=-1, eps=1e-05):
    m = x.mean(dim=axis, keepdim=True)
    v = x.var(dim=axis, keepdim=True, unbiased=False)
    return (x - m) / torch.sqrt(v + eps)

def _scatter_add(tgt, idx, src, axis=0):
    return tgt.clone().index_add_(axis, idx, src)

def _segment_sum(src, seg, n):
    out = torch.zeros((n,) + src.shape[1:], dtype=src.dtype)
    return out.index_add_(0, seg, src)

def _pad(x, pw):
    flat = tuple((v for pair in reversed(pw) for v in pair))
    return torch.nn.functional.pad(x, flat)
kernel_name = 'adaLN_cond__mha_block_cached__moe_arch_mod__prompt'

def setup_inputs():

    _w = lambda a: (lambda t: t.to(torch.bfloat16) if t.is_floating_point() else t)(torch.as_tensor(a))

    def _A_setup():
        B, Sq, D = (1, 2048, 512)
        H, Sk, Dh = (32, 2048, 128)
        return {'x': _w((lambda *s: torch.randn(*s, dtype=torch.bfloat16) * 0.1)(B, Sq, D)), 'k_cache': _w((lambda *s: torch.randn(*s, dtype=torch.bfloat16) * 0.8)(B, H, Sk, Dh)), 'v_cache': _w((lambda *s: torch.randn(*s, dtype=torch.bfloat16) * 3.2)(B, H, Sk, Dh)), 'wq': _w((lambda *s: torch.randn(*s, dtype=torch.bfloat16) * 0.8)(D, H * Dh) * D ** (-0.5)), 'wo': _w((lambda *s: torch.randn(*s, dtype=torch.bfloat16) * 3.2)(H * Dh, D) * (H * Dh) ** (-0.5))}
    _A_in = _A_setup()

    def _B_setup():
        B, S, D, F, C = (1, 2048, 512, 1792, 1)
        return {'x': _w((lambda *s: torch.randn(*s, dtype=torch.bfloat16) * 0.1)(B, S, D)), 'w_r': _w((lambda *s: torch.randn(*s, dtype=torch.bfloat16) * 3.2)(D)), 'w_gate': _w((lambda *s: torch.randn(*s, dtype=torch.bfloat16) * 3.2)(D, F)), 'w_up': _w((lambda *s: torch.randn(*s, dtype=torch.bfloat16) * 3.2)(D, F)), 'w_down': _w((lambda *s: torch.randn(*s, dtype=torch.bfloat16) * 3.2)(F, D))}
    _B_in = _B_setup()
    return {'A_x': _A_in['x'], 'A_k_cache': _A_in['k_cache'], 'A_v_cache': _A_in['v_cache'], 'A_wq': _A_in['wq'], 'A_wo': _A_in['wo'], 'B_x': _B_in['x'], 'B_w_r': _B_in['w_r'], 'B_w_gate': _B_in['w_gate'], 'B_w_up': _B_in['w_up'], 'B_w_down': _B_in['w_down'], 'c': _w((lambda *s: torch.randn(*s, dtype=torch.bfloat16) * 0.2)(1, 512) * 8.0), 'w_mod': _w((lambda *s: torch.randn(*s, dtype=torch.bfloat16) * 0.2)(512, 6 * 512) * 512 ** (-0.5) * 8.0)}

def reference(A_x, A_k_cache, A_v_cache, A_wq, A_wo, B_x, B_w_r, B_w_gate, B_w_up, B_w_down, c, w_mod):

    def _A_ref(x, k_cache, v_cache, wq, wo):
        B, Sq, D = x.shape
        H, Dh = (32, 128)
        q = (lambda x, *d: x.permute(*d))((x @ wq).reshape(B, Sq, H, Dh), 0, 2, 1, 3)
        scores = torch.einsum('bhqd,bhkd->bhqk', q, k_cache) * Dh ** (-0.5)
        p = _softmax(scores, axis=-1)
        o = torch.einsum('bhqk,bhkd->bhqd', p, v_cache)
        return (lambda x, *d: x.permute(*d))(o, 0, 2, 1, 3).reshape(B, Sq, H * Dh) @ wo

    def _B_ref(x, w_r, w_gate, w_up, w_down):
        B, S, D = x.shape
        C = 1
        r = x @ w_r
        idx = torch.argsort(-r, axis=-1)[:, :C]
        idx_e = (lambda x, n, axis: torch.repeat_interleave(x, n, dim=axis))(idx.reshape(B, C, 1), D, axis=2)
        x_sel = (lambda x, i, axis: torch.take_along_dim(x, i, dim=axis))(x, idx_e, axis=1)
        g = _silu(x_sel @ w_gate)
        u = x_sel @ w_up
        y = g * u @ w_down
        r_sel = _sigmoid((lambda x, i, axis: torch.take_along_dim(x, i, dim=axis))(r, idx, axis=1))
        y = y * r_sel.reshape(B, C, 1)
        b_off = (torch.arange(B) * S).reshape(B, 1)
        flat_idx = (idx + b_off).reshape(B * C)
        zero = (x * 0.0).reshape(B * S, D)
        upd = _scatter_add(zero, flat_idx, y.reshape(B * C, D), axis=0)
        return x + upd.reshape(B, S, D)
    x = A_x
    mod = (_silu(c) @ w_mod).reshape(c.shape[0], 6, 1, -1)
    sa, ca, ga, sb, cb, gb = (mod[:, 0].clone(), mod[:, 1].clone(), mod[:, 2].clone(), mod[:, 3].clone(), mod[:, 4].clone(), mod[:, 5].clone())
    h = _layernorm(x, axis=-1) * (1 + ca) + sa
    x = x + ga * _A_ref(h, A_k_cache, A_v_cache, A_wq, A_wo)
    h = _layernorm(x, axis=-1) * (1 + cb) + sb
    x = x + gb * _B_ref(h, B_w_r, B_w_gate, B_w_up, B_w_down)
    return x
```

```cpp
#include <hip/hip_runtime.h>
#include <hip/hip_bf16.h>
#include <stdint.h>
typedef unsigned short u16;
__device__ __forceinline__ float bf2f(u16 x){ return __uint_as_float(((unsigned)x)<<16); }
__device__ __forceinline__ u16 f2bf(float f){ __hip_bfloat16 b = __float2bfloat16(f); return *reinterpret_cast<u16*>(&b); }
__device__ __forceinline__ float rbf(float f){ return bf2f(f2bf(f)); }

__global__ void k_mod(const u16* c, const u16* wmod, float* mod){
  int j = blockIdx.x*blockDim.x+threadIdx.x; if(j>=3072) return;
  float acc=0;
  for(int k=0;k<512;k++){ float cv=bf2f(c[k]); float s=rbf(cv/(1.f+__expf(-cv))); acc+=s*bf2f(wmod[k*3072+j]); }
  mod[j]=rbf(acc);
}
__global__ void k_ln(const u16* x, const float* xf, const float* shift, const float* scale, u16* h){
  int row=blockIdx.x; int t=threadIdx.x;
  float v[8]; float s=0;
  for(int i=0;i<8;i++){ int c=t*8+i; v[i]= x? bf2f(x[row*512+c]) : xf[row*512+c]; s+=v[i]; }
  for(int o=32;o;o>>=1) s+=__shfl_xor(s,o);
  float m=s/512.f; float q=0;
  for(int i=0;i<8;i++){ float d=v[i]-m; q+=d*d; }
  for(int o=32;o;o>>=1) q+=__shfl_xor(q,o);
  float rs=1.f/sqrtf(q/512.f+1e-5f);
  for(int i=0;i<8;i++){ int c=t*8+i; h[row*512+c]=f2bf((v[i]-m)*rs*(1.f+scale[c])+shift[c]); }
}
__global__ void k_gemm(const u16* A, const u16* B, u16* C, float* Cf, int M,int N,int K){
  __shared__ float As[16][17], Bs[16][17];
  int tx=threadIdx.x, ty=threadIdx.y; int row=blockIdx.y*16+ty, col=blockIdx.x*16+tx;
  float acc=0;
  for(int k0=0;k0<K;k0+=16){
    As[ty][tx]=bf2f(A[(size_t)row*K+k0+tx]); Bs[ty][tx]=bf2f(B[(size_t)(k0+ty)*N+col]);
    __syncthreads();
    for(int k=0;k<16;k++) acc+=As[ty][k]*Bs[k][tx];
    __syncthreads();
  }
  if(C) C[(size_t)row*N+col]=f2bf(acc);
  if(Cf) Cf[(size_t)row*N+col]=acc;
}
__global__ void k_attn(const u16* Q, const u16* Kc, const u16* Vc, u16* O){
  int row=blockIdx.x, h=blockIdx.y, t=threadIdx.x;
  __shared__ float q[128]; __shared__ float p[2048]; __shared__ float red[128];
  q[t]=bf2f(Q[(size_t)row*4096+h*128+t]);
  __syncthreads();
  float mx=-1e30f;
  for(int k=t;k<2048;k+=128){ const u16* kr=Kc+((size_t)h*2048+k)*128; float s=0; for(int d=0;d<128;d++) s+=q[d]*bf2f(kr[d]); s*=0.08838834764831845f; p[k]=s; mx=fmaxf(mx,s);}
  red[t]=mx; __syncthreads();
  for(int o=64;o;o>>=1){ if(t<o) red[t]=fmaxf(red[t],red[t+o]); __syncthreads(); }
  mx=red[0]; __syncthreads();
  float sm=0; for(int k=t;k<2048;k+=128){ float e=__expf(p[k]-mx); p[k]=e; sm+=e; }
  red[t]=sm; __syncthreads();
  for(int o=64;o;o>>=1){ if(t<o) red[t]+=red[t+o]; __syncthreads(); }
  sm=red[0];
  float acc=0; for(int k=0;k<2048;k++) acc+=p[k]*bf2f(Vc[((size_t)h*2048+k)*128+t]);
  O[(size_t)row*4096+h*128+t]=f2bf(acc/sm);
}
__global__ void k_resid(const u16* x, const float* a, const float* g, float* x2){
  int i=blockIdx.x*blockDim.x+threadIdx.x; int c=i&511; x2[i]=bf2f(x[i])+g[c]*a[i];
}
__global__ void k_router(const u16* h, const u16* wr, float* r){
  int row=blockIdx.x,t=threadIdx.x; float s=0; for(int i=0;i<8;i++){int c=t*8+i; s+=bf2f(h[row*512+c])*bf2f(wr[c]);}
  for(int o=32;o;o>>=1) s+=__shfl_xor(s,o);
  if(t==0) r[row]=s;
}
__global__ void k_moe(const u16* h, const float* r, const u16* wg, const u16* wu, const u16* wd, float* y, int* idxo){
  __shared__ float xs[512]; __shared__ float act[1792]; __shared__ float bv[256]; __shared__ int bi[256];
  int t=threadIdx.x; float best=-1e30f; int b=0; for(int i=t;i<2048;i+=256){ float v=r[i]; if(v>best){best=v;b=i;} }
  bv[t]=best; bi[t]=b; __syncthreads();
  for(int o=128;o;o>>=1){ if(t<o){ if(bv[t+o]>bv[t]||(bv[t+o]==bv[t]&&bi[t+o]<bi[t])){bv[t]=bv[t+o];bi[t]=bi[t+o];} } __syncthreads(); }
  int idx=bi[0]; float rv=bv[0];
  for(int i=t;i<512;i+=256) xs[i]=bf2f(h[idx*512+i]);
  __syncthreads();
  for(int f=t;f<1792;f+=256){ float g=0,u=0; for(int k=0;k<512;k++){ g+=xs[k]*bf2f(wg[k*1792+f]); u+=xs[k]*bf2f(wu[k*1792+f]); } g=rbf(g); u=rbf(u); g=rbf(g/(1.f+__expf(-g))); act[f]=rbf(g*u);}
  __syncthreads();
  float sg=1.f/(1.f+__expf(-rv));
  for(int c=t;c<512;c+=256){ float s=0; for(int f=0;f<1792;f++) s+=act[f]*bf2f(wd[f*512+c]); y[c]=rbf(s)*sg; }
  if(t==0) *idxo=idx;
}
__global__ void k_final(const float* x2, const u16* h2, const float* gb, const float* y, const int* idx, u16* out){
  int i=blockIdx.x*blockDim.x+threadIdx.x; int c=i&511, row=i>>9; float v=bf2f(h2[i]); if(row==*idx) v+=y[c]; out[i]=f2bf(x2[i]+gb[c]*v);
}
extern "C" void kernel_launch(void* const* d_in, const int* in_sizes, int n_in, void* d_out, int out_size, void* d_ws, size_t ws_size, hipStream_t stream){
  const u16* x=(const u16*)d_in[0]; const u16* kc=(const u16*)d_in[1]; const u16* vc=(const u16*)d_in[2]; const u16* wq=(const u16*)d_in[3]; const u16* wo=(const u16*)d_in[4];
  const u16* wr=(const u16*)d_in[6]; const u16* wg=(const u16*)d_in[7]; const u16* wu=(const u16*)d_in[8]; const u16* wd=(const u16*)d_in[9]; const u16* c=(const u16*)d_in[10]; const u16* wm=(const u16*)d_in[11];
  char* ws=(char*)d_ws;
  float* mod=(float*)ws; u16* h=(u16*)(ws+(1<<20)); u16* Q=(u16*)(ws+(4<<20)); u16* O=(u16*)(ws+(24<<20)); float* att=(float*)(ws+(44<<20)); float* x2=(float*)(ws+(50<<20)); u16* h2=(u16*)(ws+(56<<20)); float* r=(float*)(ws+(60<<20)); float* y=(float*)(ws+(61<<20)); int* idx=(int*)(ws+(62<<20));
  k_mod<<<12,256,0,stream>>>(c,wm,mod);
  k_ln<<<2048,64,0,stream>>>(x,nullptr,mod,mod+512,h);
  k_gemm<<<dim3(4096/16,2048/16),dim3(16,16),0,stream>>>(h,wq,Q,nullptr,2048,4096,512);
  k_attn<<<dim3(2048,32),128,0,stream>>>(Q,kc,vc,O);
  k_gemm<<<dim3(512/16,2048/16),dim3(16,16),0,stream>>>(O,wo,nullptr,att,2048,512,4096);
  k_resid<<<2048*512/256,256,0,stream>>>(x,att,mod+1024,x2);
  k_ln<<<2048,64,0,stream>>>(nullptr,x2,mod+1536,mod+2048,h2);
  k_router<<<2048,64,0,stream>>>(h2,wr,r);
  k_moe<<<1,256,0,stream>>>(h2,r,wg,wu,wd,y,idx);
  k_final<<<2048*512/256,256,0,stream>>>(x2,h2,mod+2560,y,idx,(u16*)d_out);
}
```

```cpp
#include <hip/hip_runtime.h>
#include <stdint.h>

typedef unsigned short u16;
typedef unsigned u32x2 __attribute__((ext_vector_type(2)));
typedef unsigned u32x4 __attribute__((ext_vector_type(4)));
typedef unsigned u32x8 __attribute__((ext_vector_type(8)));
typedef float f32x2 __attribute__((ext_vector_type(2)));
typedef float f32x4 __attribute__((ext_vector_type(4)));
typedef float f32x16 __attribute__((ext_vector_type(16)));
typedef __bf16 bf16x2 __attribute__((ext_vector_type(2)));
typedef __bf16 bf16x8 __attribute__((ext_vector_type(8)));
#define DI __device__ __forceinline__

DI unsigned rfl(unsigned x) { return __builtin_amdgcn_readfirstlane(x); }
DI u32x4 mk_srd(const void* p, unsigned bytes) {
  const unsigned long long a = (unsigned long long)p;
  u32x4 r = {rfl((unsigned)a), rfl((unsigned)(a >> 32) & 0xffffu), rfl(bytes), 0x00020000u};
  return r;
}
DI float bf_lo(unsigned w) { return __uint_as_float(w << 16); }
DI float bf_hi(unsigned w) { return __uint_as_float(w & 0xffff0000u); }
DI float bf2f(u16 x) { return __uint_as_float(((unsigned)x) << 16); }
DI unsigned pk2(float lo, float hi) { f32x2 v = {lo, hi}; bf16x2 b = __builtin_convertvector(v, bf16x2); return __builtin_bit_cast(unsigned, b); }
DI float rbf(float f) { return bf_lo(pk2(f, 0.f) & 0xffffu); }
DI float silu_f(float v) { return v / (1.f + __expf(-v)); }

#define RLX_AGENT __ATOMIC_RELAXED, __HIP_MEMORY_SCOPE_AGENT
typedef __attribute__((address_space(1))) unsigned gu32;
typedef __attribute__((address_space(1))) unsigned long long gu64;
DI float dpp_ror8(float v) { return __builtin_bit_cast(float, __builtin_amdgcn_update_dpp(0, __builtin_bit_cast(int, v), 0x128, 0xf, 0xf, true)); }
DI float sum16(float v) { auto r = __builtin_amdgcn_permlane16_swap(__float_as_uint(v), __float_as_uint(v), false, false); return __uint_as_float(r[0]) + __uint_as_float(r[1]); }
DI float sum32(float v) { auto r = __builtin_amdgcn_permlane32_swap(__float_as_uint(v), __float_as_uint(v), false, false); return __uint_as_float(r[0]) + __uint_as_float(r[1]); }
DI float sum_hi3(float v) { v += dpp_ror8(v); return sum32(sum16(v)); }
template <int CTL> DI float dppf(float v) { return __builtin_bit_cast(float, __builtin_amdgcn_update_dpp(0, __builtin_bit_cast(int, v), CTL, 0xf, 0xf, true)); }
template <int CTL> DI unsigned dppu(unsigned v) { return (unsigned)__builtin_amdgcn_update_dpp(0, (int)v, CTL, 0xf, 0xf, true); }
DI float fold32(float a, float b) { auto r = __builtin_amdgcn_permlane32_swap(__float_as_uint(a), __float_as_uint(b), false, false); return __uint_as_float(r[0]) + __uint_as_float(r[1]); }
DI float fold16(float a, float b) { auto r = __builtin_amdgcn_permlane16_swap(__float_as_uint(a), __float_as_uint(b), false, false); return __uint_as_float(r[0]) + __uint_as_float(r[1]); }
DI float fold8(float a, float b, bool hi) { return (hi ? b : a) + dppf<0x128>(hi ? a : b); }
DI float fold4(float a, float b, bool hi) { const float f = hi ? a : b; const float p = dppf<0x124>(f), q = dppf<0x12c>(f); return (hi ? b : a) + (hi ? p : q); }
DI bool better(float ov, unsigned oi, float bv, unsigned bi) { return ov > bv || (ov == bv && oi < bi) || (bv != bv && ov == ov); }

DI void argmax_step(float ov, unsigned oi, float& bv, unsigned& bi) { if (better(ov, oi, bv, bi)) { bv = ov; bi = oi; } }
DI void argmax64(float& bv, unsigned& bi) {
  argmax_step(dppf<0xb1>(bv), dppu<0xb1>(bi), bv, bi);
  argmax_step(dppf<0x4e>(bv), dppu<0x4e>(bi), bv, bi);
  argmax_step(dppf<0x141>(bv), dppu<0x141>(bi), bv, bi);
  argmax_step(dppf<0x128>(bv), dppu<0x128>(bi), bv, bi);
  { auto v = __builtin_amdgcn_permlane16_swap(__float_as_uint(bv), __float_as_uint(bv), false, false); auto i = __builtin_amdgcn_permlane16_swap(bi, bi, false, false);
    bv = __uint_as_float(v[0]); bi = i[0]; argmax_step(__uint_as_float(v[1]), i[1], bv, bi); }
  { auto v = __builtin_amdgcn_permlane32_swap(__float_as_uint(bv), __float_as_uint(bv), false, false); auto i = __builtin_amdgcn_permlane32_swap(bi, bi, false, false);
    bv = __uint_as_float(v[0]); bi = i[0]; argmax_step(__uint_as_float(v[1]), i[1], bv, bi); }
}
DI float addsel(unsigned w, unsigned k, float c) { return __builtin_amdgcn_fdot2_f32_bf16(__builtin_bit_cast(bf16x2, w), __builtin_bit_cast(bf16x2, k), c, false); }
constexpr size_t WS_STAT = 65536;
constexpr size_t WS_MODB = 12288;
constexpr size_t WS_SYNC = 20480;
constexpr size_t WS_H = 1u << 20;
constexpr size_t WS_O = 4u << 20;
constexpr size_t WS_P = 24u << 20;

constexpr int NGU = 56, NDN = 64;
constexpr unsigned SPIN_CAP = 1u << 18;
constexpr int SY_DONE = 1024, SY_ACT = 2048, SY_HDR = 4096, SY_BASE = 4352, SY_ATT = 5376, SY_WORDS = 9472;
DI float sum64(float v) { v += dppf<0xb1>(v); v += dppf<0x4e>(v); v += dppf<0x141>(v); return sum_hi3(v); }
__global__ __launch_bounds__(256) void k_mod(const u16* __restrict__ c, const u16* __restrict__ wm, u16* __restrict__ modb, unsigned* __restrict__ sync,
                                             const u16* __restrict__ wq, const u16* __restrict__ x, float* __restrict__ stats, const u16* __restrict__ wr
                                             ) {
  asm volatile("" :: "s"(c), "s"(wm), "s"(modb), "s"(sync), "s"(wq), "s"(x), "s"(stats), "s"(wr));
  const int t = threadIdx.x, l = t & 63;
  if (blockIdx.x >= 96) {
    const int jb = blockIdx.x - 96, row = jb * 8 + (t >> 6) * 2;
    const u32x4 a = *(const u32x4*)(x + row * 512 + l * 8), b = *(const u32x4*)(x + (row + 1) * 512 + l * 8);
#define NPF 4
    u32x4 pf[4] = {a, a, a, a};
    #pragma unroll
    for (int k = 0; k < NPF; ++k) pf[k] = *(const u32x4*)((const char*)wq + (size_t)k * 1048576 + jb * 4096 + t * 16);
    unsigned cw = 0;
    {
      unsigned long long cb, ce;
      asm volatile("s_getpc_b64 s[20:21]\nLcode_pc0:\n\ts_add_u32 s22, s20, Lcode_begin - Lcode_pc0\n\ts_addc_u32 s23, s21, 0\n\ts_add_u32 s24, s20, Lcode_tail_begin - Lcode_pc0\n\ts_addc_u32 s25, s21, 0"
                   : "={s[22:23]}"(cb), "={s[24:25]}"(ce) :: "s20", "s21", "scc");
      const unsigned long long a0 = (cb & ~255ull) + (unsigned long long)(((jb >> 3) * 4 + (t >> 6)) * 256 + l * 4);
      if (a0 + 4 <= ce) cw = *(const unsigned*)a0;
    }
    unsigned rw = 0;
    if (jb < 8) rw = *(const unsigned*)(wr + 2 * t);
    __builtin_amdgcn_sched_barrier(0);
    float va[8], vb[8];
    #pragma unroll
    for (int e = 0; e < 4; ++e) { va[2 * e] = bf_lo(a[e]); va[2 * e + 1] = bf_hi(a[e]); vb[2 * e] = bf_lo(b[e]); vb[2 * e + 1] = bf_hi(b[e]); }
    float sa = 0, sb = 0;
    #pragma unroll
    for (int e = 0; e < 8; ++e) { sa += va[e]; sb += vb[e]; }
    const float ma = sum64(sa) * (1.f / 512.f), mb = sum64(sb) * (1.f / 512.f);
    float qa = 0, qb = 0;
    #pragma unroll
    for (int e = 0; e < 8; ++e) { const float da = va[e] - ma, dbb = vb[e] - mb; qa = fmaf(da, da, qa); qb = fmaf(dbb, dbb, qb); }
    const float ra = 1.f / sqrtf(sum64(qa) * (1.f / 512.f) + 1e-5f), rb = 1.f / sqrtf(sum64(qb) * (1.f / 512.f) + 1e-5f);
    if (l == 0) *(f32x4*)(stats + row * 2) = f32x4{ma, ra, mb, rb};
    asm volatile("" :: "v"(pf[0][0] ^ pf[1][0] ^ pf[2][0] ^ pf[3][0]));
    asm volatile("" :: "v"(cw));
    asm volatile("" :: "v"(rw));
    return;
  }
  __shared__ float red[4][32];
  const int cq = t & 3, r = t >> 2, col0 = blockIdx.x * 32 + cq * 8;
  u32x4 w[8]; u16 cv[8];
  #pragma unroll
  for (int i = 0; i < 8; ++i) cv[i] = c[r + 64 * i];
  #pragma unroll
  for (int i = 0; i < 8; ++i) w[i] = *(const u32x4*)(wm + (size_t)(r + 64 * i) * 3072 + col0);
  __builtin_amdgcn_sched_barrier(0);
  if (blockIdx.x < SY_WORDS / 256) sync[blockIdx.x * 256 + t] = 0;
  float acc[8] = {0, 0, 0, 0, 0, 0, 0, 0};
  #pragma unroll
  for (int i = 0; i < 8; ++i) {
    const float s = rbf(silu_f(bf2f(cv[i])));
    #pragma unroll
    for (int j = 0; j < 4; ++j) { acc[2 * j] += s * bf_lo(w[i][j]); acc[2 * j + 1] += s * bf_hi(w[i][j]); }
  }
  {
    float b[4], cc[2];
    #pragma unroll
    for (int i = 0; i < 4; ++i) b[i] = fold32(acc[i], acc[i + 4]);
    #pragma unroll
    for (int i = 0; i < 2; ++i) cc[i] = fold16(b[i], b[i + 2]);
    float d = fold8(cc[0], cc[1], l & 8);
    const float p = dppf<0x124>(d), q = dppf<0x12c>(d);
    d += (l & 4) ? p : q;
    if ((l & 4) == 0) red[t >> 6][cq * 8 + (l >> 5) * 4 + ((l >> 4) & 1) * 2 + ((l >> 3) & 1)] = d;
  }
  __syncthreads();
  if (t < 32) { const float v = rbf(red[0][t] + red[1][t] + red[2][t] + red[3][t]); modb[blockIdx.x * 32 + t] = (u16)(__float_as_uint(v) >> 16); }
}

#define LN1_R 2
__global__ __launch_bounds__(256) void k_ln1(const u16* __restrict__ x, const u16* __restrict__ modb, const float* __restrict__ stats, u16* __restrict__ hf) {
  asm volatile("Lcode_begin:");
  asm volatile("" :: "s"(x), "s"(modb), "s"(stats), "s"(hf));
  const int l = threadIdx.x & 63, row0 = blockIdx.x * (4 * LN1_R) + (threadIdx.x >> 6) * LN1_R;
  const int step = l >> 3, ks = (l >> 2) & 1, g = l & 3, k0 = 64 * step + 32 * ks + 4 * g;
  u32x2 a[LN1_R], b[LN1_R]; f32x2 st[LN1_R];
  #pragma unroll
  for (int r = 0; r < LN1_R; ++r) {
    a[r] = *(const u32x2*)(x + (row0 + r) * 512 + k0); b[r] = *(const u32x2*)(x + (row0 + r) * 512 + k0 + 16); st[r] = *(const f32x2*)(stats + (row0 + r) * 2);
  }
  const u32x2 sa0 = *(const u32x2*)(modb + k0), sa1 = *(const u32x2*)(modb + k0 + 16);
  const u32x2 ca0 = *(const u32x2*)(modb + 512 + k0), ca1 = *(const u32x2*)(modb + 512 + k0 + 16);
  __builtin_amdgcn_sched_barrier(0);
  const float C = 0.12751743082f;
  #pragma unroll
  for (int r = 0; r < LN1_R; ++r) {
    const float m = st[r][0], rs = st[r][1];
    u32x4 o;
    #pragma unroll
    for (int e = 0; e < 4; ++e) {
      const unsigned xw = e < 2 ? a[r][e] : b[r][e - 2], sw = e < 2 ? sa0[e] : sa1[e - 2], cw = e < 2 ? ca0[e] : ca1[e - 2];
      o[e] = pk2(fmaf((bf_lo(xw) - m) * rs, rbf(1.f + bf_lo(cw)), bf_lo(sw)) * C, fmaf((bf_hi(xw) - m) * rs, rbf(1.f + bf_hi(cw)), bf_hi(sw)) * C);
    }
    const int row = row0 + r, rb = row >> 6, qq = (row >> 4) & 3, cc = row & 15;
    *(u32x4*)((char*)hf + (size_t)rb * 65536 + step * 8192 + qq * 2048 + ks * 1024 + (16 * g + cc) * 16) = o;
  }
}

DI void attn_slow(const u16* __restrict__ hf, const u16* __restrict__ wq, const u16* __restrict__ kc, const u16* __restrict__ vc, u16* __restrict__ of,
                  unsigned head, unsigned rb, unsigned lane, char* own_lds) {
  const unsigned c = lane & 15u, g = lane >> 4;
  const __amdgpu_buffer_rsrc_t rsO = __builtin_amdgcn_make_buffer_rsrc((char*)of + ((size_t)rfl(head) * 32 + rfl(rb)) * 16384, 0, 16384, 0x00020000);
  for (unsigned q = 0; q < 4; ++q) {
    f32x4 qa[8];
    #pragma unroll
    for (int db = 0; db < 8; ++db) qa[db] = f32x4{0, 0, 0, 0};
    for (unsigned st = 0; st < 16; ++st) {
      const bf16x8 bfr = *(const bf16x8*)((const char*)hf + (size_t)rb * 65536 + (st >> 1) * 8192 + q * 2048 + (st & 1) * 1024 + lane * 16);
      #pragma unroll
      for (int db = 0; db < 8; ++db) {
        u32x4 a;
        #pragma unroll
        for (int j = 0; j < 4; ++j) {
          const unsigned k0 = 32 * st + (j >> 1) * 16 + 4 * g + (j & 1) * 2;
          const u16 lo = wq[(size_t)k0 * 4096 + head * 128 + 16 * db + c], hi = wq[(size_t)(k0 + 1) * 4096 + head * 128 + 16 * db + c];
          a[j] = (unsigned)lo | ((unsigned)hi << 16);
        }
        qa[db] = __builtin_amdgcn_mfma_f32_16x16x32_bf16(__builtin_bit_cast(bf16x8, a), bfr, qa[db], 0, 0, 0);
      }
    }
    bf16x8 qf[4];
    #pragma unroll
    for (int ds = 0; ds < 4; ++ds) {
      u32x4 w = {pk2(qa[2 * ds][0], qa[2 * ds][1]), pk2(qa[2 * ds][2], qa[2 * ds][3]), pk2(qa[2 * ds + 1][0], qa[2 * ds + 1][1]), pk2(qa[2 * ds + 1][2], qa[2 * ds + 1][3])};
      qf[ds] = __builtin_bit_cast(bf16x8, w);
    }
    f32x4 oa[8];
    #pragma unroll
    for (int db = 0; db < 8; ++db) oa[db] = f32x4{0, 0, 0, 0};
    float m = -INFINITY, l = 0.f;
    for (unsigned kt = 0; kt < 64; ++kt) {
      f32x4 s[2];
      #pragma unroll
      for (int kb = 0; kb < 2; ++kb) {
        s[kb] = f32x4{0, 0, 0, 0};
        const u16* kr = kc + ((size_t)head * 2048 + kt * 32 + kb * 16 + c) * 128;
        #pragma unroll
        for (int ds = 0; ds < 4; ++ds) {
          const u32x2 lo = *(const u32x2*)(kr + 32 * ds + 4 * g), hi = *(const u32x2*)(kr + 32 * ds + 16 + 4 * g);
          u32x4 a = {lo[0], lo[1], hi[0], hi[1]};
          s[kb] = __builtin_amdgcn_mfma_f32_16x16x32_bf16(__builtin_bit_cast(bf16x8, a), qf[ds], s[kb], 0, 0, 0);
        }
      }
      float mx = fmaxf(fmaxf(fmaxf(s[0][0], s[0][1]), fmaxf(s[0][2], s[0][3])), fmaxf(fmaxf(s[1][0], s[1][1]), fmaxf(s[1][2], s[1][3])));
      mx = fmaxf(mx, __shfl_xor(mx, 16)); mx = fmaxf(mx, __shfl_xor(mx, 32));
      const float mn = fmaxf(m, mx);
      const float al = (mn == -INFINITY) ? 1.f : exp2f(m - mn);
      const float sub = (mn == -INFINITY) ? 0.f : mn;
      m = mn;
      float p[8], ps = 0.f;
      #pragma unroll
      for (int j = 0; j < 8; ++j) { p[j] = exp2f(s[j >> 2][j & 3] - sub); ps += p[j]; }
      l = l * al + ps;
      u32x4 pw = {pk2(p[0], p[1]), pk2(p[2], p[3]), pk2(p[4], p[5]), pk2(p[6], p[7])};
      const bf16x8 pf = __builtin_bit_cast(bf16x8, pw);
      #pragma unroll
      for (int db = 0; db < 8; ++db) {
        u32x4 a;
        #pragma unroll
        for (int j = 0; j < 4; ++j) {
          const unsigned k0 = kt * 32 + (j >> 1) * 16 + 4 * g + (j & 1) * 2;
          const u16 lo = vc[((size_t)head * 2048 + k0) * 128 + 16 * db + c], hi = vc[((size_t)head * 2048 + k0 + 1) * 128 + 16 * db + c];
          a[j] = (unsigned)lo | ((unsigned)hi << 16);
        }
        oa[db] *= al;
        oa[db] = __builtin_amdgcn_mfma_f32_16x16x32_bf16(__builtin_bit_cast(bf16x8, a), pf, oa[db], 0, 0, 0);
      }
    }
    l += __shfl_xor(l, 16); l += __shfl_xor(l, 32);
    const float il = 1.f / l;
    #pragma unroll
    for (int ds = 0; ds < 4; ++ds) {
      u32x4 w = {pk2(oa[2 * ds][0] * il, oa[2 * ds][1] * il), pk2(oa[2 * ds][2] * il, oa[2 * ds][3] * il),
                 pk2(oa[2 * ds + 1][0] * il, oa[2 * ds + 1][1] * il), pk2(oa[2 * ds + 1][2] * il, oa[2 * ds + 1][3] * il)};
      __builtin_amdgcn_raw_buffer_store_b128(w, rsO, (ds >> 1) * 8192 + q * 2048 + (ds & 1) * 1024 + lane * 16, 0, 16);
      if (own_lds) *(u32x4*)(own_lds + (ds >> 1) * 8192 + q * 2048 + (ds & 1) * 1024 + lane * 16) = w;
    }
  }
}

constexpr unsigned ATTN_LDS = 163840;
__global__ __launch_bounds__(256, 1)
void k_attn(const u16* __restrict__ Ht, const u16* __restrict__ Wq, const u16* __restrict__ Kc, const u16* __restrict__ Vc, u16* __restrict__ O,
            const u16* __restrict__ Wo, const u16* __restrict__ Wg, const u16* __restrict__ Wu, const u16* __restrict__ Wd,
            u16* __restrict__ P, unsigned* __restrict__ sync) {
  asm volatile("" :: "s"(Ht), "s"(Wq), "s"(Kc), "s"(Vc), "s"(O), "s"(Wo), "s"(Wg), "s"(Wu), "s"(Wd), "s"(P), "s"(sync));
  extern __shared__ __attribute__((aligned(16))) char lds[];
  const unsigned ldsb = rfl((unsigned)(size_t)lds);
  const unsigned tid = threadIdx.x, lane = tid & 63u, w = rfl(tid >> 6);
  const unsigned b = blockIdx.x;
  const unsigned head = (b & 7u) * 4u + ((b >> 3) & 3u), qblk = b >> 5;
  const unsigned r16 = lane & 15u, g = lane >> 4, qp = (lane & 15u) >> 2, p = lane & 3u;
  u32x4 kb, dk, wb, wb2, wdv; u32x8 vb; u32x2 dvv;
  const unsigned vrow = 256u * (4u * g + qp), vx = (4u * (g & 1u) + qp) << 1;
  #pragma unroll
  for (unsigned i = 0; i < 8; ++i) vb[i] = ldsb + vrow + 16u * ((2u * i + (p >> 1)) ^ vx) + 8u * (p & 1u);
  #pragma unroll
  for (unsigned i = 0; i < 4; ++i) {
    kb[i] = ldsb + 98304u + 256u * r16 + 16u * ((4u * i + g) ^ r16);
    dk[i] = 256u * g + 16u * (r16 ^ (4u * i + g));
    wb[i] = ldsb + 32768u + vrow + 16u * ((4u * i + p) ^ vx);
    wb2[i] = wb[i] + 65536u;
    wdv[i] = 8192u * g + 16u * (r16 ^ ((4u * (i & 1u) + g) << 1)) + (32768u - 1024u) * i;
  }
  dvv[0] = 256u * g + 16u * (r16 ^ (g << 1));
  dvv[1] = 256u * g + 16u * (r16 ^ ((4u + g) << 1));
  const unsigned lane16 = lane * 16u;
  u32x4 srdK = mk_srd((const char*)Kc + (size_t)head * 524288u, 524288u);
  u32x4 srdV = mk_srd((const char*)Vc + (size_t)head * 524288u - 16384, 524288u + 16384u);
  u32x4 srdH = mk_srd((const char*)Ht + (size_t)(qblk * 4u + w) * 65536u, 65536u);
  u32x4 srdW = mk_srd((const char*)Wq + head * 256u, 512u * 8192u - head * 256u);
  u32x4 srdO = mk_srd((char*)O + ((size_t)head * 32u + qblk * 4u + w) * 16384u, 16384u);
  const unsigned w4k = w * 4096u;
  u32x4 mk = {ldsb + 98304u + w4k, ldsb + 98304u + 16384u + w4k, ldsb + 98304u + 32768u + w4k, ldsb + 98304u + 49152u + w4k};
  u32x4 mv = {ldsb + w4k, ldsb + 16384u + w4k, ldsb + 32768u + w4k, ldsb + 49152u + w4k};
  u32x4 mw = {ldsb + 32768u + w4k, ldsb + 49152u + w4k, ldsb + 65536u + w4k, ldsb + 81920u + w4k};
  const unsigned wi = b * 4u + w;
  const char* pf; unsigned pfn;
  if (wi < 293u) { pf = (const char*)Wo + wi * 14336u; pfn = wi == 292u ? 4194304u - 292u * 14336u : 14336u; }
  else if (wi < 677u) { const unsigned k = wi - 293u; pf = (const char*)(k < 128u ? Wg : k < 256u ? Wu : Wd) + (k & 127u) * 14336u; pfn = 14336u; }
  else {
    unsigned long long cb, ce;
    asm volatile("s_getpc_b64 s[90:91]\nLcode_pc:\n\ts_add_u32 s92, s90, Lcode_tail_begin - Lcode_pc\n\ts_addc_u32 s93, s91, 0\n\ts_add_u32 s94, s90, Lcode_end - Lcode_pc\n\ts_addc_u32 s95, s91, 0"
                 : "={s[92:93]}"(cb), "={s[94:95]}"(ce) :: "s90", "s91", "scc");
    const unsigned ci = b >= 170u ? ((b - 170u) >> 3) * 4u + w : 0xffffu;
    const unsigned long long tgt = cb + (unsigned long long)ci * 1024u;
    const bool on = b >= 170u && tgt + 1024u <= ce;
    pf = on ? (const char*)(tgt - 13312u) : (const char*)Wo; pfn = on ? 14336u : 0u;
  }
  u32x4 srdPF = mk_srd(pf, pfn);
  const unsigned mpf = ldsb + 65536u + w4k;
  const unsigned s4 = head >> 3, rh = (head >> 2) & 1u, n = head & 3u;
  u32x4 srdWO = mk_srd((const char*)Wo + (size_t)s4 * 1024u * 1024u + n * 256u, 1024u * 1024u - n * 256u);
  u32x4 wdo;
  #pragma unroll
  for (unsigned i = 0; i < 4; ++i) wdo[i] = 1024u * g + 16u * (r16 ^ ((4u * (i & 1u) + g) << 1)) + (4096u - 1024u) * i;
  unsigned swo = (head & 7u) * 131072u + w4k * 4u;
  unsigned shb = ((head + 1u) & 7u) * 524288u;
  const unsigned sown = (w >> 1) == rh ? ldsb + 32768u + (w & 1u) * 16384u : 0xffffffffu;
  const unsigned sownr = ldsb + 32768u + (w >> 1) * 16384u + (w & 1u) * 4096u;
  u32x4 srdB = mk_srd((const char*)O + ((size_t)s4 * 8u * 32u + qblk * 4u + rh * 2u + (w >> 1)) * 16384u + (w & 1u) * 4096u, 7u * 524288u + 16384u);
  u32x4 srdP = mk_srd((char*)P + (size_t)(16u * qblk + 8u * rh + 2u * w) * 65536u + s4 * 16384u + n * 4096u, 65536u + 4096u);
  u32x4 srdF = mk_srd((const char*)(sync + SY_ATT) + (qblk * 32u + s4 * 8u) * 64u, 512u);
  const unsigned sfl = (head & 7u) * 64u;
  const unsigned mlw = ldsb + w4k;
  unsigned flag;
  asm volatile(
    "s_mov_b32 s50, s29\n\t"
    "s_lshl_b32 s50, s50, 5\n\t"
    "s_mov_b32 s51, 0\n\t"
    "s_mov_b32 m0, s40\n\t"
    "s_nop 0\n\t"
    "buffer_load_dwordx4 v228, s[20:23], s50 offen lds\n\t"
    "buffer_load_dwordx4 v229, s[20:23], s50 offen offset:1024 lds\n\t"
    "buffer_load_dwordx4 v230, s[20:23], s50 offen offset:2048 lds\n\t"
    "buffer_load_dwordx4 v231, s[20:23], s50 offen offset:3072 lds\n\t"
    "s_add_u32 s50, s50, 524288\n\t"
    "buffer_load_dwordx4 a[64:67], v232, s[16:19], s51 offen\n\t"
    "buffer_load_dwordx4 a[68:71], v232, s[16:19], s51 offen offset:1024\n\t"
    "buffer_load_dwordx4 a[72:75], v232, s[16:19], s51 offen offset:2048\n\t"
    "buffer_load_dwordx4 a[76:79], v232, s[16:19], s51 offen offset:3072\n\t"
    "s_add_u32 s51, s51, 4096\n\t"
    "buffer_load_dwordx4 a[80:83], v232, s[16:19], s51 offen\n\t"
    "buffer_load_dwordx4 a[84:87], v232, s[16:19], s51 offen offset:1024\n\t"
    "buffer_load_dwordx4 a[88:91], v232, s[16:19], s51 offen offset:2048\n\t"
    "buffer_load_dwordx4 a[92:95], v232, s[16:19], s51 offen offset:3072\n\t"
    "s_add_u32 s51, s51, 4096\n\t"
    "s_mov_b32 m0, s41\n\t"
    "s_nop 0\n\t"
    "buffer_load_dwordx4 v228, s[20:23], s50 offen lds\n\t"
    "buffer_load_dwordx4 v229, s[20:23], s50 offen offset:1024 lds\n\t"
    "buffer_load_dwordx4 v230, s[20:23], s50 offen offset:2048 lds\n\t"
    "buffer_load_dwordx4 v231, s[20:23], s50 offen offset:3072 lds\n\t"
    "s_add_u32 s50, s50, 524288\n\t"
    "buffer_load_dwordx4 a[96:99], v232, s[16:19], s51 offen\n\t"
    "buffer_load_dwordx4 a[100:103], v232, s[16:19], s51 offen offset:1024\n\t"
    "buffer_load_dwordx4 a[104:107], v232, s[16:19], s51 offen offset:2048\n\t"
    "buffer_load_dwordx4 a[108:111], v232, s[16:19], s51 offen offset:3072\n\t"
    "s_add_u32 s51, s51, 4096\n\t"
    "buffer_load_dwordx4 a[112:115], v232, s[16:19], s51 offen\n\t"
    "buffer_load_dwordx4 a[116:119], v232, s[16:19], s51 offen offset:1024\n\t"
    "buffer_load_dwordx4 a[120:123], v232, s[16:19], s51 offen offset:2048\n\t"
    "buffer_load_dwordx4 a[124:127], v232, s[16:19], s51 offen offset:3072\n\t"
    "s_add_u32 s51, s51, 4096\n\t"
    "s_waitcnt vmcnt(12)\n\t"
    "s_barrier\n\t"
    "ds_read_b64_tr_b16 a[0:1], v224 offset:0\n\t"
    "ds_read_b64_tr_b16 a[2:3], v224 offset:4096\n\t"
    "ds_read_b64_tr_b16 a[4:5], v224 offset:8192\n\t"
    "ds_read_b64_tr_b16 a[6:7], v224 offset:12288\n\t"
    "ds_read_b64_tr_b16 a[8:9], v224 offset:8\n\t"
    "ds_read_b64_tr_b16 a[10:11], v224 offset:4104\n\t"
    "ds_read_b64_tr_b16 a[12:13], v224 offset:8200\n\t"
    "ds_read_b64_tr_b16 a[14:15], v224 offset:12296\n\t"
    "s_waitcnt lgkmcnt(4)\n\t"
    "v_mfma_f32_16x16x32_bf16 v[0:3], a[0:3], a[64:67], 0\n\t"
    "ds_read_b64_tr_b16 a[16:17], v225 offset:0\n\t"
    "v_mfma_f32_16x16x32_bf16 v[0:3], a[4:7], a[68:71], v[0:3]\n\t"
    "ds_read_b64_tr_b16 a[18:19], v225 offset:4096\n\t"
    "v_mfma_f32_16x16x32_bf16 v[4:7], a[0:3], a[72:75], 0\n\t"
    "ds_read_b64_tr_b16 a[20:21], v225 offset:8192\n\t"
    "v_mfma_f32_16x16x32_bf16 v[4:7], a[4:7], a[76:79], v[4:7]\n\t"
    "ds_read_b64_tr_b16 a[22:23], v225 offset:12288\n\t"
    "v_mfma_f32_16x16x32_bf16 v[8:11], a[0:3], a[80:83], 0\n\t"
    "s_mov_b32 m0, s42\n\t"
    "s_nop 0\n\t"
    "buffer_load_dwordx4 v228, s[20:23], s50 offen lds\n\t"
    "v_mfma_f32_16x16x32_bf16 v[8:11], a[4:7], a[84:87], v[8:11]\n\t"
    "v_mfma_f32_16x16x32_bf16 v[12:15], a[0:3], a[88:91], 0\n\t"
    "buffer_load_dwordx4 v229, s[20:23], s50 offen offset:1024 lds\n\t"
    "v_mfma_f32_16x16x32_bf16 v[12:15], a[4:7], a[92:95], v[12:15]\n\t"
    "s_waitcnt lgkmcnt(4)\n\t"
    "v_mfma_f32_16x16x32_bf16 v[16:19], a[8:11], a[64:67], 0\n\t"
    "ds_read_b64_tr_b16 a[24:25], v225 offset:8\n\t"
    "v_mfma_f32_16x16x32_bf16 v[16:19], a[12:15], a[68:71], v[16:19]\n\t"
    "ds_read_b64_tr_b16 a[26:27], v225 offset:4104\n\t"
    "v_mfma_f32_16x16x32_bf16 v[20:23], a[8:11], a[72:75], 0\n\t"
    "ds_read_b64_tr_b16 a[28:29], v225 offset:8200\n\t"
    "v_mfma_f32_16x16x32_bf16 v[20:23], a[12:15], a[76:79], v[20:23]\n\t"
    "ds_read_b64_tr_b16 a[30:31], v225 offset:12296\n\t"
    "v_mfma_f32_16x16x32_bf16 v[24:27], a[8:11], a[80:83], 0\n\t"
    "v_mfma_f32_16x16x32_bf16 v[24:27], a[12:15], a[84:87], v[24:27]\n\t"
    "buffer_load_dwordx4 v230, s[20:23], s50 offen offset:2048 lds\n\t"
    "v_mfma_f32_16x16x32_bf16 v[28:31], a[8:11], a[88:91], 0\n\t"
    "v_mfma_f32_16x16x32_bf16 v[28:31], a[12:15], a[92:95], v[28:31]\n\t"
    "buffer_load_dwordx4 v231, s[20:23], s50 offen offset:3072 lds\n\t"
    "s_add_u32 s50, s50, 524288\n\t"
    "s_waitcnt lgkmcnt(4)\n\t"
    "v_mfma_f32_16x16x32_bf16 v[32:35], a[16:19], a[64:67], 0\n\t"
    "ds_read_b64_tr_b16 a[32:33], v226 offset:0\n\t"
    "v_mfma_f32_16x16x32_bf16 v[32:35], a[20:23], a[68:71], v[32:35]\n\t"
    "ds_read_b64_tr_b16 a[34:35], v226 offset:4096\n\t"
    "v_mfma_f32_16x16x32_bf16 v[36:39], a[16:19], a[72:75], 0\n\t"
    "ds_read_b64_tr_b16 a[36:37], v226 offset:8192\n\t"
    "v_mfma_f32_16x16x32_bf16 v[36:39], a[20:23], a[76:79], v[36:39]\n\t"
    "ds_read_b64_tr_b16 a[38:39], v226 offset:12288\n\t"
    "v_mfma_f32_16x16x32_bf16 v[40:43], a[16:19], a[80:83], 0\n\t"
    "buffer_load_dwordx4 a[192:195], v232, s[16:19], s51 offen\n\t"
    "v_mfma_f32_16x16x32_bf16 v[40:43], a[20:23], a[84:87], v[40:43]\n\t"
    "v_mfma_f32_16x16x32_bf16 v[44:47], a[16:19], a[88:91], 0\n\t"
    "buffer_load_dwordx4 a[196:199], v232, s[16:19], s51 offen offset:1024\n\t"
    "v_mfma_f32_16x16x32_bf16 v[44:47], a[20:23], a[92:95], v[44:47]\n\t"
    "s_waitcnt lgkmcnt(4)\n\t"
    "v_mfma_f32_16x16x32_bf16 v[48:51], a[24:27], a[64:67], 0\n\t"
    "ds_read_b64_tr_b16 a[40:41], v226 offset:8\n\t"
    "v_mfma_f32_16x16x32_bf16 v[48:51], a[28:31], a[68:71], v[48:51]\n\t"
    "ds_read_b64_tr_b16 a[42:43], v226 offset:4104\n\t"
    "v_mfma_f32_16x16x32_bf16 v[52:55], a[24:27], a[72:75], 0\n\t"
    "ds_read_b64_tr_b16 a[44:45], v226 offset:8200\n\t"
    "v_mfma_f32_16x16x32_bf16 v[52:55], a[28:31], a[76:79], v[52:55]\n\t"
    "ds_read_b64_tr_b16 a[46:47], v226 offset:12296\n\t"
    "v_mfma_f32_16x16x32_bf16 v[56:59], a[24:27], a[80:83], 0\n\t"
    "v_mfma_f32_16x16x32_bf16 v[56:59], a[28:31], a[84:87], v[56:59]\n\t"
    "buffer_load_dwordx4 a[200:203], v232, s[16:19], s51 offen offset:2048\n\t"
    "v_mfma_f32_16x16x32_bf16 v[60:63], a[24:27], a[88:91], 0\n\t"
    "v_mfma_f32_16x16x32_bf16 v[60:63], a[28:31], a[92:95], v[60:63]\n\t"
    "buffer_load_dwordx4 a[204:207], v232, s[16:19], s51 offen offset:3072\n\t"
    "s_add_u32 s51, s51, 4096\n\t"
    "s_waitcnt lgkmcnt(4)\n\t"
    "v_mfma_f32_16x16x32_bf16 v[64:67], a[32:35], a[64:67], 0\n\t"
    "ds_read_b64_tr_b16 a[48:49], v227 offset:0\n\t"
    "v_mfma_f32_16x16x32_bf16 v[64:67], a[36:39], a[68:71], v[64:67]\n\t"
    "ds_read_b64_tr_b16 a[50:51], v227 offset:4096\n\t"
    "v_mfma_f32_16x16x32_bf16 v[68:71], a[32:35], a[72:75], 0\n\t"
    "ds_read_b64_tr_b16 a[52:53], v227 offset:8192\n\t"
    "v_mfma_f32_16x16x32_bf16 v[68:71], a[36:39], a[76:79], v[68:71]\n\t"
    "ds_read_b64_tr_b16 a[54:55], v227 offset:12288\n\t"
    "v_mfma_f32_16x16x32_bf16 v[72:75], a[32:35], a[80:83], 0\n\t"
    "buffer_load_dwordx4 a[208:211], v232, s[16:19], s51 offen\n\t"
    "v_mfma_f32_16x16x32_bf16 v[72:75], a[36:39], a[84:87], v[72:75]\n\t"
    "v_mfma_f32_16x16x32_bf16 v[76:79], a[32:35], a[88:91], 0\n\t"
    "buffer_load_dwordx4 a[212:215], v232, s[16:19], s51 offen offset:1024\n\t"
    "v_mfma_f32_16x16x32_bf16 v[76:79], a[36:39], a[92:95], v[76:79]\n\t"
    "s_waitcnt lgkmcnt(4)\n\t"
    "v_mfma_f32_16x16x32_bf16 v[80:83], a[40:43], a[64:67], 0\n\t"
    "ds_read_b64_tr_b16 a[56:57], v227 offset:8\n\t"
    "v_mfma_f32_16x16x32_bf16 v[80:83], a[44:47], a[68:71], v[80:83]\n\t"
    "ds_read_b64_tr_b16 a[58:59], v227 offset:4104\n\t"
    "v_mfma_f32_16x16x32_bf16 v[84:87], a[40:43], a[72:75], 0\n\t"
    "ds_read_b64_tr_b16 a[60:61], v227 offset:8200\n\t"
    "v_mfma_f32_16x16x32_bf16 v[84:87], a[44:47], a[76:79], v[84:87]\n\t"
    "ds_read_b64_tr_b16 a[62:63], v227 offset:12296\n\t"
    "v_mfma_f32_16x16x32_bf16 v[88:91], a[40:43], a[80:83], 0\n\t"
    "v_mfma_f32_16x16x32_bf16 v[88:91], a[44:47], a[84:87], v[88:91]\n\t"
    "buffer_load_dwordx4 a[216:219], v232, s[16:19], s51 offen offset:2048\n\t"
    "v_mfma_f32_16x16x32_bf16 v[92:95], a[40:43], a[88:91], 0\n\t"
    "v_mfma_f32_16x16x32_bf16 v[92:95], a[44:47], a[92:95], v[92:95]\n\t"
    "buffer_load_dwordx4 a[220:223], v232, s[16:19], s51 offen offset:3072\n\t"
    "s_add_u32 s51, s51, 4096\n\t"
    "s_waitcnt lgkmcnt(0)\n\t"
    "s_waitcnt vmcnt(12)\n\t"
    "s_barrier\n\t"
    "v_mfma_f32_16x16x32_bf16 v[160:163], a[48:51], a[64:67], 0\n\t"
    "ds_read_b64_tr_b16 a[0:1], v224 offset:16384\n\t"
    "v_mfma_f32_16x16x32_bf16 v[160:163], a[52:55], a[68:71], v[160:163]\n\t"
    "ds_read_b64_tr_b16 a[2:3], v224 offset:20480\n\t"
    "v_mfma_f32_16x16x32_bf16 v[164:167], a[48:51], a[72:75], 0\n\t"
    "ds_read_b64_tr_b16 a[4:5], v224 offset:24576\n\t"
    "v_mfma_f32_16x16x32_bf16 v[164:167], a[52:55], a[76:79], v[164:167]\n\t"
    "ds_read_b64_tr_b16 a[6:7], v224 offset:28672\n\t"
    "v_mfma_f32_16x16x32_bf16 v[168:171], a[48:51], a[80:83], 0\n\t"
    "v_mfma_f32_16x16x32_bf16 v[168:171], a[52:55], a[84:87], v[168:171]\n\t"
    "s_mov_b32 m0, s43\n\t"
    "s_nop 0\n\t"
    "buffer_load_dwordx4 v228, s[20:23], s50 offen lds\n\t"
    "v_mfma_f32_16x16x32_bf16 v[172:175], a[48:51], a[88:91], 0\n\t"
    "v_mfma_f32_16x16x32_bf16 v[172:175], a[52:55], a[92:95], v[172:175]\n\t"
    "s_waitcnt lgkmcnt(4)\n\t"
    "v_mfma_f32_16x16x32_bf16 v[176:179], a[56:59], a[64:67], 0\n\t"
    "ds_read_b64_tr_b16 a[8:9], v224 offset:16392\n\t"
    "v_mfma_f32_16x16x32_bf16 v[176:179], a[60:63], a[68:71], v[176:179]\n\t"
    "ds_read_b64_tr_b16 a[10:11], v224 offset:20488\n\t"
    "v_mfma_f32_16x16x32_bf16 v[180:183], a[56:59], a[72:75], 0\n\t"
    "ds_read_b64_tr_b16 a[12:13], v224 offset:24584\n\t"
    "v_mfma_f32_16x16x32_bf16 v[180:183], a[60:63], a[76:79], v[180:183]\n\t"
    "ds_read_b64_tr_b16 a[14:15], v224 offset:28680\n\t"
    "v_mfma_f32_16x16x32_bf16 v[184:187], a[56:59], a[80:83], 0\n\t"
    "v_mfma_f32_16x16x32_bf16 v[184:187], a[60:63], a[84:87], v[184:187]\n\t"
    "buffer_load_dwordx4 v229, s[20:23], s50 offen offset:1024 lds\n\t"
    "v_mfma_f32_16x16x32_bf16 v[188:191], a[56:59], a[88:91], 0\n\t"
    "v_mfma_f32_16x16x32_bf16 v[188:191], a[60:63], a[92:95], v[188:191]\n\t"
    "buffer_load_dwordx4 v230, s[20:23], s50 offen offset:2048 lds\n\t"
    "s_waitcnt lgkmcnt(4)\n\t"
    "v_mfma_f32_16x16x32_bf16 v[0:3], a[0:3], a[96:99], v[0:3]\n\t"
    "ds_read_b64_tr_b16 a[16:17], v225 offset:16384\n\t"
    "v_mfma_f32_16x16x32_bf16 v[0:3], a[4:7], a[100:103], v[0:3]\n\t"
    "ds_read_b64_tr_b16 a[18:19], v225 offset:20480\n\t"
    "v_mfma_f32_16x16x32_bf16 v[4:7], a[0:3], a[104:107], v[4:7]\n\t"
    "ds_read_b64_tr_b16 a[20:21], v225 offset:24576\n\t"
    "v_mfma_f32_16x16x32_bf16 v[4:7], a[4:7], a[108:111], v[4:7]\n\t"
    "ds_read_b64_tr_b16 a[22:23], v225 offset:28672\n\t"
    "v_mfma_f32_16x16x32_bf16 v[8:11], a[0:3], a[112:115], v[8:11]\n\t"
    "buffer_load_dwordx4 v231, s[20:23], s50 offen offset:3072 lds\n\t"
    "s_add_u32 s50, s50, 524288\n\t"
    "v_mfma_f32_16x16x32_bf16 v[8:11], a[4:7], a[116:119], v[8:11]\n\t"
    "v_mfma_f32_16x16x32_bf16 v[12:15], a[0:3], a[120:123], v[12:15]\n\t"
    "buffer_load_dwordx4 a[224:227], v232, s[16:19], s51 offen\n\t"
    "v_mfma_f32_16x16x32_bf16 v[12:15], a[4:7], a[124:127], v[12:15]\n\t"
    "s_waitcnt lgkmcnt(4)\n\t"
    "v_mfma_f32_16x16x32_bf16 v[16:19], a[8:11], a[96:99], v[16:19]\n\t"
    "ds_read_b64_tr_b16 a[24:25], v225 offset:16392\n\t"
    "v_mfma_f32_16x16x32_bf16 v[16:19], a[12:15], a[100:103], v[16:19]\n\t"
    "ds_read_b64_tr_b16 a[26:27], v225 offset:20488\n\t"
    "v_mfma_f32_16x16x32_bf16 v[20:23], a[8:11], a[104:107], v[20:23]\n\t"
    "ds_read_b64_tr_b16 a[28:29], v225 offset:24584\n\t"
    "v_mfma_f32_16x16x32_bf16 v[20:23], a[12:15], a[108:111], v[20:23]\n\t"
    "ds_read_b64_tr_b16 a[30:31], v225 offset:28680\n\t"
    "v_mfma_f32_16x16x32_bf16 v[24:27], a[8:11], a[112:115], v[24:27]\n\t"
    "v_mfma_f32_16x16x32_bf16 v[24:27], a[12:15], a[116:119], v[24:27]\n\t"
    "buffer_load_dwordx4 a[228:231], v232, s[16:19], s51 offen offset:1024\n\t"
    "v_mfma_f32_16x16x32_bf16 v[28:31], a[8:11], a[120:123], v[28:31]\n\t"
    "v_mfma_f32_16x16x32_bf16 v[28:31], a[12:15], a[124:127], v[28:31]\n\t"
    "buffer_load_dwordx4 a[232:235], v232, s[16:19], s51 offen offset:2048\n\t"
    "s_waitcnt lgkmcnt(4)\n\t"
    "v_mfma_f32_16x16x32_bf16 v[32:35], a[16:19], a[96:99], v[32:35]\n\t"
    "ds_read_b64_tr_b16 a[32:33], v226 offset:16384\n\t"
    "v_mfma_f32_16x16x32_bf16 v[32:35], a[20:23], a[100:103], v[32:35]\n\t"
    "ds_read_b64_tr_b16 a[34:35], v226 offset:20480\n\t"
    "v_mfma_f32_16x16x32_bf16 v[36:39], a[16:19], a[104:107], v[36:39]\n\t"
    "ds_read_b64_tr_b16 a[36:37], v226 offset:24576\n\t"
    "v_mfma_f32_16x16x32_bf16 v[36:39], a[20:23], a[108:111], v[36:39]\n\t"
    "ds_read_b64_tr_b16 a[38:39], v226 offset:28672\n\t"
    "v_mfma_f32_16x16x32_bf16 v[40:43], a[16:19], a[112:115], v[40:43]\n\t"
    "buffer_load_dwordx4 a[236:239], v232, s[16:19], s51 offen offset:3072\n\t"
    "s_add_u32 s51, s51, 4096\n\t"
    "v_mfma_f32_16x16x32_bf16 v[40:43], a[20:23], a[116:119], v[40:43]\n\t"
    "v_mfma_f32_16x16x32_bf16 v[44:47], a[16:19], a[120:123], v[44:47]\n\t"
    "buffer_load_dwordx4 a[240:243], v232, s[16:19], s51 offen\n\t"
    "v_mfma_f32_16x16x32_bf16 v[44:47], a[20:23], a[124:127], v[44:47]\n\t"
    "s_waitcnt lgkmcnt(4)\n\t"
    "v_mfma_f32_16x16x32_bf16 v[48:51], a[24:27], a[96:99], v[48:51]\n\t"
    "ds_read_b64_tr_b16 a[40:41], v226 offset:16392\n\t"
    "v_mfma_f32_16x16x32_bf16 v[48:51], a[28:31], a[100:103], v[48:51]\n\t"
    "ds_read_b64_tr_b16 a[42:43], v226 offset:20488\n\t"
    "v_mfma_f32_16x16x32_bf16 v[52:55], a[24:27], a[104:107], v[52:55]\n\t"
    "ds_read_b64_tr_b16 a[44:45], v226 offset:24584\n\t"
    "v_mfma_f32_16x16x32_bf16 v[52:55], a[28:31], a[108:111], v[52:55]\n\t"
    "ds_read_b64_tr_b16 a[46:47], v226 offset:28680\n\t"
    "v_mfma_f32_16x16x32_bf16 v[56:59], a[24:27], a[112:115], v[56:59]\n\t"
    "v_mfma_f32_16x16x32_bf16 v[56:59], a[28:31], a[116:119], v[56:59]\n\t"
    "buffer_load_dwordx4 a[244:247], v232, s[16:19], s51 offen offset:1024\n\t"
    "v_mfma_f32_16x16x32_bf16 v[60:63], a[24:27], a[120:123], v[60:63]\n\t"
    "v_mfma_f32_16x16x32_bf16 v[60:63], a[28:31], a[124:127], v[60:63]\n\t"
    "buffer_load_dwordx4 a[248:251], v232, s[16:19], s51 offen offset:2048\n\t"
    "s_waitcnt lgkmcnt(4)\n\t"
    "v_mfma_f32_16x16x32_bf16 v[64:67], a[32:35], a[96:99], v[64:67]\n\t"
    "ds_read_b64_tr_b16 a[48:49], v227 offset:16384\n\t"
    "v_mfma_f32_16x16x32_bf16 v[64:67], a[36:39], a[100:103], v[64:67]\n\t"
    "ds_read_b64_tr_b16 a[50:51], v227 offset:20480\n\t"
    "v_mfma_f32_16x16x32_bf16 v[68:71], a[32:35], a[104:107], v[68:71]\n\t"
    "ds_read_b64_tr_b16 a[52:53], v227 offset:24576\n\t"
    "v_mfma_f32_16x16x32_bf16 v[68:71], a[36:39], a[108:111], v[68:71]\n\t"
    "ds_read_b64_tr_b16 a[54:55], v227 offset:28672\n\t"
    "v_mfma_f32_16x16x32_bf16 v[72:75], a[32:35], a[112:115], v[72:75]\n\t"
    "buffer_load_dwordx4 a[252:255], v232, s[16:19], s51 offen offset:3072\n\t"
    "s_add_u32 s51, s51, 4096\n\t"
    "v_mfma_f32_16x16x32_bf16 v[72:75], a[36:39], a[116:119], v[72:75]\n\t"
    "v_mfma_f32_16x16x32_bf16 v[76:79], a[32:35], a[120:123], v[76:79]\n\t"
    "v_mfma_f32_16x16x32_bf16 v[76:79], a[36:39], a[124:127], v[76:79]\n\t"
    "s_waitcnt lgkmcnt(4)\n\t"
    "v_mfma_f32_16x16x32_bf16 v[80:83], a[40:43], a[96:99], v[80:83]\n\t"
    "ds_read_b64_tr_b16 a[56:57], v227 offset:16392\n\t"
    "v_mfma_f32_16x16x32_bf16 v[80:83], a[44:47], a[100:103], v[80:83]\n\t"
    "ds_read_b64_tr_b16 a[58:59], v227 offset:20488\n\t"
    "v_mfma_f32_16x16x32_bf16 v[84:87], a[40:43], a[104:107], v[84:87]\n\t"
    "ds_read_b64_tr_b16 a[60:61], v227 offset:24584\n\t"
    "v_mfma_f32_16x16x32_bf16 v[84:87], a[44:47], a[108:111], v[84:87]\n\t"
    "ds_read_b64_tr_b16 a[62:63], v227 offset:28680\n\t"
    "v_mfma_f32_16x16x32_bf16 v[88:91], a[40:43], a[112:115], v[88:91]\n\t"
    "v_mfma_f32_16x16x32_bf16 v[88:91], a[44:47], a[116:119], v[88:91]\n\t"
    "v_mfma_f32_16x16x32_bf16 v[92:95], a[40:43], a[120:123], v[92:95]\n\t"
    "v_mfma_f32_16x16x32_bf16 v[92:95], a[44:47], a[124:127], v[92:95]\n\t"
    "s_waitcnt lgkmcnt(0)\n\t"
    "s_waitcnt vmcnt(12)\n\t"
    "s_barrier\n\t"
    "v_mfma_f32_16x16x32_bf16 v[160:163], a[48:51], a[96:99], v[160:163]\n\t"
    "ds_read_b64_tr_b16 a[0:1], v224 offset:32768\n\t"
    "v_mfma_f32_16x16x32_bf16 v[160:163], a[52:55], a[100:103], v[160:163]\n\t"
    "ds_read_b64_tr_b16 a[2:3], v224 offset:36864\n\t"
    "v_mfma_f32_16x16x32_bf16 v[164:167], a[48:51], a[104:107], v[164:167]\n\t"
    "ds_read_b64_tr_b16 a[4:5], v224 offset:40960\n\t"
    "v_mfma_f32_16x16x32_bf16 v[164:167], a[52:55], a[108:111], v[164:167]\n\t"
    "ds_read_b64_tr_b16 a[6:7], v224 offset:45056\n\t"
    "v_mfma_f32_16x16x32_bf16 v[168:171], a[48:51], a[112:115], v[168:171]\n\t"
    "v_mfma_f32_16x16x32_bf16 v[168:171], a[52:55], a[116:119], v[168:171]\n\t"
    "s_mov_b32 m0, s40\n\t"
    "s_nop 0\n\t"
    "buffer_load_dwordx4 v228, s[20:23], s50 offen lds\n\t"
    "v_mfma_f32_16x16x32_bf16 v[172:175], a[48:51], a[120:123], v[172:175]\n\t"
    "v_mfma_f32_16x16x32_bf16 v[172:175], a[52:55], a[124:127], v[172:175]\n\t"
    "s_waitcnt lgkmcnt(4)\n\t"
    "v_mfma_f32_16x16x32_bf16 v[176:179], a[56:59], a[96:99], v[176:179]\n\t"
    "ds_read_b64_tr_b16 a[8:9], v224 offset:32776\n\t"
    "v_mfma_f32_16x16x32_bf16 v[176:179], a[60:63], a[100:103], v[176:179]\n\t"
    "ds_read_b64_tr_b16 a[10:11], v224 offset:36872\n\t"
    "v_mfma_f32_16x16x32_bf16 v[180:183], a[56:59], a[104:107], v[180:183]\n\t"
    "ds_read_b64_tr_b16 a[12:13], v224 offset:40968\n\t"
    "v_mfma_f32_16x16x32_bf16 v[180:183], a[60:63], a[108:111], v[180:183]\n\t"
    "ds_read_b64_tr_b16 a[14:15], v224 offset:45064\n\t"
    "v_mfma_f32_16x16x32_bf16 v[184:187], a[56:59], a[112:115], v[184:187]\n\t"
    "v_mfma_f32_16x16x32_bf16 v[184:187], a[60:63], a[116:119], v[184:187]\n\t"
    "buffer_load_dwordx4 v229, s[20:23], s50 offen offset:1024 lds\n\t"
    "v_mfma_f32_16x16x32_bf16 v[188:191], a[56:59], a[120:123], v[188:191]\n\t"
    "v_mfma_f32_16x16x32_bf16 v[188:191], a[60:63], a[124:127], v[188:191]\n\t"
    "buffer_load_dwordx4 v230, s[20:23], s50 offen offset:2048 lds\n\t"
    "s_waitcnt lgkmcnt(4)\n\t"
    "v_mfma_f32_16x16x32_bf16 v[0:3], a[0:3], a[192:195], v[0:3]\n\t"
    "ds_read_b64_tr_b16 a[16:17], v225 offset:32768\n\t"
    "v_mfma_f32_16x16x32_bf16 v[0:3], a[4:7], a[196:199], v[0:3]\n\t"
    "ds_read_b64_tr_b16 a[18:19], v225 offset:36864\n\t"
    "v_mfma_f32_16x16x32_bf16 v[4:7], a[0:3], a[200:203], v[4:7]\n\t"
    "ds_read_b64_tr_b16 a[20:21], v225 offset:40960\n\t"
    "v_mfma_f32_16x16x32_bf16 v[4:7], a[4:7], a[204:207], v[4:7]\n\t"
    "ds_read_b64_tr_b16 a[22:23], v225 offset:45056\n\t"
    "v_mfma_f32_16x16x32_bf16 v[8:11], a[0:3], a[208:211], v[8:11]\n\t"
    "buffer_load_dwordx4 v231, s[20:23], s50 offen offset:3072 lds\n\t"
    "s_add_u32 s50, s50, 524288\n\t"
    "v_mfma_f32_16x16x32_bf16 v[8:11], a[4:7], a[212:215], v[8:11]\n\t"
    "v_mfma_f32_16x16x32_bf16 v[12:15], a[0:3], a[216:219], v[12:15]\n\t"
    "buffer_load_dwordx4 a[64:67], v232, s[16:19], s51 offen\n\t"
    "v_mfma_f32_16x16x32_bf16 v[12:15], a[4:7], a[220:223], v[12:15]\n\t"
    "s_waitcnt lgkmcnt(4)\n\t"
    "v_mfma_f32_16x16x32_bf16 v[16:19], a[8:11], a[192:195], v[16:19]\n\t"
    "ds_read_b64_tr_b16 a[24:25], v225 offset:32776\n\t"
    "v_mfma_f32_16x16x32_bf16 v[16:19], a[12:15], a[196:199], v[16:19]\n\t"
    "ds_read_b64_tr_b16 a[26:27], v225 offset:36872\n\t"
    "v_mfma_f32_16x16x32_bf16 v[20:23], a[8:11], a[200:203], v[20:23]\n\t"
    "ds_read_b64_tr_b16 a[28:29], v225 offset:40968\n\t"
    "v_mfma_f32_16x16x32_bf16 v[20:23], a[12:15], a[204:207], v[20:23]\n\t"
    "ds_read_b64_tr_b16 a[30:31], v225 offset:45064\n\t"
    "v_mfma_f32_16x16x32_bf16 v[24:27], a[8:11], a[208:211], v[24:27]\n\t"
    "v_mfma_f32_16x16x32_bf16 v[24:27], a[12:15], a[212:215], v[24:27]\n\t"
    "buffer_load_dwordx4 a[68:71], v232, s[16:19], s51 offen offset:1024\n\t"
    "v_mfma_f32_16x16x32_bf16 v[28:31], a[8:11], a[216:219], v[28:31]\n\t"
    "v_mfma_f32_16x16x32_bf16 v[28:31], a[12:15], a[220:223], v[28:31]\n\t"
    "buffer_load_dwordx4 a[72:75], v232, s[16:19], s51 offen offset:2048\n\t"
    "s_waitcnt lgkmcnt(4)\n\t"
    "v_mfma_f32_16x16x32_bf16 v[32:35], a[16:19], a[192:195], v[32:35]\n\t"
    "ds_read_b64_tr_b16 a[32:33], v226 offset:32768\n\t"
    "v_mfma_f32_16x16x32_bf16 v[32:35], a[20:23], a[196:199], v[32:35]\n\t"
    "ds_read_b64_tr_b16 a[34:35], v226 offset:36864\n\t"
    "v_mfma_f32_16x16x32_bf16 v[36:39], a[16:19], a[200:203], v[36:39]\n\t"
    "ds_read_b64_tr_b16 a[36:37], v226 offset:40960\n\t"
    "v_mfma_f32_16x16x32_bf16 v[36:39], a[20:23], a[204:207], v[36:39]\n\t"
    "ds_read_b64_tr_b16 a[38:39], v226 offset:45056\n\t"
    "v_mfma_f32_16x16x32_bf16 v[40:43], a[16:19], a[208:211], v[40:43]\n\t"
    "buffer_load_dwordx4 a[76:79], v232, s[16:19], s51 offen offset:3072\n\t"
    "s_add_u32 s51, s51, 4096\n\t"
    "v_mfma_f32_16x16x32_bf16 v[40:43], a[20:23], a[212:215], v[40:43]\n\t"
    "v_mfma_f32_16x16x32_bf16 v[44:47], a[16:19], a[216:219], v[44:47]\n\t"
    "buffer_load_dwordx4 a[80:83], v232, s[16:19], s51 offen\n\t"
    "v_mfma_f32_16x16x32_bf16 v[44:47], a[20:23], a[220:223], v[44:47]\n\t"
    "s_waitcnt lgkmcnt(4)\n\t"
    "v_mfma_f32_16x16x32_bf16 v[48:51], a[24:27], a[192:195], v[48:51]\n\t"
    "ds_read_b64_tr_b16 a[40:41], v226 offset:32776\n\t"
    "v_mfma_f32_16x16x32_bf16 v[48:51], a[28:31], a[196:199], v[48:51]\n\t"
    "ds_read_b64_tr_b16 a[42:43], v226 offset:36872\n\t"
    "v_mfma_f32_16x16x32_bf16 v[52:55], a[24:27], a[200:203], v[52:55]\n\t"
    "ds_read_b64_tr_b16 a[44:45], v226 offset:40968\n\t"
    "v_mfma_f32_16x16x32_bf16 v[52:55], a[28:31], a[204:207], v[52:55]\n\t"
    "ds_read_b64_tr_b16 a[46:47], v226 offset:45064\n\t"
    "v_mfma_f32_16x16x32_bf16 v[56:59], a[24:27], a[208:211], v[56:59]\n\t"
    "v_mfma_f32_16x16x32_bf16 v[56:59], a[28:31], a[212:215], v[56:59]\n\t"
    "buffer_load_dwordx4 a[84:87], v232, s[16:19], s51 offen offset:1024\n\t"
    "v_mfma_f32_16x16x32_bf16 v[60:63], a[24:27], a[216:219], v[60:63]\n\t"
    "v_mfma_f32_16x16x32_bf16 v[60:63], a[28:31], a[220:223], v[60:63]\n\t"
    "buffer_load_dwordx4 a[88:91], v232, s[16:19], s51 offen offset:2048\n\t"
    "s_waitcnt lgkmcnt(4)\n\t"
    "v_mfma_f32_16x16x32_bf16 v[64:67], a[32:35], a[192:195], v[64:67]\n\t"
    "ds_read_b64_tr_b16 a[48:49], v227 offset:32768\n\t"
    "v_mfma_f32_16x16x32_bf16 v[64:67], a[36:39], a[196:199], v[64:67]\n\t"
    "ds_read_b64_tr_b16 a[50:51], v227 offset:36864\n\t"
    "v_mfma_f32_16x16x32_bf16 v[68:71], a[32:35], a[200:203], v[68:71]\n\t"
    "ds_read_b64_tr_b16 a[52:53], v227 offset:40960\n\t"
    "v_mfma_f32_16x16x32_bf16 v[68:71], a[36:39], a[204:207], v[68:71]\n\t"
    "ds_read_b64_tr_b16 a[54:55], v227 offset:45056\n\t"
    "v_mfma_f32_16x16x32_bf16 v[72:75], a[32:35], a[208:211], v[72:75]\n\t"
    "buffer_load_dwordx4 a[92:95], v232, s[16:19], s51 offen offset:3072\n\t"
    "s_add_u32 s51, s51, 4096\n\t"
    "v_mfma_f32_16x16x32_bf16 v[72:75], a[36:39], a[212:215], v[72:75]\n\t"
    "v_mfma_f32_16x16x32_bf16 v[76:79], a[32:35], a[216:219], v[76:79]\n\t"
    "v_mfma_f32_16x16x32_bf16 v[76:79], a[36:39], a[220:223], v[76:79]\n\t"
    "s_waitcnt lgkmcnt(4)\n\t"
    "v_mfma_f32_16x16x32_bf16 v[80:83], a[40:43], a[192:195], v[80:83]\n\t"
    "ds_read_b64_tr_b16 a[56:57], v227 offset:32776\n\t"
    "v_mfma_f32_16x16x32_bf16 v[80:83], a[44:47], a[196:199], v[80:83]\n\t"
    "ds_read_b64_tr_b16 a[58:59], v227 offset:36872\n\t"
    "v_mfma_f32_16x16x32_bf16 v[84:87], a[40:43], a[200:203], v[84:87]\n\t"
    "ds_read_b64_tr_b16 a[60:61], v227 offset:40968\n\t"
    "v_mfma_f32_16x16x32_bf16 v[84:87], a[44:47], a[204:207], v[84:87]\n\t"
    "ds_read_b64_tr_b16 a[62:63], v227 offset:45064\n\t"
    "v_mfma_f32_16x16x32_bf16 v[88:91], a[40:43], a[208:211], v[88:91]\n\t"
    "v_mfma_f32_16x16x32_bf16 v[88:91], a[44:47], a[212:215], v[88:91]\n\t"
    "v_mfma_f32_16x16x32_bf16 v[92:95], a[40:43], a[216:219], v[92:95]\n\t"
    "v_mfma_f32_16x16x32_bf16 v[92:95], a[44:47], a[220:223], v[92:95]\n\t"
    "s_waitcnt lgkmcnt(0)\n\t"
    "s_waitcnt vmcnt(12)\n\t"
    "s_barrier\n\t"
    "v_mfma_f32_16x16x32_bf16 v[160:163], a[48:51], a[192:195], v[160:163]\n\t"
    "ds_read_b64_tr_b16 a[0:1], v224 offset:49152\n\t"
    "v_mfma_f32_16x16x32_bf16 v[160:163], a[52:55], a[196:199], v[160:163]\n\t"
    "ds_read_b64_tr_b16 a[2:3], v224 offset:53248\n\t"
    "v_mfma_f32_16x16x32_bf16 v[164:167], a[48:51], a[200:203], v[164:167]\n\t"
    "ds_read_b64_tr_b16 a[4:5], v224 offset:57344\n\t"
    "v_mfma_f32_16x16x32_bf16 v[164:167], a[52:55], a[204:207], v[164:167]\n\t"
    "ds_read_b64_tr_b16 a[6:7], v224 offset:61440\n\t"
    "v_mfma_f32_16x16x32_bf16 v[168:171], a[48:51], a[208:211], v[168:171]\n\t"
    "v_mfma_f32_16x16x32_bf16 v[168:171], a[52:55], a[212:215], v[168:171]\n\t"
    "s_mov_b32 m0, s41\n\t"
    "s_nop 0\n\t"
    "buffer_load_dwordx4 v228, s[20:23], s50 offen lds\n\t"
    "v_mfma_f32_16x16x32_bf16 v[172:175], a[48:51], a[216:219], v[172:175]\n\t"
    "v_mfma_f32_16x16x32_bf16 v[172:175], a[52:55], a[220:223], v[172:175]\n\t"
    "s_waitcnt lgkmcnt(4)\n\t"
    "v_mfma_f32_16x16x32_bf16 v[176:179], a[56:59], a[192:195], v[176:179]\n\t"
    "ds_read_b64_tr_b16 a[8:9], v224 offset:49160\n\t"
    "v_mfma_f32_16x16x32_bf16 v[176:179], a[60:63], a[196:199], v[176:179]\n\t"
    "ds_read_b64_tr_b16 a[10:11], v224 offset:53256\n\t"
    "v_mfma_f32_16x16x32_bf16 v[180:183], a[56:59], a[200:203], v[180:183]\n\t"
    "ds_read_b64_tr_b16 a[12:13], v224 offset:57352\n\t"
    "v_mfma_f32_16x16x32_bf16 v[180:183], a[60:63], a[204:207], v[180:183]\n\t"
    "ds_read_b64_tr_b16 a[14:15], v224 offset:61448\n\t"
    "v_mfma_f32_16x16x32_bf16 v[184:187], a[56:59], a[208:211], v[184:187]\n\t"
    "v_mfma_f32_16x16x32_bf16 v[184:187], a[60:63], a[212:215], v[184:187]\n\t"
    "buffer_load_dwordx4 v229, s[20:23], s50 offen offset:1024 lds\n\t"
    "v_mfma_f32_16x16x32_bf16 v[188:191], a[56:59], a[216:219], v[188:191]\n\t"
    "v_mfma_f32_16x16x32_bf16 v[188:191], a[60:63], a[220:223], v[188:191]\n\t"
    "buffer_load_dwordx4 v230, s[20:23], s50 offen offset:2048 lds\n\t"
    "s_waitcnt lgkmcnt(4)\n\t"
    "v_mfma_f32_16x16x32_bf16 v[0:3], a[0:3], a[224:227], v[0:3]\n\t"
    "ds_read_b64_tr_b16 a[16:17], v225 offset:49152\n\t"
    "v_mfma_f32_16x16x32_bf16 v[0:3], a[4:7], a[228:231], v[0:3]\n\t"
    "ds_read_b64_tr_b16 a[18:19], v225 offset:53248\n\t"
    "v_mfma_f32_16x16x32_bf16 v[4:7], a[0:3], a[232:235], v[4:7]\n\t"
    "ds_read_b64_tr_b16 a[20:21], v225 offset:57344\n\t"
    "v_mfma_f32_16x16x32_bf16 v[4:7], a[4:7], a[236:239], v[4:7]\n\t"
    "ds_read_b64_tr_b16 a[22:23], v225 offset:61440\n\t"
    "v_mfma_f32_16x16x32_bf16 v[8:11], a[0:3], a[240:243], v[8:11]\n\t"
    "buffer_load_dwordx4 v231, s[20:23], s50 offen offset:3072 lds\n\t"
    "s_add_u32 s50, s50, 524288\n\t"
    "v_mfma_f32_16x16x32_bf16 v[8:11], a[4:7], a[244:247], v[8:11]\n\t"
    "v_mfma_f32_16x16x32_bf16 v[12:15], a[0:3], a[248:251], v[12:15]\n\t"
    "buffer_load_dwordx4 a[96:99], v232, s[16:19], s51 offen\n\t"
    "v_mfma_f32_16x16x32_bf16 v[12:15], a[4:7], a[252:255], v[12:15]\n\t"
    "s_waitcnt lgkmcnt(4)\n\t"
    "v_mfma_f32_16x16x32_bf16 v[16:19], a[8:11], a[224:227], v[16:19]\n\t"
    "ds_read_b64_tr_b16 a[24:25], v225 offset:49160\n\t"
    "v_mfma_f32_16x16x32_bf16 v[16:19], a[12:15], a[228:231], v[16:19]\n\t"
    "ds_read_b64_tr_b16 a[26:27], v225 offset:53256\n\t"
    "v_mfma_f32_16x16x32_bf16 v[20:23], a[8:11], a[232:235], v[20:23]\n\t"
    "ds_read_b64_tr_b16 a[28:29], v225 offset:57352\n\t"
    "v_mfma_f32_16x16x32_bf16 v[20:23], a[12:15], a[236:239], v[20:23]\n\t"
    "ds_read_b64_tr_b16 a[30:31], v225 offset:61448\n\t"
    "v_mfma_f32_16x16x32_bf16 v[24:27], a[8:11], a[240:243], v[24:27]\n\t"
    "v_mfma_f32_16x16x32_bf16 v[24:27], a[12:15], a[244:247], v[24:27]\n\t"
    "buffer_load_dwordx4 a[100:103], v232, s[16:19], s51 offen offset:1024\n\t"
    "v_mfma_f32_16x16x32_bf16 v[28:31], a[8:11], a[248:251], v[28:31]\n\t"
    "v_mfma_f32_16x16x32_bf16 v[28:31], a[12:15], a[252:255], v[28:31]\n\t"
    "buffer_load_dwordx4 a[104:107], v232, s[16:19], s51 offen offset:2048\n\t"
    "s_waitcnt lgkmcnt(4)\n\t"
    "v_mfma_f32_16x16x32_bf16 v[32:35], a[16:19], a[224:227], v[32:35]\n\t"
    "ds_read_b64_tr_b16 a[32:33], v226 offset:49152\n\t"
    "v_mfma_f32_16x16x32_bf16 v[32:35], a[20:23], a[228:231], v[32:35]\n\t"
    "ds_read_b64_tr_b16 a[34:35], v226 offset:53248\n\t"
    "v_mfma_f32_16x16x32_bf16 v[36:39], a[16:19], a[232:235], v[36:39]\n\t"
    "ds_read_b64_tr_b16 a[36:37], v226 offset:57344\n\t"
    "v_mfma_f32_16x16x32_bf16 v[36:39], a[20:23], a[236:239], v[36:39]\n\t"
    "ds_read_b64_tr_b16 a[38:39], v226 offset:61440\n\t"
    "v_mfma_f32_16x16x32_bf16 v[40:43], a[16:19], a[240:243], v[40:43]\n\t"
    "buffer_load_dwordx4 a[108:111], v232, s[16:19], s51 offen offset:3072\n\t"
    "s_add_u32 s51, s51, 4096\n\t"
    "v_mfma_f32_16x16x32_bf16 v[40:43], a[20:23], a[244:247], v[40:43]\n\t"
    "v_mfma_f32_16x16x32_bf16 v[44:47], a[16:19], a[248:251], v[44:47]\n\t"
    "buffer_load_dwordx4 a[112:115], v232, s[16:19], s51 offen\n\t"
    "v_mfma_f32_16x16x32_bf16 v[44:47], a[20:23], a[252:255], v[44:47]\n\t"
    "s_waitcnt lgkmcnt(4)\n\t"
    "v_mfma_f32_16x16x32_bf16 v[48:51], a[24:27], a[224:227], v[48:51]\n\t"
    "ds_read_b64_tr_b16 a[40:41], v226 offset:49160\n\t"
    "v_mfma_f32_16x16x32_bf16 v[48:51], a[28:31], a[228:231], v[48:51]\n\t"
    "ds_read_b64_tr_b16 a[42:43], v226 offset:53256\n\t"
    "v_mfma_f32_16x16x32_bf16 v[52:55], a[24:27], a[232:235], v[52:55]\n\t"
    "ds_read_b64_tr_b16 a[44:45], v226 offset:57352\n\t"
    "v_mfma_f32_16x16x32_bf16 v[52:55], a[28:31], a[236:239], v[52:55]\n\t"
    "ds_read_b64_tr_b16 a[46:47], v226 offset:61448\n\t"
    "v_mfma_f32_16x16x32_bf16 v[56:59], a[24:27], a[240:243], v[56:59]\n\t"
    "v_mfma_f32_16x16x32_bf16 v[56:59], a[28:31], a[244:247], v[56:59]\n\t"
    "buffer_load_dwordx4 a[116:119], v232, s[16:19], s51 offen offset:1024\n\t"
    "v_mfma_f32_16x16x32_bf16 v[60:63], a[24:27], a[248:251], v[60:63]\n\t"
    "v_mfma_f32_16x16x32_bf16 v[60:63], a[28:31], a[252:255], v[60:63]\n\t"
    "buffer_load_dwordx4 a[120:123], v232, s[16:19], s51 offen offset:2048\n\t"
    "s_waitcnt lgkmcnt(4)\n\t"
    "v_mfma_f32_16x16x32_bf16 v[64:67], a[32:35], a[224:227], v[64:67]\n\t"
    "ds_read_b64_tr_b16 a[48:49], v227 offset:49152\n\t"
    "v_mfma_f32_16x16x32_bf16 v[64:67], a[36:39], a[228:231], v[64:67]\n\t"
    "ds_read_b64_tr_b16 a[50:51], v227 offset:53248\n\t"
    "v_mfma_f32_16x16x32_bf16 v[68:71], a[32:35], a[232:235], v[68:71]\n\t"
    "ds_read_b64_tr_b16 a[52:53], v227 offset:57344\n\t"
    "v_mfma_f32_16x16x32_bf16 v[68:71], a[36:39], a[236:239], v[68:71]\n\t"
    "ds_read_b64_tr_b16 a[54:55], v227 offset:61440\n\t"
    "v_mfma_f32_16x16x32_bf16 v[72:75], a[32:35], a[240:243], v[72:75]\n\t"
    "buffer_load_dwordx4 a[124:127], v232, s[16:19], s51 offen offset:3072\n\t"
    "s_add_u32 s51, s51, 4096\n\t"
    "v_mfma_f32_16x16x32_bf16 v[72:75], a[36:39], a[244:247], v[72:75]\n\t"
    "v_mfma_f32_16x16x32_bf16 v[76:79], a[32:35], a[248:251], v[76:79]\n\t"
    "v_mfma_f32_16x16x32_bf16 v[76:79], a[36:39], a[252:255], v[76:79]\n\t"
    "s_waitcnt lgkmcnt(4)\n\t"
    "v_mfma_f32_16x16x32_bf16 v[80:83], a[40:43], a[224:227], v[80:83]\n\t"
    "ds_read_b64_tr_b16 a[56:57], v227 offset:49160\n\t"
    "v_mfma_f32_16x16x32_bf16 v[80:83], a[44:47], a[228:231], v[80:83]\n\t"
    "ds_read_b64_tr_b16 a[58:59], v227 offset:53256\n\t"
    "v_mfma_f32_16x16x32_bf16 v[84:87], a[40:43], a[232:235], v[84:87]\n\t"
    "ds_read_b64_tr_b16 a[60:61], v227 offset:57352\n\t"
    "v_mfma_f32_16x16x32_bf16 v[84:87], a[44:47], a[236:239], v[84:87]\n\t"
    "ds_read_b64_tr_b16 a[62:63], v227 offset:61448\n\t"
    "v_mfma_f32_16x16x32_bf16 v[88:91], a[40:43], a[240:243], v[88:91]\n\t"
    "v_mfma_f32_16x16x32_bf16 v[88:91], a[44:47], a[244:247], v[88:91]\n\t"
    "v_mfma_f32_16x16x32_bf16 v[92:95], a[40:43], a[248:251], v[92:95]\n\t"
    "v_mfma_f32_16x16x32_bf16 v[92:95], a[44:47], a[252:255], v[92:95]\n\t"
    "s_waitcnt lgkmcnt(0)\n\t"
    "s_waitcnt vmcnt(12)\n\t"
    "s_barrier\n\t"
    "v_mfma_f32_16x16x32_bf16 v[160:163], a[48:51], a[224:227], v[160:163]\n\t"
    "ds_read_b64_tr_b16 a[0:1], v224 offset:0\n\t"
    "v_mfma_f32_16x16x32_bf16 v[160:163], a[52:55], a[228:231], v[160:163]\n\t"
    "ds_read_b64_tr_b16 a[2:3], v224 offset:4096\n\t"
    "v_mfma_f32_16x16x32_bf16 v[164:167], a[48:51], a[232:235], v[164:167]\n\t"
    "ds_read_b64_tr_b16 a[4:5], v224 offset:8192\n\t"
    "v_mfma_f32_16x16x32_bf16 v[164:167], a[52:55], a[236:239], v[164:167]\n\t"
    "ds_read_b64_tr_b16 a[6:7], v224 offset:12288\n\t"
    "v_mfma_f32_16x16x32_bf16 v[168:171], a[48:51], a[240:243], v[168:171]\n\t"
    "v_mfma_f32_16x16x32_bf16 v[168:171], a[52:55], a[244:247], v[168:171]\n\t"
    "s_mov_b32 m0, s42\n\t"
    "s_nop 0\n\t"
    "buffer_load_dwordx4 v228, s[20:23], s50 offen lds\n\t"
    "v_mfma_f32_16x16x32_bf16 v[172:175], a[48:51], a[248:251], v[172:175]\n\t"
    "v_mfma_f32_16x16x32_bf16 v[172:175], a[52:55], a[252:255], v[172:175]\n\t"
    "s_waitcnt lgkmcnt(4)\n\t"
    "v_mfma_f32_16x16x32_bf16 v[176:179], a[56:59], a[224:227], v[176:179]\n\t"
    "ds_read_b64_tr_b16 a[8:9], v224 offset:8\n\t"
    "v_mfma_f32_16x16x32_bf16 v[176:179], a[60:63], a[228:231], v[176:179]\n\t"
    "ds_read_b64_tr_b16 a[10:11], v224 offset:4104\n\t"
    "v_mfma_f32_16x16x32_bf16 v[180:183], a[56:59], a[232:235], v[180:183]\n\t"
    "ds_read_b64_tr_b16 a[12:13], v224 offset:8200\n\t"
    "v_mfma_f32_16x16x32_bf16 v[180:183], a[60:63], a[236:239], v[180:183]\n\t"
    "ds_read_b64_tr_b16 a[14:15], v224 offset:12296\n\t"
    "v_mfma_f32_16x16x32_bf16 v[184:187], a[56:59], a[240:243], v[184:187]\n\t"
    "v_mfma_f32_16x16x32_bf16 v[184:187], a[60:63], a[244:247], v[184:187]\n\t"
    "buffer_load_dwordx4 v229, s[20:23], s50 offen offset:1024 lds\n\t"
    "v_mfma_f32_16x16x32_bf16 v[188:191], a[56:59], a[248:251], v[188:191]\n\t"
    "v_mfma_f32_16x16x32_bf16 v[188:191], a[60:63], a[252:255], v[188:191]\n\t"
    "buffer_load_dwordx4 v230, s[20:23], s50 offen offset:2048 lds\n\t"
    "s_waitcnt lgkmcnt(4)\n\t"
    "v_mfma_f32_16x16x32_bf16 v[0:3], a[0:3], a[64:67], v[0:3]\n\t"
    "ds_read_b64_tr_b16 a[16:17], v225 offset:0\n\t"
    "v_mfma_f32_16x16x32_bf16 v[0:3], a[4:7], a[68:71], v[0:3]\n\t"
    "ds_read_b64_tr_b16 a[18:19], v225 offset:4096\n\t"
    "v_mfma_f32_16x16x32_bf16 v[4:7], a[0:3], a[72:75], v[4:7]\n\t"
    "ds_read_b64_tr_b16 a[20:21], v225 offset:8192\n\t"
    "v_mfma_f32_16x16x32_bf16 v[4:7], a[4:7], a[76:79], v[4:7]\n\t"
    "ds_read_b64_tr_b16 a[22:23], v225 offset:12288\n\t"
    "v_mfma_f32_16x16x32_bf16 v[8:11], a[0:3], a[80:83], v[8:11]\n\t"
    "buffer_load_dwordx4 v231, s[20:23], s50 offen offset:3072 lds\n\t"
    "s_add_u32 s50, s50, 524288\n\t"
    "v_mfma_f32_16x16x32_bf16 v[8:11], a[4:7], a[84:87], v[8:11]\n\t"
    "v_mfma_f32_16x16x32_bf16 v[12:15], a[0:3], a[88:91], v[12:15]\n\t"
    "buffer_load_dwordx4 a[192:195], v232, s[16:19], s51 offen\n\t"
    "v_mfma_f32_16x16x32_bf16 v[12:15], a[4:7], a[92:95], v[12:15]\n\t"
    "s_waitcnt lgkmcnt(4)\n\t"
    "v_mfma_f32_16x16x32_bf16 v[16:19], a[8:11], a[64:67], v[16:19]\n\t"
    "ds_read_b64_tr_b16 a[24:25], v225 offset:8\n\t"
    "v_mfma_f32_16x16x32_bf16 v[16:19], a[12:15], a[68:71], v[16:19]\n\t"
    "ds_read_b64_tr_b16 a[26:27], v225 offset:4104\n\t"
    "v_mfma_f32_16x16x32_bf16 v[20:23], a[8:11], a[72:75], v[20:23]\n\t"
    "ds_read_b64_tr_b16 a[28:29], v225 offset:8200\n\t"
    "v_mfma_f32_16x16x32_bf16 v[20:23], a[12:15], a[76:79], v[20:23]\n\t"
    "ds_read_b64_tr_b16 a[30:31], v225 offset:12296\n\t"
    "v_mfma_f32_16x16x32_bf16 v[24:27], a[8:11], a[80:83], v[24:27]\n\t"
    "v_mfma_f32_16x16x32_bf16 v[24:27], a[12:15], a[84:87], v[24:27]\n\t"
    "buffer_load_dwordx4 a[196:199], v232, s[16:19], s51 offen offset:1024\n\t"
    "v_mfma_f32_16x16x32_bf16 v[28:31], a[8:11], a[88:91], v[28:31]\n\t"
    "v_mfma_f32_16x16x32_bf16 v[28:31], a[12:15], a[92:95], v[28:31]\n\t"
    "buffer_load_dwordx4 a[200:203], v232, s[16:19], s51 offen offset:2048\n\t"
    "s_waitcnt lgkmcnt(4)\n\t"
    "v_mfma_f32_16x16x32_bf16 v[32:35], a[16:19], a[64:67], v[32:35]\n\t"
    "ds_read_b64_tr_b16 a[32:33], v226 offset:0\n\t"
    "v_mfma_f32_16x16x32_bf16 v[32:35], a[20:23], a[68:71], v[32:35]\n\t"
    "ds_read_b64_tr_b16 a[34:35], v226 offset:4096\n\t"
    "v_mfma_f32_16x16x32_bf16 v[36:39], a[16:19], a[72:75], v[36:39]\n\t"
    "ds_read_b64_tr_b16 a[36:37], v226 offset:8192\n\t"
    "v_mfma_f32_16x16x32_bf16 v[36:39], a[20:23], a[76:79], v[36:39]\n\t"
    "ds_read_b64_tr_b16 a[38:39], v226 offset:12288\n\t"
    "v_mfma_f32_16x16x32_bf16 v[40:43], a[16:19], a[80:83], v[40:43]\n\t"
    "buffer_load_dwordx4 a[204:207], v232, s[16:19], s51 offen offset:3072\n\t"
    "s_add_u32 s51, s51, 4096\n\t"
    "v_mfma_f32_16x16x32_bf16 v[40:43], a[20:23], a[84:87], v[40:43]\n\t"
    "v_mfma_f32_16x16x32_bf16 v[44:47], a[16:19], a[88:91], v[44:47]\n\t"
    "buffer_load_dwordx4 a[208:211], v232, s[16:19], s51 offen\n\t"
    "v_mfma_f32_16x16x32_bf16 v[44:47], a[20:23], a[92:95], v[44:47]\n\t"
    "s_waitcnt lgkmcnt(4)\n\t"
    "v_mfma_f32_16x16x32_bf16 v[48:51], a[24:27], a[64:67], v[48:51]\n\t"
    "ds_read_b64_tr_b16 a[40:41], v226 offset:8\n\t"
    "v_mfma_f32_16x16x32_bf16 v[48:51], a[28:31], a[68:71], v[48:51]\n\t"
    "ds_read_b64_tr_b16 a[42:43], v226 offset:4104\n\t"
    "v_mfma_f32_16x16x32_bf16 v[52:55], a[24:27], a[72:75], v[52:55]\n\t"
    "ds_read_b64_tr_b16 a[44:45], v226 offset:8200\n\t"
    "v_mfma_f32_16x16x32_bf16 v[52:55], a[28:31], a[76:79], v[52:55]\n\t"
    "ds_read_b64_tr_b16 a[46:47], v226 offset:12296\n\t"
    "v_mfma_f32_16x16x32_bf16 v[56:59], a[24:27], a[80:83], v[56:59]\n\t"
    "v_mfma_f32_16x16x32_bf16 v[56:59], a[28:31], a[84:87], v[56:59]\n\t"
    "buffer_load_dwordx4 a[212:215], v232, s[16:19], s51 offen offset:1024\n\t"
    "v_mfma_f32_16x16x32_bf16 v[60:63], a[24:27], a[88:91], v[60:63]\n\t"
    "v_mfma_f32_16x16x32_bf16 v[60:63], a[28:31], a[92:95], v[60:63]\n\t"
    "buffer_load_dwordx4 a[216:219], v232, s[16:19], s51 offen offset:2048\n\t"
    "s_waitcnt lgkmcnt(4)\n\t"
    "v_mfma_f32_16x16x32_bf16 v[64:67], a[32:35], a[64:67], v[64:67]\n\t"
    "ds_read_b64_tr_b16 a[48:49], v227 offset:0\n\t"
    "v_mfma_f32_16x16x32_bf16 v[64:67], a[36:39], a[68:71], v[64:67]\n\t"
    "ds_read_b64_tr_b16 a[50:51], v227 offset:4096\n\t"
    "v_mfma_f32_16x16x32_bf16 v[68:71], a[32:35], a[72:75], v[68:71]\n\t"
    "ds_read_b64_tr_b16 a[52:53], v227 offset:8192\n\t"
    "v_mfma_f32_16x16x32_bf16 v[68:71], a[36:39], a[76:79], v[68:71]\n\t"
    "ds_read_b64_tr_b16 a[54:55], v227 offset:12288\n\t"
    "v_mfma_f32_16x16x32_bf16 v[72:75], a[32:35], a[80:83], v[72:75]\n\t"
    "buffer_load_dwordx4 a[220:223], v232, s[16:19], s51 offen offset:3072\n\t"
    "s_add_u32 s51, s51, 4096\n\t"
    "v_mfma_f32_16x16x32_bf16 v[72:75], a[36:39], a[84:87], v[72:75]\n\t"
    "v_mfma_f32_16x16x32_bf16 v[76:79], a[32:35], a[88:91], v[76:79]\n\t"
    "v_mfma_f32_16x16x32_bf16 v[76:79], a[36:39], a[92:95], v[76:79]\n\t"
    "s_waitcnt lgkmcnt(4)\n\t"
    "v_mfma_f32_16x16x32_bf16 v[80:83], a[40:43], a[64:67], v[80:83]\n\t"
    "ds_read_b64_tr_b16 a[56:57], v227 offset:8\n\t"
    "v_mfma_f32_16x16x32_bf16 v[80:83], a[44:47], a[68:71], v[80:83]\n\t"
    "ds_read_b64_tr_b16 a[58:59], v227 offset:4104\n\t"
    "v_mfma_f32_16x16x32_bf16 v[84:87], a[40:43], a[72:75], v[84:87]\n\t"
    "ds_read_b64_tr_b16 a[60:61], v227 offset:8200\n\t"
    "v_mfma_f32_16x16x32_bf16 v[84:87], a[44:47], a[76:79], v[84:87]\n\t"
    "ds_read_b64_tr_b16 a[62:63], v227 offset:12296\n\t"
    "v_mfma_f32_16x16x32_bf16 v[88:91], a[40:43], a[80:83], v[88:91]\n\t"
    "v_mfma_f32_16x16x32_bf16 v[88:91], a[44:47], a[84:87], v[88:91]\n\t"
    "v_mfma_f32_16x16x32_bf16 v[92:95], a[40:43], a[88:91], v[92:95]\n\t"
    "v_mfma_f32_16x16x32_bf16 v[92:95], a[44:47], a[92:95], v[92:95]\n\t"
    "s_waitcnt lgkmcnt(0)\n\t"
    "s_waitcnt vmcnt(12)\n\t"
    "s_barrier\n\t"
    "v_mfma_f32_16x16x32_bf16 v[160:163], a[48:51], a[64:67], v[160:163]\n\t"
    "ds_read_b64_tr_b16 a[0:1], v224 offset:16384\n\t"
    "v_mfma_f32_16x16x32_bf16 v[160:163], a[52:55], a[68:71], v[160:163]\n\t"
    "ds_read_b64_tr_b16 a[2:3], v224 offset:20480\n\t"
    "v_mfma_f32_16x16x32_bf16 v[164:167], a[48:51], a[72:75], v[164:167]\n\t"
    "ds_read_b64_tr_b16 a[4:5], v224 offset:24576\n\t"
    "v_mfma_f32_16x16x32_bf16 v[164:167], a[52:55], a[76:79], v[164:167]\n\t"
    "ds_read_b64_tr_b16 a[6:7], v224 offset:28672\n\t"
    "v_mfma_f32_16x16x32_bf16 v[168:171], a[48:51], a[80:83], v[168:171]\n\t"
    "v_mfma_f32_16x16x32_bf16 v[168:171], a[52:55], a[84:87], v[168:171]\n\t"
    "s_mov_b32 m0, s43\n\t"
    "s_nop 0\n\t"
    "buffer_load_dwordx4 v228, s[20:23], s50 offen lds\n\t"
    "v_mfma_f32_16x16x32_bf16 v[172:175], a[48:51], a[88:91], v[172:175]\n\t"
    "v_mfma_f32_16x16x32_bf16 v[172:175], a[52:55], a[92:95], v[172:175]\n\t"
    "s_waitcnt lgkmcnt(4)\n\t"
    "v_mfma_f32_16x16x32_bf16 v[176:179], a[56:59], a[64:67], v[176:179]\n\t"
    "ds_read_b64_tr_b16 a[8:9], v224 offset:16392\n\t"
    "v_mfma_f32_16x16x32_bf16 v[176:179], a[60:63], a[68:71], v[176:179]\n\t"
    "ds_read_b64_tr_b16 a[10:11], v224 offset:20488\n\t"
    "v_mfma_f32_16x16x32_bf16 v[180:183], a[56:59], a[72:75], v[180:183]\n\t"
    "ds_read_b64_tr_b16 a[12:13], v224 offset:24584\n\t"
    "v_mfma_f32_16x16x32_bf16 v[180:183], a[60:63], a[76:79], v[180:183]\n\t"
    "ds_read_b64_tr_b16 a[14:15], v224 offset:28680\n\t"
    "v_mfma_f32_16x16x32_bf16 v[184:187], a[56:59], a[80:83], v[184:187]\n\t"
    "v_mfma_f32_16x16x32_bf16 v[184:187], a[60:63], a[84:87], v[184:187]\n\t"
    "buffer_load_dwordx4 v229, s[20:23], s50 offen offset:1024 lds\n\t"
    "v_mfma_f32_16x16x32_bf16 v[188:191], a[56:59], a[88:91], v[188:191]\n\t"
    "v_mfma_f32_16x16x32_bf16 v[188:191], a[60:63], a[92:95], v[188:191]\n\t"
    "buffer_load_dwordx4 v230, s[20:23], s50 offen offset:2048 lds\n\t"
    "s_waitcnt lgkmcnt(4)\n\t"
    "v_mfma_f32_16x16x32_bf16 v[0:3], a[0:3], a[96:99], v[0:3]\n\t"
    "ds_read_b64_tr_b16 a[16:17], v225 offset:16384\n\t"
    "v_mfma_f32_16x16x32_bf16 v[0:3], a[4:7], a[100:103], v[0:3]\n\t"
    "ds_read_b64_tr_b16 a[18:19], v225 offset:20480\n\t"
    "v_mfma_f32_16x16x32_bf16 v[4:7], a[0:3], a[104:107], v[4:7]\n\t"
    "ds_read_b64_tr_b16 a[20:21], v225 offset:24576\n\t"
    "v_mfma_f32_16x16x32_bf16 v[4:7], a[4:7], a[108:111], v[4:7]\n\t"
    "ds_read_b64_tr_b16 a[22:23], v225 offset:28672\n\t"
    "v_mfma_f32_16x16x32_bf16 v[8:11], a[0:3], a[112:115], v[8:11]\n\t"
    "buffer_load_dwordx4 v231, s[20:23], s50 offen offset:3072 lds\n\t"
    "s_add_u32 s50, s50, 524288\n\t"
    "v_mfma_f32_16x16x32_bf16 v[8:11], a[4:7], a[116:119], v[8:11]\n\t"
    "v_mfma_f32_16x16x32_bf16 v[12:15], a[0:3], a[120:123], v[12:15]\n\t"
    "buffer_load_dwordx4 a[224:227], v232, s[16:19], s51 offen\n\t"
    "v_mfma_f32_16x16x32_bf16 v[12:15], a[4:7], a[124:127], v[12:15]\n\t"
    "s_waitcnt lgkmcnt(4)\n\t"
    "v_mfma_f32_16x16x32_bf16 v[16:19], a[8:11], a[96:99], v[16:19]\n\t"
    "ds_read_b64_tr_b16 a[24:25], v225 offset:16392\n\t"
    "v_mfma_f32_16x16x32_bf16 v[16:19], a[12:15], a[100:103], v[16:19]\n\t"
    "ds_read_b64_tr_b16 a[26:27], v225 offset:20488\n\t"
    "v_mfma_f32_16x16x32_bf16 v[20:23], a[8:11], a[104:107], v[20:23]\n\t"
    "ds_read_b64_tr_b16 a[28:29], v225 offset:24584\n\t"
    "v_mfma_f32_16x16x32_bf16 v[20:23], a[12:15], a[108:111], v[20:23]\n\t"
    "ds_read_b64_tr_b16 a[30:31], v225 offset:28680\n\t"
    "v_mfma_f32_16x16x32_bf16 v[24:27], a[8:11], a[112:115], v[24:27]\n\t"
    "v_mfma_f32_16x16x32_bf16 v[24:27], a[12:15], a[116:119], v[24:27]\n\t"
    "buffer_load_dwordx4 a[228:231], v232, s[16:19], s51 offen offset:1024\n\t"
    "v_mfma_f32_16x16x32_bf16 v[28:31], a[8:11], a[120:123], v[28:31]\n\t"
    "v_mfma_f32_16x16x32_bf16 v[28:31], a[12:15], a[124:127], v[28:31]\n\t"
    "buffer_load_dwordx4 a[232:235], v232, s[16:19], s51 offen offset:2048\n\t"
    "s_waitcnt lgkmcnt(4)\n\t"
    "v_mfma_f32_16x16x32_bf16 v[32:35], a[16:19], a[96:99], v[32:35]\n\t"
    "ds_read_b64_tr_b16 a[32:33], v226 offset:16384\n\t"
    "v_mfma_f32_16x16x32_bf16 v[32:35], a[20:23], a[100:103], v[32:35]\n\t"
    "ds_read_b64_tr_b16 a[34:35], v226 offset:20480\n\t"
    "v_mfma_f32_16x16x32_bf16 v[36:39], a[16:19], a[104:107], v[36:39]\n\t"
    "ds_read_b64_tr_b16 a[36:37], v226 offset:24576\n\t"
    "v_mfma_f32_16x16x32_bf16 v[36:39], a[20:23], a[108:111], v[36:39]\n\t"
    "ds_read_b64_tr_b16 a[38:39], v226 offset:28672\n\t"
    "v_mfma_f32_16x16x32_bf16 v[40:43], a[16:19], a[112:115], v[40:43]\n\t"
    "buffer_load_dwordx4 a[236:239], v232, s[16:19], s51 offen offset:3072\n\t"
    "s_add_u32 s51, s51, 4096\n\t"
    "v_mfma_f32_16x16x32_bf16 v[40:43], a[20:23], a[116:119], v[40:43]\n\t"
    "v_mfma_f32_16x16x32_bf16 v[44:47], a[16:19], a[120:123], v[44:47]\n\t"
    "buffer_load_dwordx4 a[240:243], v232, s[16:19], s51 offen\n\t"
    "v_mfma_f32_16x16x32_bf16 v[44:47], a[20:23], a[124:127], v[44:47]\n\t"
    "s_waitcnt lgkmcnt(4)\n\t"
    "v_mfma_f32_16x16x32_bf16 v[48:51], a[24:27], a[96:99], v[48:51]\n\t"
    "ds_read_b64_tr_b16 a[40:41], v226 offset:16392\n\t"
    "v_mfma_f32_16x16x32_bf16 v[48:51], a[28:31], a[100:103], v[48:51]\n\t"
    "ds_read_b64_tr_b16 a[42:43], v226 offset:20488\n\t"
    "v_mfma_f32_16x16x32_bf16 v[52:55], a[24:27], a[104:107], v[52:55]\n\t"
    "ds_read_b64_tr_b16 a[44:45], v226 offset:24584\n\t"
    "v_mfma_f32_16x16x32_bf16 v[52:55], a[28:31], a[108:111], v[52:55]\n\t"
    "ds_read_b64_tr_b16 a[46:47], v226 offset:28680\n\t"
    "v_mfma_f32_16x16x32_bf16 v[56:59], a[24:27], a[112:115], v[56:59]\n\t"
    "v_mfma_f32_16x16x32_bf16 v[56:59], a[28:31], a[116:119], v[56:59]\n\t"
    "buffer_load_dwordx4 a[244:247], v232, s[16:19], s51 offen offset:1024\n\t"
    "v_mfma_f32_16x16x32_bf16 v[60:63], a[24:27], a[120:123], v[60:63]\n\t"
    "v_mfma_f32_16x16x32_bf16 v[60:63], a[28:31], a[124:127], v[60:63]\n\t"
    "buffer_load_dwordx4 a[248:251], v232, s[16:19], s51 offen offset:2048\n\t"
    "s_waitcnt lgkmcnt(4)\n\t"
    "v_mfma_f32_16x16x32_bf16 v[64:67], a[32:35], a[96:99], v[64:67]\n\t"
    "ds_read_b64_tr_b16 a[48:49], v227 offset:16384\n\t"
    "v_mfma_f32_16x16x32_bf16 v[64:67], a[36:39], a[100:103], v[64:67]\n\t"
    "ds_read_b64_tr_b16 a[50:51], v227 offset:20480\n\t"
    "v_mfma_f32_16x16x32_bf16 v[68:71], a[32:35], a[104:107], v[68:71]\n\t"
    "ds_read_b64_tr_b16 a[52:53], v227 offset:24576\n\t"
    "v_mfma_f32_16x16x32_bf16 v[68:71], a[36:39], a[108:111], v[68:71]\n\t"
    "ds_read_b64_tr_b16 a[54:55], v227 offset:28672\n\t"
    "v_mfma_f32_16x16x32_bf16 v[72:75], a[32:35], a[112:115], v[72:75]\n\t"
    "buffer_load_dwordx4 a[252:255], v232, s[16:19], s51 offen offset:3072\n\t"
    "s_add_u32 s51, s51, 4096\n\t"
    "v_mfma_f32_16x16x32_bf16 v[72:75], a[36:39], a[116:119], v[72:75]\n\t"
    "v_mfma_f32_16x16x32_bf16 v[76:79], a[32:35], a[120:123], v[76:79]\n\t"
    "v_mfma_f32_16x16x32_bf16 v[76:79], a[36:39], a[124:127], v[76:79]\n\t"
    "s_waitcnt lgkmcnt(4)\n\t"
    "v_mfma_f32_16x16x32_bf16 v[80:83], a[40:43], a[96:99], v[80:83]\n\t"
    "ds_read_b64_tr_b16 a[56:57], v227 offset:16392\n\t"
    "v_mfma_f32_16x16x32_bf16 v[80:83], a[44:47], a[100:103], v[80:83]\n\t"
    "ds_read_b64_tr_b16 a[58:59], v227 offset:20488\n\t"
    "v_mfma_f32_16x16x32_bf16 v[84:87], a[40:43], a[104:107], v[84:87]\n\t"
    "ds_read_b64_tr_b16 a[60:61], v227 offset:24584\n\t"
    "v_mfma_f32_16x16x32_bf16 v[84:87], a[44:47], a[108:111], v[84:87]\n\t"
    "ds_read_b64_tr_b16 a[62:63], v227 offset:28680\n\t"
    "v_mfma_f32_16x16x32_bf16 v[88:91], a[40:43], a[112:115], v[88:91]\n\t"
    "v_mfma_f32_16x16x32_bf16 v[88:91], a[44:47], a[116:119], v[88:91]\n\t"
    "v_mfma_f32_16x16x32_bf16 v[92:95], a[40:43], a[120:123], v[92:95]\n\t"
    "v_mfma_f32_16x16x32_bf16 v[92:95], a[44:47], a[124:127], v[92:95]\n\t"
    "s_waitcnt lgkmcnt(0)\n\t"
    "s_waitcnt vmcnt(12)\n\t"
    "s_barrier\n\t"
    "v_mfma_f32_16x16x32_bf16 v[160:163], a[48:51], a[96:99], v[160:163]\n\t"
    "ds_read_b64_tr_b16 a[0:1], v224 offset:32768\n\t"
    "v_mfma_f32_16x16x32_bf16 v[160:163], a[52:55], a[100:103], v[160:163]\n\t"
    "ds_read_b64_tr_b16 a[2:3], v224 offset:36864\n\t"
    "v_mfma_f32_16x16x32_bf16 v[164:167], a[48:51], a[104:107], v[164:167]\n\t"
    "ds_read_b64_tr_b16 a[4:5], v224 offset:40960\n\t"
    "v_mfma_f32_16x16x32_bf16 v[164:167], a[52:55], a[108:111], v[164:167]\n\t"
    "ds_read_b64_tr_b16 a[6:7], v224 offset:45056\n\t"
    "v_mfma_f32_16x16x32_bf16 v[168:171], a[48:51], a[112:115], v[168:171]\n\t"
    "v_mfma_f32_16x16x32_bf16 v[168:171], a[52:55], a[116:119], v[168:171]\n\t"
    "s_mov_b32 m0, s32\n\t"
    "s_add_u32 s28, s29, 0\n\t"
    "buffer_load_dwordx4 v208, s[8:11], s28 offen lds\n\t"
    "v_mfma_f32_16x16x32_bf16 v[172:175], a[48:51], a[120:123], v[172:175]\n\t"
    "v_mfma_f32_16x16x32_bf16 v[172:175], a[52:55], a[124:127], v[172:175]\n\t"
    "s_waitcnt lgkmcnt(4)\n\t"
    "v_mfma_f32_16x16x32_bf16 v[176:179], a[56:59], a[96:99], v[176:179]\n\t"
    "ds_read_b64_tr_b16 a[8:9], v224 offset:32776\n\t"
    "v_mfma_f32_16x16x32_bf16 v[176:179], a[60:63], a[100:103], v[176:179]\n\t"
    "ds_read_b64_tr_b16 a[10:11], v224 offset:36872\n\t"
    "v_mfma_f32_16x16x32_bf16 v[180:183], a[56:59], a[104:107], v[180:183]\n\t"
    "ds_read_b64_tr_b16 a[12:13], v224 offset:40968\n\t"
    "v_mfma_f32_16x16x32_bf16 v[180:183], a[60:63], a[108:111], v[180:183]\n\t"
    "ds_read_b64_tr_b16 a[14:15], v224 offset:45064\n\t"
    "v_mfma_f32_16x16x32_bf16 v[184:187], a[56:59], a[112:115], v[184:187]\n\t"
    "v_mfma_f32_16x16x32_bf16 v[184:187], a[60:63], a[116:119], v[184:187]\n\t"
    "buffer_load_dwordx4 v209, s[8:11], s28 offen offset:1024 lds\n\t"
    "v_mfma_f32_16x16x32_bf16 v[188:191], a[56:59], a[120:123], v[188:191]\n\t"
    "v_mfma_f32_16x16x32_bf16 v[188:191], a[60:63], a[124:127], v[188:191]\n\t"
    "buffer_load_dwordx4 v210, s[8:11], s28 offen offset:2048 lds\n\t"
    "s_waitcnt lgkmcnt(4)\n\t"
    "v_mfma_f32_16x16x32_bf16 v[0:3], a[0:3], a[192:195], v[0:3]\n\t"
    "ds_read_b64_tr_b16 a[16:17], v225 offset:32768\n\t"
    "v_mfma_f32_16x16x32_bf16 v[0:3], a[4:7], a[196:199], v[0:3]\n\t"
    "ds_read_b64_tr_b16 a[18:19], v225 offset:36864\n\t"
    "v_mfma_f32_16x16x32_bf16 v[4:7], a[0:3], a[200:203], v[4:7]\n\t"
    "ds_read_b64_tr_b16 a[20:21], v225 offset:40960\n\t"
    "v_mfma_f32_16x16x32_bf16 v[4:7], a[4:7], a[204:207], v[4:7]\n\t"
    "ds_read_b64_tr_b16 a[22:23], v225 offset:45056\n\t"
    "v_mfma_f32_16x16x32_bf16 v[8:11], a[0:3], a[208:211], v[8:11]\n\t"
    "buffer_load_dwordx4 v211, s[8:11], s28 offen offset:3072 lds\n\t"
    "v_mfma_f32_16x16x32_bf16 v[8:11], a[4:7], a[212:215], v[8:11]\n\t"
    "v_mfma_f32_16x16x32_bf16 v[12:15], a[0:3], a[216:219], v[12:15]\n\t"
    "s_mov_b32 m0, s33\n\t"
    "s_add_u32 s28, s29, 16384\n\t"
    "buffer_load_dwordx4 v208, s[8:11], s28 offen lds\n\t"
    "v_mfma_f32_16x16x32_bf16 v[12:15], a[4:7], a[220:223], v[12:15]\n\t"
    "s_waitcnt lgkmcnt(4)\n\t"
    "v_mfma_f32_16x16x32_bf16 v[16:19], a[8:11], a[192:195], v[16:19]\n\t"
    "ds_read_b64_tr_b16 a[24:25], v225 offset:32776\n\t"
    "v_mfma_f32_16x16x32_bf16 v[16:19], a[12:15], a[196:199], v[16:19]\n\t"
    "ds_read_b64_tr_b16 a[26:27], v225 offset:36872\n\t"
    "v_mfma_f32_16x16x32_bf16 v[20:23], a[8:11], a[200:203], v[20:23]\n\t"
    "ds_read_b64_tr_b16 a[28:29], v225 offset:40968\n\t"
    "v_mfma_f32_16x16x32_bf16 v[20:23], a[12:15], a[204:207], v[20:23]\n\t"
    "ds_read_b64_tr_b16 a[30:31], v225 offset:45064\n\t"
    "v_mfma_f32_16x16x32_bf16 v[24:27], a[8:11], a[208:211], v[24:27]\n\t"
    "v_mfma_f32_16x16x32_bf16 v[24:27], a[12:15], a[212:215], v[24:27]\n\t"
    "buffer_load_dwordx4 v209, s[8:11], s28 offen offset:1024 lds\n\t"
    "v_mfma_f32_16x16x32_bf16 v[28:31], a[8:11], a[216:219], v[28:31]\n\t"
    "v_mfma_f32_16x16x32_bf16 v[28:31], a[12:15], a[220:223], v[28:31]\n\t"
    "buffer_load_dwordx4 v210, s[8:11], s28 offen offset:2048 lds\n\t"
    "s_waitcnt lgkmcnt(4)\n\t"
    "v_mfma_f32_16x16x32_bf16 v[32:35], a[16:19], a[192:195], v[32:35]\n\t"
    "ds_read_b64_tr_b16 a[32:33], v226 offset:32768\n\t"
    "v_mfma_f32_16x16x32_bf16 v[32:35], a[20:23], a[196:199], v[32:35]\n\t"
    "ds_read_b64_tr_b16 a[34:35], v226 offset:36864\n\t"
    "v_mfma_f32_16x16x32_bf16 v[36:39], a[16:19], a[200:203], v[36:39]\n\t"
    "ds_read_b64_tr_b16 a[36:37], v226 offset:40960\n\t"
    "v_mfma_f32_16x16x32_bf16 v[36:39], a[20:23], a[204:207], v[36:39]\n\t"
    "ds_read_b64_tr_b16 a[38:39], v226 offset:45056\n\t"
    "v_mfma_f32_16x16x32_bf16 v[40:43], a[16:19], a[208:211], v[40:43]\n\t"
    "buffer_load_dwordx4 v211, s[8:11], s28 offen offset:3072 lds\n\t"
    "v_mfma_f32_16x16x32_bf16 v[40:43], a[20:23], a[212:215], v[40:43]\n\t"
    "v_mfma_f32_16x16x32_bf16 v[44:47], a[16:19], a[216:219], v[44:47]\n\t"
    "s_mov_b32 m0, s36\n\t"
    "s_add_u32 s28, s29, 16384\n\t"
    "buffer_load_dwordx4 v212, s[12:15], s28 offen lds\n\t"
    "v_mfma_f32_16x16x32_bf16 v[44:47], a[20:23], a[220:223], v[44:47]\n\t"
    "s_waitcnt lgkmcnt(4)\n\t"
    "v_mfma_f32_16x16x32_bf16 v[48:51], a[24:27], a[192:195], v[48:51]\n\t"
    "ds_read_b64_tr_b16 a[40:41], v226 offset:32776\n\t"
    "v_mfma_f32_16x16x32_bf16 v[48:51], a[28:31], a[196:199], v[48:51]\n\t"
    "ds_read_b64_tr_b16 a[42:43], v226 offset:36872\n\t"
    "v_mfma_f32_16x16x32_bf16 v[52:55], a[24:27], a[200:203], v[52:55]\n\t"
    "ds_read_b64_tr_b16 a[44:45], v226 offset:40968\n\t"
    "v_mfma_f32_16x16x32_bf16 v[52:55], a[28:31], a[204:207], v[52:55]\n\t"
    "ds_read_b64_tr_b16 a[46:47], v226 offset:45064\n\t"
    "v_mfma_f32_16x16x32_bf16 v[56:59], a[24:27], a[208:211], v[56:59]\n\t"
    "v_mfma_f32_16x16x32_bf16 v[56:59], a[28:31], a[212:215], v[56:59]\n\t"
    "buffer_load_dwordx4 v213, s[12:15], s28 offen offset:1024 lds\n\t"
    "v_mfma_f32_16x16x32_bf16 v[60:63], a[24:27], a[216:219], v[60:63]\n\t"
    "v_mfma_f32_16x16x32_bf16 v[60:63], a[28:31], a[220:223], v[60:63]\n\t"
    "buffer_load_dwordx4 v212, s[12:15], s28 offen offset:2048 lds\n\t"
    "s_waitcnt lgkmcnt(4)\n\t"
    "v_mfma_f32_16x16x32_bf16 v[64:67], a[32:35], a[192:195], v[64:67]\n\t"
    "ds_read_b64_tr_b16 a[48:49], v227 offset:32768\n\t"
    "v_mfma_f32_16x16x32_bf16 v[64:67], a[36:39], a[196:199], v[64:67]\n\t"
    "ds_read_b64_tr_b16 a[50:51], v227 offset:36864\n\t"
    "v_mfma_f32_16x16x32_bf16 v[68:71], a[32:35], a[200:203], v[68:71]\n\t"
    "ds_read_b64_tr_b16 a[52:53], v227 offset:40960\n\t"
    "v_mfma_f32_16x16x32_bf16 v[68:71], a[36:39], a[204:207], v[68:71]\n\t"
    "ds_read_b64_tr_b16 a[54:55], v227 offset:45056\n\t"
    "v_mfma_f32_16x16x32_bf16 v[72:75], a[32:35], a[208:211], v[72:75]\n\t"
    "buffer_load_dwordx4 v213, s[12:15], s28 offen offset:3072 lds\n\t"
    "v_mfma_f32_16x16x32_bf16 v[72:75], a[36:39], a[212:215], v[72:75]\n\t"
    "v_mfma_f32_16x16x32_bf16 v[76:79], a[32:35], a[216:219], v[76:79]\n\t"
    "v_mfma_f32_16x16x32_bf16 v[76:79], a[36:39], a[220:223], v[76:79]\n\t"
    "s_waitcnt lgkmcnt(4)\n\t"
    "v_mfma_f32_16x16x32_bf16 v[80:83], a[40:43], a[192:195], v[80:83]\n\t"
    "ds_read_b64_tr_b16 a[56:57], v227 offset:32776\n\t"
    "v_mfma_f32_16x16x32_bf16 v[80:83], a[44:47], a[196:199], v[80:83]\n\t"
    "ds_read_b64_tr_b16 a[58:59], v227 offset:36872\n\t"
    "v_mfma_f32_16x16x32_bf16 v[84:87], a[40:43], a[200:203], v[84:87]\n\t"
    "ds_read_b64_tr_b16 a[60:61], v227 offset:40968\n\t"
    "v_mfma_f32_16x16x32_bf16 v[84:87], a[44:47], a[204:207], v[84:87]\n\t"
    "ds_read_b64_tr_b16 a[62:63], v227 offset:45064\n\t"
    "v_mfma_f32_16x16x32_bf16 v[88:91], a[40:43], a[208:211], v[88:91]\n\t"
    "v_mfma_f32_16x16x32_bf16 v[88:91], a[44:47], a[212:215], v[88:91]\n\t"
    "v_mfma_f32_16x16x32_bf16 v[92:95], a[40:43], a[216:219], v[92:95]\n\t"
    "v_mfma_f32_16x16x32_bf16 v[92:95], a[44:47], a[220:223], v[92:95]\n\t"
    "s_waitcnt lgkmcnt(0)\n\t"
    "s_waitcnt vmcnt(12)\n\t"
    "s_barrier\n\t"
    "v_mfma_f32_16x16x32_bf16 v[160:163], a[48:51], a[192:195], v[160:163]\n\t"
    "ds_read_b64_tr_b16 a[0:1], v224 offset:49152\n\t"
    "v_mfma_f32_16x16x32_bf16 v[160:163], a[52:55], a[196:199], v[160:163]\n\t"
    "ds_read_b64_tr_b16 a[2:3], v224 offset:53248\n\t"
    "v_mfma_f32_16x16x32_bf16 v[164:167], a[48:51], a[200:203], v[164:167]\n\t"
    "ds_read_b64_tr_b16 a[4:5], v224 offset:57344\n\t"
    "v_mfma_f32_16x16x32_bf16 v[164:167], a[52:55], a[204:207], v[164:167]\n\t"
    "ds_read_b64_tr_b16 a[6:7], v224 offset:61440\n\t"
    "v_mfma_f32_16x16x32_bf16 v[168:171], a[48:51], a[208:211], v[168:171]\n\t"
    "v_mfma_f32_16x16x32_bf16 v[168:171], a[52:55], a[212:215], v[168:171]\n\t"
    "s_mov_b32 m0, s34\n\t"
    "s_add_u32 s28, s29, 32768\n\t"
    "buffer_load_dwordx4 v208, s[8:11], s28 offen lds\n\t"
    "v_mfma_f32_16x16x32_bf16 v[172:175], a[48:51], a[216:219], v[172:175]\n\t"
    "v_mfma_f32_16x16x32_bf16 v[172:175], a[52:55], a[220:223], v[172:175]\n\t"
    "s_waitcnt lgkmcnt(4)\n\t"
    "v_mfma_f32_16x16x32_bf16 v[176:179], a[56:59], a[192:195], v[176:179]\n\t"
    "ds_read_b64_tr_b16 a[8:9], v224 offset:49160\n\t"
    "v_mfma_f32_16x16x32_bf16 v[176:179], a[60:63], a[196:199], v[176:179]\n\t"
    "ds_read_b64_tr_b16 a[10:11], v224 offset:53256\n\t"
    "v_mfma_f32_16x16x32_bf16 v[180:183], a[56:59], a[200:203], v[180:183]\n\t"
    "ds_read_b64_tr_b16 a[12:13], v224 offset:57352\n\t"
    "v_mfma_f32_16x16x32_bf16 v[180:183], a[60:63], a[204:207], v[180:183]\n\t"
    "ds_read_b64_tr_b16 a[14:15], v224 offset:61448\n\t"
    "v_mfma_f32_16x16x32_bf16 v[184:187], a[56:59], a[208:211], v[184:187]\n\t"
    "v_mfma_f32_16x16x32_bf16 v[184:187], a[60:63], a[212:215], v[184:187]\n\t"
    "buffer_load_dwordx4 v209, s[8:11], s28 offen offset:1024 lds\n\t"
    "v_mfma_f32_16x16x32_bf16 v[188:191], a[56:59], a[216:219], v[188:191]\n\t"
    "v_mfma_f32_16x16x32_bf16 v[188:191], a[60:63], a[220:223], v[188:191]\n\t"
    "buffer_load_dwordx4 v210, s[8:11], s28 offen offset:2048 lds\n\t"
    "s_waitcnt lgkmcnt(4)\n\t"
    "v_mfma_f32_16x16x32_bf16 v[0:3], a[0:3], a[224:227], v[0:3]\n\t"
    "ds_read_b64_tr_b16 a[16:17], v225 offset:49152\n\t"
    "v_mfma_f32_16x16x32_bf16 v[0:3], a[4:7], a[228:231], v[0:3]\n\t"
    "ds_read_b64_tr_b16 a[18:19], v225 offset:53248\n\t"
    "v_mfma_f32_16x16x32_bf16 v[4:7], a[0:3], a[232:235], v[4:7]\n\t"
    "ds_read_b64_tr_b16 a[20:21], v225 offset:57344\n\t"
    "v_mfma_f32_16x16x32_bf16 v[4:7], a[4:7], a[236:239], v[4:7]\n\t"
    "ds_read_b64_tr_b16 a[22:23], v225 offset:61440\n\t"
    "v_mfma_f32_16x16x32_bf16 v[8:11], a[0:3], a[240:243], v[8:11]\n\t"
    "buffer_load_dwordx4 v211, s[8:11], s28 offen offset:3072 lds\n\t"
    "v_mfma_f32_16x16x32_bf16 v[8:11], a[4:7], a[244:247], v[8:11]\n\t"
    "v_mfma_f32_16x16x32_bf16 v[12:15], a[0:3], a[248:251], v[12:15]\n\t"
    "s_mov_b32 m0, s37\n\t"
    "s_add_u32 s28, s29, 32768\n\t"
    "buffer_load_dwordx4 v212, s[12:15], s28 offen lds\n\t"
    "v_mfma_f32_16x16x32_bf16 v[12:15], a[4:7], a[252:255], v[12:15]\n\t"
    "s_waitcnt lgkmcnt(4)\n\t"
    "v_mfma_f32_16x16x32_bf16 v[16:19], a[8:11], a[224:227], v[16:19]\n\t"
    "ds_read_b64_tr_b16 a[24:25], v225 offset:49160\n\t"
    "v_mfma_f32_16x16x32_bf16 v[16:19], a[12:15], a[228:231], v[16:19]\n\t"
    "ds_read_b64_tr_b16 a[26:27], v225 offset:53256\n\t"
    "v_mfma_f32_16x16x32_bf16 v[20:23], a[8:11], a[232:235], v[20:23]\n\t"
    "ds_read_b64_tr_b16 a[28:29], v225 offset:57352\n\t"
    "v_mfma_f32_16x16x32_bf16 v[20:23], a[12:15], a[236:239], v[20:23]\n\t"
    "ds_read_b64_tr_b16 a[30:31], v225 offset:61448\n\t"
    "v_mfma_f32_16x16x32_bf16 v[24:27], a[8:11], a[240:243], v[24:27]\n\t"
    "v_mfma_f32_16x16x32_bf16 v[24:27], a[12:15], a[244:247], v[24:27]\n\t"
    "buffer_load_dwordx4 v213, s[12:15], s28 offen offset:1024 lds\n\t"
    "v_mfma_f32_16x16x32_bf16 v[28:31], a[8:11], a[248:251], v[28:31]\n\t"
    "v_mfma_f32_16x16x32_bf16 v[28:31], a[12:15], a[252:255], v[28:31]\n\t"
    "buffer_load_dwordx4 v212, s[12:15], s28 offen offset:2048 lds\n\t"
    "s_waitcnt lgkmcnt(4)\n\t"
    "v_mfma_f32_16x16x32_bf16 v[32:35], a[16:19], a[224:227], v[32:35]\n\t"
    "ds_read_b64_tr_b16 a[32:33], v226 offset:49152\n\t"
    "v_mfma_f32_16x16x32_bf16 v[32:35], a[20:23], a[228:231], v[32:35]\n\t"
    "ds_read_b64_tr_b16 a[34:35], v226 offset:53248\n\t"
    "v_mfma_f32_16x16x32_bf16 v[36:39], a[16:19], a[232:235], v[36:39]\n\t"
    "ds_read_b64_tr_b16 a[36:37], v226 offset:57344\n\t"
    "v_mfma_f32_16x16x32_bf16 v[36:39], a[20:23], a[236:239], v[36:39]\n\t"
    "ds_read_b64_tr_b16 a[38:39], v226 offset:61440\n\t"
    "v_mfma_f32_16x16x32_bf16 v[40:43], a[16:19], a[240:243], v[40:43]\n\t"
    "buffer_load_dwordx4 v213, s[12:15], s28 offen offset:3072 lds\n\t"
    "v_mfma_f32_16x16x32_bf16 v[40:43], a[20:23], a[244:247], v[40:43]\n\t"
    "v_mfma_f32_16x16x32_bf16 v[44:47], a[16:19], a[248:251], v[44:47]\n\t"
    "v_mfma_f32_16x16x32_bf16 v[44:47], a[20:23], a[252:255], v[44:47]\n\t"
    "s_waitcnt lgkmcnt(4)\n\t"
    "v_mfma_f32_16x16x32_bf16 v[48:51], a[24:27], a[224:227], v[48:51]\n\t"
    "ds_read_b64_tr_b16 a[40:41], v226 offset:49160\n\t"
    "v_cvt_pk_bf16_f32 v96, v0, v1\n\t"
    "v_mfma_f32_16x16x32_bf16 v[48:51], a[28:31], a[228:231], v[48:51]\n\t"
    "ds_read_b64_tr_b16 a[42:43], v226 offset:53256\n\t"
    "v_cvt_pk_bf16_f32 v97, v2, v3\n\t"
    "v_mfma_f32_16x16x32_bf16 v[52:55], a[24:27], a[232:235], v[52:55]\n\t"
    "ds_read_b64_tr_b16 a[44:45], v226 offset:57352\n\t"
    "v_cvt_pk_bf16_f32 v98, v16, v17\n\t"
    "v_mfma_f32_16x16x32_bf16 v[52:55], a[28:31], a[236:239], v[52:55]\n\t"
    "ds_read_b64_tr_b16 a[46:47], v226 offset:61448\n\t"
    "v_cvt_pk_bf16_f32 v99, v18, v19\n\t"
    "v_mfma_f32_16x16x32_bf16 v[56:59], a[24:27], a[240:243], v[56:59]\n\t"
    "v_cvt_pk_bf16_f32 v112, v4, v5\n\t"
    "v_cvt_pk_bf16_f32 v113, v6, v7\n\t"
    "v_mfma_f32_16x16x32_bf16 v[56:59], a[28:31], a[244:247], v[56:59]\n\t"
    "v_cvt_pk_bf16_f32 v114, v20, v21\n\t"
    "v_cvt_pk_bf16_f32 v115, v22, v23\n\t"
    "v_mfma_f32_16x16x32_bf16 v[60:63], a[24:27], a[248:251], v[60:63]\n\t"
    "v_cvt_pk_bf16_f32 v128, v8, v9\n\t"
    "v_cvt_pk_bf16_f32 v129, v10, v11\n\t"
    "v_mfma_f32_16x16x32_bf16 v[60:63], a[28:31], a[252:255], v[60:63]\n\t"
    "v_cvt_pk_bf16_f32 v130, v24, v25\n\t"
    "v_cvt_pk_bf16_f32 v131, v26, v27\n\t"
    "s_waitcnt lgkmcnt(4)\n\t"
    "v_mfma_f32_16x16x32_bf16 v[64:67], a[32:35], a[224:227], v[64:67]\n\t"
    "ds_read_b64_tr_b16 a[48:49], v227 offset:49152\n\t"
    "v_cvt_pk_bf16_f32 v144, v12, v13\n\t"
    "v_mfma_f32_16x16x32_bf16 v[64:67], a[36:39], a[228:231], v[64:67]\n\t"
    "ds_read_b64_tr_b16 a[50:51], v227 offset:53248\n\t"
    "v_cvt_pk_bf16_f32 v145, v14, v15\n\t"
    "v_mfma_f32_16x16x32_bf16 v[68:71], a[32:35], a[232:235], v[68:71]\n\t"
    "ds_read_b64_tr_b16 a[52:53], v227 offset:57344\n\t"
    "v_cvt_pk_bf16_f32 v146, v28, v29\n\t"
    "v_mfma_f32_16x16x32_bf16 v[68:71], a[36:39], a[236:239], v[68:71]\n\t"
    "ds_read_b64_tr_b16 a[54:55], v227 offset:61440\n\t"
    "v_cvt_pk_bf16_f32 v147, v30, v31\n\t"
    "v_mfma_f32_16x16x32_bf16 v[72:75], a[32:35], a[240:243], v[72:75]\n\t"
    "v_mfma_f32_16x16x32_bf16 v[72:75], a[36:39], a[244:247], v[72:75]\n\t"
    "v_mfma_f32_16x16x32_bf16 v[76:79], a[32:35], a[248:251], v[76:79]\n\t"
    "v_mfma_f32_16x16x32_bf16 v[76:79], a[36:39], a[252:255], v[76:79]\n\t"
    "s_waitcnt lgkmcnt(4)\n\t"
    "v_mfma_f32_16x16x32_bf16 v[80:83], a[40:43], a[224:227], v[80:83]\n\t"
    "ds_read_b64_tr_b16 a[56:57], v227 offset:49160\n\t"
    "v_cvt_pk_bf16_f32 v100, v32, v33\n\t"
    "v_mfma_f32_16x16x32_bf16 v[80:83], a[44:47], a[228:231], v[80:83]\n\t"
    "ds_read_b64_tr_b16 a[58:59], v227 offset:53256\n\t"
    "v_cvt_pk_bf16_f32 v101, v34, v35\n\t"
    "v_mfma_f32_16x16x32_bf16 v[84:87], a[40:43], a[232:235], v[84:87]\n\t"
    "ds_read_b64_tr_b16 a[60:61], v227 offset:57352\n\t"
    "v_cvt_pk_bf16_f32 v102, v48, v49\n\t"
    "v_mfma_f32_16x16x32_bf16 v[84:87], a[44:47], a[236:239], v[84:87]\n\t"
    "ds_read_b64_tr_b16 a[62:63], v227 offset:61448\n\t"
    "v_cvt_pk_bf16_f32 v103, v50, v51\n\t"
    "v_mfma_f32_16x16x32_bf16 v[88:91], a[40:43], a[240:243], v[88:91]\n\t"
    "v_cvt_pk_bf16_f32 v116, v36, v37\n\t"
    "v_cvt_pk_bf16_f32 v117, v38, v39\n\t"
    "v_mfma_f32_16x16x32_bf16 v[88:91], a[44:47], a[244:247], v[88:91]\n\t"
    "v_cvt_pk_bf16_f32 v118, v52, v53\n\t"
    "v_cvt_pk_bf16_f32 v119, v54, v55\n\t"
    "v_mfma_f32_16x16x32_bf16 v[92:95], a[40:43], a[248:251], v[92:95]\n\t"
    "v_cvt_pk_bf16_f32 v132, v40, v41\n\t"
    "v_cvt_pk_bf16_f32 v133, v42, v43\n\t"
    "v_mfma_f32_16x16x32_bf16 v[92:95], a[44:47], a[252:255], v[92:95]\n\t"
    "v_cvt_pk_bf16_f32 v134, v56, v57\n\t"
    "v_cvt_pk_bf16_f32 v135, v58, v59\n\t"
    "s_waitcnt lgkmcnt(4)\n\t"
    "v_mfma_f32_16x16x32_bf16 v[160:163], a[48:51], a[224:227], v[160:163]\n\t"
    "v_cvt_pk_bf16_f32 v148, v44, v45\n\t"
    "v_cvt_pk_bf16_f32 v149, v46, v47\n\t"
    "v_mfma_f32_16x16x32_bf16 v[160:163], a[52:55], a[228:231], v[160:163]\n\t"
    "v_cvt_pk_bf16_f32 v150, v60, v61\n\t"
    "v_cvt_pk_bf16_f32 v151, v62, v63\n\t"
    "v_mfma_f32_16x16x32_bf16 v[164:167], a[48:51], a[232:235], v[164:167]\n\t"
    "v_mfma_f32_16x16x32_bf16 v[164:167], a[52:55], a[236:239], v[164:167]\n\t"
    "v_mfma_f32_16x16x32_bf16 v[168:171], a[48:51], a[240:243], v[168:171]\n\t"
    "v_mfma_f32_16x16x32_bf16 v[168:171], a[52:55], a[244:247], v[168:171]\n\t"
    "v_mfma_f32_16x16x32_bf16 v[172:175], a[48:51], a[248:251], v[172:175]\n\t"
    "v_mfma_f32_16x16x32_bf16 v[172:175], a[52:55], a[252:255], v[172:175]\n\t"
    "s_waitcnt lgkmcnt(0)\n\t"
    "v_mfma_f32_16x16x32_bf16 v[176:179], a[56:59], a[224:227], v[176:179]\n\t"
    "v_cvt_pk_bf16_f32 v104, v64, v65\n\t"
    "v_cvt_pk_bf16_f32 v105, v66, v67\n\t"
    "v_mfma_f32_16x16x32_bf16 v[176:179], a[60:63], a[228:231], v[176:179]\n\t"
    "v_cvt_pk_bf16_f32 v106, v80, v81\n\t"
    "v_cvt_pk_bf16_f32 v107, v82, v83\n\t"
    "v_mfma_f32_16x16x32_bf16 v[180:183], a[56:59], a[232:235], v[180:183]\n\t"
    "v_cvt_pk_bf16_f32 v120, v68, v69\n\t"
    "v_cvt_pk_bf16_f32 v121, v70, v71\n\t"
    "v_mfma_f32_16x16x32_bf16 v[180:183], a[60:63], a[236:239], v[180:183]\n\t"
    "v_cvt_pk_bf16_f32 v122, v84, v85\n\t"
    "v_cvt_pk_bf16_f32 v123, v86, v87\n\t"
    "v_mfma_f32_16x16x32_bf16 v[184:187], a[56:59], a[240:243], v[184:187]\n\t"
    "v_cvt_pk_bf16_f32 v136, v72, v73\n\t"
    "v_cvt_pk_bf16_f32 v137, v74, v75\n\t"
    "v_mfma_f32_16x16x32_bf16 v[184:187], a[60:63], a[244:247], v[184:187]\n\t"
    "v_cvt_pk_bf16_f32 v138, v88, v89\n\t"
    "v_cvt_pk_bf16_f32 v139, v90, v91\n\t"
    "v_mfma_f32_16x16x32_bf16 v[188:191], a[56:59], a[248:251], v[188:191]\n\t"
    "v_cvt_pk_bf16_f32 v152, v76, v77\n\t"
    "v_cvt_pk_bf16_f32 v153, v78, v79\n\t"
    "v_mfma_f32_16x16x32_bf16 v[188:191], a[60:63], a[252:255], v[188:191]\n\t"
    "v_cvt_pk_bf16_f32 v154, v92, v93\n\t"
    "v_cvt_pk_bf16_f32 v155, v94, v95\n\t"
    "s_waitcnt vmcnt(16)\n\t"
    "s_barrier\n\t"
    "ds_read_b128 a[128:131], v196 offset:0\n\t"
    "ds_read_b128 a[132:135], v197 offset:0\n\t"
    "ds_read_b128 a[136:139], v198 offset:0\n\t"
    "ds_read_b128 a[140:143], v199 offset:0\n\t"
    "ds_read_b128 a[144:147], v196 offset:4096\n\t"
    "ds_read_b128 a[148:151], v197 offset:4096\n\t"
    "ds_read_b128 a[152:155], v198 offset:4096\n\t"
    "ds_read_b128 a[156:159], v199 offset:4096\n\t"
    "ds_read_b128 a[160:163], v196 offset:8192\n\t"
    "ds_read_b128 a[164:167], v197 offset:8192\n\t"
    "ds_read_b128 a[168:171], v198 offset:8192\n\t"
    "ds_read_b128 a[172:175], v199 offset:8192\n\t"
    "ds_read_b128 a[176:179], v196 offset:12288\n\t"
    "ds_read_b128 a[180:183], v197 offset:12288\n\t"
    "ds_read_b128 a[184:187], v198 offset:12288\n\t"
    "ds_read_b128 a[188:191], v199 offset:12288\n\t"
    "s_nop 3\n\t"
    "v_cvt_pk_bf16_f32 v108, v160, v161\n\t"
    "v_cvt_pk_bf16_f32 v109, v162, v163\n\t"
    "v_cvt_pk_bf16_f32 v110, v176, v177\n\t"
    "v_cvt_pk_bf16_f32 v111, v178, v179\n\t"
    "v_cvt_pk_bf16_f32 v124, v164, v165\n\t"
    "v_cvt_pk_bf16_f32 v125, v166, v167\n\t"
    "v_cvt_pk_bf16_f32 v126, v180, v181\n\t"
    "v_cvt_pk_bf16_f32 v127, v182, v183\n\t"
    "v_cvt_pk_bf16_f32 v140, v168, v169\n\t"
    "v_cvt_pk_bf16_f32 v141, v170, v171\n\t"
    "v_cvt_pk_bf16_f32 v142, v184, v185\n\t"
    "v_cvt_pk_bf16_f32 v143, v186, v187\n\t"
    "v_cvt_pk_bf16_f32 v156, v172, v173\n\t"
    "v_cvt_pk_bf16_f32 v157, v174, v175\n\t"
    "v_cvt_pk_bf16_f32 v158, v188, v189\n\t"
    "v_cvt_pk_bf16_f32 v159, v190, v191\n\t"
    "v_mov_b32_e32 v214, 0\n\t"
    "v_mov_b32_e32 v215, 0\n\t"
    "v_mov_b32_e32 v216, 0\n\t"
    "v_mov_b32_e32 v217, 0\n\t"
    "v_mov_b32_e32 v192, 0x3f803f80\n\t"
    "v_mov_b32_e32 v193, 0x3f803f80\n\t"
    "v_mov_b32_e32 v194, 0x3f803f80\n\t"
    "v_mov_b32_e32 v195, 0x3f803f80\n\t"
    "s_waitcnt lgkmcnt(12)\n\t"
    "v_mfma_f32_16x16x32_bf16 v[0:3], a[128:131], v[96:99], 0\n\t"
    "v_mfma_f32_16x16x32_bf16 v[0:3], a[132:135], v[100:103], v[0:3]\n\t"
    "v_mfma_f32_16x16x32_bf16 v[0:3], a[136:139], v[104:107], v[0:3]\n\t"
    "v_mfma_f32_16x16x32_bf16 v[0:3], a[140:143], v[108:111], v[0:3]\n\t"
    "v_mfma_f32_16x16x32_bf16 v[16:19], a[128:131], v[112:115], 0\n\t"
    "v_mfma_f32_16x16x32_bf16 v[16:19], a[132:135], v[116:119], v[16:19]\n\t"
    "v_mfma_f32_16x16x32_bf16 v[16:19], a[136:139], v[120:123], v[16:19]\n\t"
    "v_mfma_f32_16x16x32_bf16 v[16:19], a[140:143], v[124:127], v[16:19]\n\t"
    "v_mfma_f32_16x16x32_bf16 v[32:35], a[128:131], v[128:131], 0\n\t"
    "v_mfma_f32_16x16x32_bf16 v[32:35], a[132:135], v[132:135], v[32:35]\n\t"
    "v_mfma_f32_16x16x32_bf16 v[32:35], a[136:139], v[136:139], v[32:35]\n\t"
    "v_mfma_f32_16x16x32_bf16 v[32:35], a[140:143], v[140:143], v[32:35]\n\t"
    "v_mfma_f32_16x16x32_bf16 v[48:51], a[128:131], v[144:147], 0\n\t"
    "v_mfma_f32_16x16x32_bf16 v[48:51], a[132:135], v[148:151], v[48:51]\n\t"
    "v_mfma_f32_16x16x32_bf16 v[48:51], a[136:139], v[152:155], v[48:51]\n\t"
    "v_mfma_f32_16x16x32_bf16 v[48:51], a[140:143], v[156:159], v[48:51]\n\t"
    "s_waitcnt lgkmcnt(0)\n\t"
    "s_nop 3\n\t"
    "v_max3_f32 v64, v0, v1, v2\n\t"
    "v_max3_f32 v65, v16, v17, v18\n\t"
    "v_max3_f32 v66, v32, v33, v34\n\t"
    "v_max3_f32 v67, v48, v49, v50\n\t"
    "v_max_f32_e32 v64, v64, v3\n\t"
    "v_max_f32_e32 v65, v65, v19\n\t"
    "v_max_f32_e32 v66, v66, v35\n\t"
    "v_max_f32_e32 v67, v67, v51\n\t"
    "v_mov_b32_e32 v68, v64\n\t"
    "v_mov_b32_e32 v69, v65\n\t"
    "v_mov_b32_e32 v70, v66\n\t"
    "v_mov_b32_e32 v71, v67\n\t"
    "s_nop 1\n\t"
    "v_permlane32_swap_b32_e32 v64, v68\n\t"
    "v_permlane32_swap_b32_e32 v65, v69\n\t"
    "v_permlane32_swap_b32_e32 v66, v70\n\t"
    "v_permlane32_swap_b32_e32 v67, v71\n\t"
    "s_nop 1\n\t"
    "v_max_f32_e32 v64, v64, v68\n\t"
    "v_max_f32_e32 v65, v65, v69\n\t"
    "v_max_f32_e32 v66, v66, v70\n\t"
    "v_max_f32_e32 v67, v67, v71\n\t"
    "v_mov_b32_e32 v68, v64\n\t"
    "v_mov_b32_e32 v69, v65\n\t"
    "v_mov_b32_e32 v70, v66\n\t"
    "v_mov_b32_e32 v71, v67\n\t"
    "s_nop 1\n\t"
    "v_permlane16_swap_b32_e32 v64, v68\n\t"
    "v_permlane16_swap_b32_e32 v65, v69\n\t"
    "v_permlane16_swap_b32_e32 v66, v70\n\t"
    "v_permlane16_swap_b32_e32 v67, v71\n\t"
    "s_nop 1\n\t"
    "v_max_f32_e32 v64, v64, v68\n\t"
    "v_max_f32_e32 v65, v65, v69\n\t"
    "v_max_f32_e32 v66, v66, v70\n\t"
    "v_max_f32_e32 v67, v67, v71\n\t"
    "v_sub_f32_e32 v160, 0, v64\n\t"
    "v_sub_f32_e32 v161, 0, v64\n\t"
    "v_sub_f32_e32 v162, 0, v64\n\t"
    "v_sub_f32_e32 v163, 0, v64\n\t"
    "v_sub_f32_e32 v164, 0, v65\n\t"
    "v_sub_f32_e32 v165, 0, v65\n\t"
    "v_sub_f32_e32 v166, 0, v65\n\t"
    "v_sub_f32_e32 v167, 0, v65\n\t"
    "v_sub_f32_e32 v168, 0, v66\n\t"
    "v_sub_f32_e32 v169, 0, v66\n\t"
    "v_sub_f32_e32 v170, 0, v66\n\t"
    "v_sub_f32_e32 v171, 0, v66\n\t"
    "v_sub_f32_e32 v172, 0, v67\n\t"
    "v_sub_f32_e32 v173, 0, v67\n\t"
    "v_sub_f32_e32 v174, 0, v67\n\t"
    "v_sub_f32_e32 v175, 0, v67\n\t"
    "v_sub_f32_e32 v0, v0, v64\n\t"
    "v_sub_f32_e32 v1, v1, v64\n\t"
    "v_sub_f32_e32 v2, v2, v64\n\t"
    "v_sub_f32_e32 v3, v3, v64\n\t"
    "v_sub_f32_e32 v16, v16, v65\n\t"
    "v_sub_f32_e32 v17, v17, v65\n\t"
    "v_sub_f32_e32 v18, v18, v65\n\t"
    "v_sub_f32_e32 v19, v19, v65\n\t"
    "v_sub_f32_e32 v32, v32, v66\n\t"
    "v_sub_f32_e32 v33, v33, v66\n\t"
    "v_sub_f32_e32 v34, v34, v66\n\t"
    "v_sub_f32_e32 v35, v35, v66\n\t"
    "v_sub_f32_e32 v48, v48, v67\n\t"
    "v_sub_f32_e32 v49, v49, v67\n\t"
    "v_sub_f32_e32 v50, v50, v67\n\t"
    "v_sub_f32_e32 v51, v51, v67\n\t"
    "s_add_u32 s28, s29, 49152\n\t"
    "s_mov_b32 s48, 0\n\t"
    "v_mfma_f32_16x16x32_bf16 v[4:7], a[156:159], v[108:111], v[160:163]\n\t"
    "v_mfma_f32_16x16x32_bf16 v[4:7], a[152:155], v[104:107], v[4:7]\n\t"
    "v_mfma_f32_16x16x32_bf16 v[4:7], a[148:151], v[100:103], v[4:7]\n\t"
    "v_mfma_f32_16x16x32_bf16 v[4:7], a[144:147], v[96:99], v[4:7]\n\t"
    "v_mfma_f32_16x16x32_bf16 v[8:11], a[160:163], v[96:99], v[160:163]\n\t"
    "v_mfma_f32_16x16x32_bf16 v[8:11], a[164:167], v[100:103], v[8:11]\n\t"
    "v_mfma_f32_16x16x32_bf16 v[8:11], a[168:171], v[104:107], v[8:11]\n\t"
    "v_mfma_f32_16x16x32_bf16 v[8:11], a[172:175], v[108:111], v[8:11]\n\t"
    "v_mfma_f32_16x16x32_bf16 v[12:15], a[188:191], v[108:111], v[160:163]\n\t"
    "v_mfma_f32_16x16x32_bf16 v[12:15], a[184:187], v[104:107], v[12:15]\n\t"
    "v_mfma_f32_16x16x32_bf16 v[12:15], a[180:183], v[100:103], v[12:15]\n\t"
    "v_mfma_f32_16x16x32_bf16 v[12:15], a[176:179], v[96:99], v[12:15]\n\t"
    "v_exp_f32_e32 v0, v0\n\t"
    "v_exp_f32_e32 v1, v1\n\t"
    "v_exp_f32_e32 v2, v2\n\t"
    "v_exp_f32_e32 v3, v3\n\t"
    "v_exp_f32_e32 v4, v4\n\t"
    "v_exp_f32_e32 v5, v5\n\t"
    "v_exp_f32_e32 v6, v6\n\t"
    "v_exp_f32_e32 v7, v7\n\t"
    "v_exp_f32_e32 v8, v8\n\t"
    "v_exp_f32_e32 v9, v9\n\t"
    "v_exp_f32_e32 v10, v10\n\t"
    "v_exp_f32_e32 v11, v11\n\t"
    "v_exp_f32_e32 v12, v12\n\t"
    "v_exp_f32_e32 v13, v13\n\t"
    "v_exp_f32_e32 v14, v14\n\t"
    "v_exp_f32_e32 v15, v15\n\t"
    "v_mfma_f32_16x16x32_bf16 v[20:23], a[156:159], v[124:127], v[164:167]\n\t"
    "v_mfma_f32_16x16x32_bf16 v[20:23], a[152:155], v[120:123], v[20:23]\n\t"
    "v_mfma_f32_16x16x32_bf16 v[20:23], a[148:151], v[116:119], v[20:23]\n\t"
    "v_cvt_pk_bf16_f32 v64, v0, v1\n\t"
    "v_cvt_pk_bf16_f32 v65, v2, v3\n\t"
    "v_mfma_f32_16x16x32_bf16 v[20:23], a[144:147], v[112:115], v[20:23]\n\t"
    "v_exp_f32_e32 v16, v16\n\t"
    "v_mfma_f32_16x16x32_bf16 v[24:27], a[160:163], v[112:115], v[164:167]\n\t"
    "v_exp_f32_e32 v17, v17\n\t"
    "v_mfma_f32_16x16x32_bf16 v[24:27], a[164:167], v[116:119], v[24:27]\n\t"
    "v_exp_f32_e32 v18, v18\n\t"
    "v_mfma_f32_16x16x32_bf16 v[24:27], a[168:171], v[120:123], v[24:27]\n\t"
    "v_exp_f32_e32 v19, v19\n\t"
    "v_mfma_f32_16x16x32_bf16 v[24:27], a[172:175], v[124:127], v[24:27]\n\t"
    "v_exp_f32_e32 v20, v20\n\t"
    "v_mfma_f32_16x16x32_bf16 v[28:31], a[188:191], v[124:127], v[164:167]\n\t"
    "v_exp_f32_e32 v21, v21\n\t"
    "v_mfma_f32_16x16x32_bf16 v[28:31], a[184:187], v[120:123], v[28:31]\n\t"
    "v_exp_f32_e32 v22, v22\n\t"
    "v_mfma_f32_16x16x32_bf16 v[28:31], a[180:183], v[116:119], v[28:31]\n\t"
    "v_exp_f32_e32 v23, v23\n\t"
    "v_mfma_f32_16x16x32_bf16 v[28:31], a[176:179], v[112:115], v[28:31]\n\t"
    "v_exp_f32_e32 v24, v24\n\t"
    "s_waitcnt vmcnt(8)\n\t"
    "s_barrier\n\t"
    "ds_read_b64_tr_b16 a[192:193], v200 offset:0\n\t"
    "v_cvt_pk_bf16_f32 v66, v4, v5\n\t"
    "ds_read_b64_tr_b16 a[194:195], v200 offset:4096\n\t"
    "v_cvt_pk_bf16_f32 v67, v6, v7\n\t"
    "ds_read_b64_tr_b16 a[196:197], v200 offset:8192\n\t"
    "v_cvt_pk_bf16_f32 v68, v8, v9\n\t"
    "ds_read_b64_tr_b16 a[198:199], v200 offset:12288\n\t"
    "v_cvt_pk_bf16_f32 v69, v10, v11\n\t"
    "ds_read_b64_tr_b16 a[200:201], v201 offset:0\n\t"
    "v_cvt_pk_bf16_f32 v70, v12, v13\n\t"
    "ds_read_b64_tr_b16 a[202:203], v201 offset:4096\n\t"
    "v_cvt_pk_bf16_f32 v71, v14, v15\n\t"
    "ds_read_b64_tr_b16 a[204:205], v201 offset:8192\n\t"
    "ds_read_b64_tr_b16 a[206:207], v201 offset:12288\n\t"
    "ds_read_b64_tr_b16 a[208:209], v202 offset:0\n\t"
    "ds_read_b64_tr_b16 a[210:211], v202 offset:4096\n\t"
    "ds_read_b64_tr_b16 a[212:213], v202 offset:8192\n\t"
    "ds_read_b64_tr_b16 a[214:215], v202 offset:12288\n\t"
    "ds_read_b64_tr_b16 a[216:217], v203 offset:0\n\t"
    "ds_read_b64_tr_b16 a[218:219], v203 offset:4096\n\t"
    "ds_read_b64_tr_b16 a[220:221], v203 offset:8192\n\t"
    "v_cvt_pk_bf16_f32 v72, v16, v17\n\t"
    "ds_read_b64_tr_b16 a[222:223], v203 offset:12288\n\t"
    "v_cvt_pk_bf16_f32 v73, v18, v19\n\t"
    "ds_read_b64_tr_b16 a[224:225], v204 offset:0\n\t"
    "v_cvt_pk_bf16_f32 v74, v20, v21\n\t"
    "ds_read_b64_tr_b16 a[226:227], v204 offset:4096\n\t"
    "v_cvt_pk_bf16_f32 v75, v22, v23\n\t"
    "ds_read_b64_tr_b16 a[228:229], v204 offset:8192\n\t"
    "v_add_f32_e32 v214, v214, v8\n\t"
    "ds_read_b64_tr_b16 a[230:231], v204 offset:12288\n\t"
    "v_add_f32_e32 v214, v214, v9\n\t"
    "v_mfma_f32_16x16x32_bf16 v[36:39], a[156:159], v[140:143], v[168:171]\n\t"
    "ds_read_b64_tr_b16 a[232:233], v205 offset:0\n\t"
    "v_add_f32_e32 v214, v214, v10\n\t"
    "v_mfma_f32_16x16x32_bf16 v[36:39], a[152:155], v[136:139], v[36:39]\n\t"
    "ds_read_b64_tr_b16 a[234:235], v205 offset:4096\n\t"
    "v_add_f32_e32 v214, v214, v11\n\t"
    "v_mfma_f32_16x16x32_bf16 v[36:39], a[148:151], v[132:135], v[36:39]\n\t"
    "ds_read_b64_tr_b16 a[236:237], v205 offset:8192\n\t"
    "v_add_f32_e32 v214, v214, v12\n\t"
    "v_mfma_f32_16x16x32_bf16 v[36:39], a[144:147], v[128:131], v[36:39]\n\t"
    "ds_read_b64_tr_b16 a[238:239], v205 offset:12288\n\t"
    "v_add_f32_e32 v214, v214, v13\n\t"
    "v_mfma_f32_16x16x32_bf16 v[40:43], a[160:163], v[128:131], v[168:171]\n\t"
    "ds_read_b64_tr_b16 a[240:241], v206 offset:0\n\t"
    "v_add_f32_e32 v214, v214, v14\n\t"
    "v_mfma_f32_16x16x32_bf16 v[40:43], a[164:167], v[132:135], v[40:43]\n\t"
    "ds_read_b64_tr_b16 a[242:243], v206 offset:4096\n\t"
    "v_add_f32_e32 v214, v214, v15\n\t"
    "v_mfma_f32_16x16x32_bf16 v[40:43], a[168:171], v[136:139], v[40:43]\n\t"
    "ds_read_b64_tr_b16 a[244:245], v206 offset:8192\n\t"
    "v_add_f32_e32 v215, v215, v24\n\t"
    "v_mfma_f32_16x16x32_bf16 v[40:43], a[172:175], v[140:143], v[40:43]\n\t"
    "ds_read_b64_tr_b16 a[246:247], v206 offset:12288\n\t"
    "v_mfma_f32_16x16x32_bf16 v[44:47], a[188:191], v[140:143], v[168:171]\n\t"
    "ds_read_b64_tr_b16 a[248:249], v207 offset:0\n\t"
    "v_mfma_f32_16x16x32_bf16 v[44:47], a[184:187], v[136:139], v[44:47]\n\t"
    "ds_read_b64_tr_b16 a[250:251], v207 offset:4096\n\t"
    "v_mfma_f32_16x16x32_bf16 v[44:47], a[180:183], v[132:135], v[44:47]\n\t"
    "ds_read_b64_tr_b16 a[252:253], v207 offset:8192\n\t"
    "v_mfma_f32_16x16x32_bf16 v[44:47], a[176:179], v[128:131], v[44:47]\n\t"
    "ds_read_b64_tr_b16 a[254:255], v207 offset:12288\n\t"
    "s_waitcnt lgkmcnt(15)\n\t"
    "v_mfma_f32_16x16x32_bf16 a[0:3], a[192:195], v[64:67], 0\n\t"
    "v_exp_f32_e32 v25, v25\n\t"
    "v_mfma_f32_16x16x32_bf16 a[0:3], a[196:199], v[68:71], a[0:3]\n\t"
    "v_exp_f32_e32 v26, v26\n\t"
    "v_mfma_f32_16x16x32_bf16 a[16:19], a[200:203], v[64:67], 0\n\t"
    "v_exp_f32_e32 v27, v27\n\t"
    "v_mfma_f32_16x16x32_bf16 a[16:19], a[204:207], v[68:71], a[16:19]\n\t"
    "v_exp_f32_e32 v28, v28\n\t"
    "v_mfma_f32_16x16x32_bf16 a[32:35], a[208:211], v[64:67], 0\n\t"
    "v_exp_f32_e32 v29, v29\n\t"
    "v_mfma_f32_16x16x32_bf16 a[32:35], a[212:215], v[68:71], a[32:35]\n\t"
    "v_exp_f32_e32 v30, v30\n\t"
    "v_mfma_f32_16x16x32_bf16 a[48:51], a[216:219], v[64:67], 0\n\t"
    "v_exp_f32_e32 v31, v31\n\t"
    "v_mfma_f32_16x16x32_bf16 a[48:51], a[220:223], v[68:71], a[48:51]\n\t"
    "s_mov_b32 m0, s35\n\t"
    "s_waitcnt lgkmcnt(0)\n\t"
    "v_mfma_f32_16x16x32_bf16 a[64:67], a[224:227], v[64:67], 0\n\t"
    "buffer_load_dwordx4 v208, s[8:11], s28 offen lds\n\t"
    "v_cvt_pk_bf16_f32 v76, v24, v25\n\t"
    "v_mfma_f32_16x16x32_bf16 a[64:67], a[228:231], v[68:71], a[64:67]\n\t"
    "v_exp_f32_e32 v32, v32\n\t"
    "v_mfma_f32_16x16x32_bf16 a[80:83], a[232:235], v[64:67], 0\n\t"
    "v_exp_f32_e32 v33, v33\n\t"
    "v_mfma_f32_16x16x32_bf16 a[80:83], a[236:239], v[68:71], a[80:83]\n\t"
    "v_exp_f32_e32 v34, v34\n\t"
    "v_mfma_f32_16x16x32_bf16 a[96:99], a[240:243], v[64:67], 0\n\t"
    "v_exp_f32_e32 v35, v35\n\t"
    "v_mfma_f32_16x16x32_bf16 a[96:99], a[244:247], v[68:71], a[96:99]\n\t"
    "v_exp_f32_e32 v36, v36\n\t"
    "v_mfma_f32_16x16x32_bf16 a[112:115], a[248:251], v[64:67], 0\n\t"
    "buffer_load_dwordx4 v209, s[8:11], s28 offen offset:1024 lds\n\t"
    "v_cvt_pk_bf16_f32 v77, v26, v27\n\t"
    "v_mfma_f32_16x16x32_bf16 a[112:115], a[252:255], v[68:71], a[112:115]\n\t"
    "v_exp_f32_e32 v37, v37\n\t"
    "v_mfma_f32_16x16x32_bf16 v[176:179], v[192:195], v[64:67], 0\n\t"
    "v_exp_f32_e32 v38, v38\n\t"
    "v_exp_f32_e32 v39, v39\n\t"
    "v_exp_f32_e32 v40, v40\n\t"
    "v_exp_f32_e32 v41, v41\n\t"
    "v_exp_f32_e32 v42, v42\n\t"
    "v_mfma_f32_16x16x32_bf16 v[52:55], a[156:159], v[156:159], v[172:175]\n\t"
    "v_exp_f32_e32 v43, v43\n\t"
    "v_mfma_f32_16x16x32_bf16 v[52:55], a[152:155], v[152:155], v[52:55]\n\t"
    "v_exp_f32_e32 v44, v44\n\t"
    "v_mfma_f32_16x16x32_bf16 v[52:55], a[148:151], v[148:151], v[52:55]\n\t"
    "v_exp_f32_e32 v45, v45\n\t"
    "v_mfma_f32_16x16x32_bf16 v[52:55], a[144:147], v[144:147], v[52:55]\n\t"
    "v_exp_f32_e32 v46, v46\n\t"
    "v_mfma_f32_16x16x32_bf16 v[56:59], a[160:163], v[144:147], v[172:175]\n\t"
    "v_exp_f32_e32 v47, v47\n\t"
    "v_mfma_f32_16x16x32_bf16 v[56:59], a[164:167], v[148:151], v[56:59]\n\t"
    "v_cvt_pk_bf16_f32 v78, v28, v29\n\t"
    "v_cvt_pk_bf16_f32 v79, v30, v31\n\t"
    "v_mfma_f32_16x16x32_bf16 v[56:59], a[168:171], v[152:155], v[56:59]\n\t"
    "buffer_load_dwordx4 v210, s[8:11], s28 offen offset:2048 lds\n\t"
    "v_cvt_pk_bf16_f32 v80, v32, v33\n\t"
    "v_mfma_f32_16x16x32_bf16 v[56:59], a[172:175], v[156:159], v[56:59]\n\t"
    "ds_read_b128 a[128:131], v196 offset:16384\n\t"
    "v_cvt_pk_bf16_f32 v81, v34, v35\n\t"
    "v_mfma_f32_16x16x32_bf16 v[60:63], a[188:191], v[156:159], v[172:175]\n\t"
    "ds_read_b128 a[132:135], v197 offset:16384\n\t"
    "v_cvt_pk_bf16_f32 v82, v36, v37\n\t"
    "v_mfma_f32_16x16x32_bf16 v[60:63], a[184:187], v[152:155], v[60:63]\n\t"
    "ds_read_b128 a[136:139], v198 offset:16384\n\t"
    "v_cvt_pk_bf16_f32 v83, v38, v39\n\t"
    "v_mfma_f32_16x16x32_bf16 v[60:63], a[180:183], v[148:151], v[60:63]\n\t"
    "ds_read_b128 a[140:143], v199 offset:16384\n\t"
    "v_cvt_pk_bf16_f32 v84, v40, v41\n\t"
    "v_mfma_f32_16x16x32_bf16 v[60:63], a[176:179], v[144:147], v[60:63]\n\t"
    "ds_read_b128 a[156:159], v199 offset:20480\n\t"
    "v_cvt_pk_bf16_f32 v85, v42, v43\n\t"
    "v_mfma_f32_16x16x32_bf16 a[4:7], a[192:195], v[72:75], 0\n\t"
    "ds_read_b128 a[152:155], v198 offset:20480\n\t"
    "v_cvt_pk_bf16_f32 v86, v44, v45\n\t"
    "v_mfma_f32_16x16x32_bf16 a[4:7], a[196:199], v[76:79], a[4:7]\n\t"
    "buffer_load_dwordx4 v211, s[8:11], s28 offen offset:3072 lds\n\t"
    "v_cvt_pk_bf16_f32 v87, v46, v47\n\t"
    "v_mfma_f32_16x16x32_bf16 a[20:23], a[200:203], v[72:75], 0\n\t"
    "ds_read_b128 a[148:151], v197 offset:20480\n\t"
    "v_add_f32_e32 v215, v215, v25\n\t"
    "v_mfma_f32_16x16x32_bf16 a[20:23], a[204:207], v[76:79], a[20:23]\n\t"
    "ds_read_b128 a[144:147], v196 offset:20480\n\t"
    "v_add_f32_e32 v215, v215, v26\n\t"
    "v_mfma_f32_16x16x32_bf16 a[36:39], a[208:211], v[72:75], 0\n\t"
    "ds_read_b128 a[160:163], v196 offset:24576\n\t"
    "v_add_f32_e32 v215, v215, v27\n\t"
    "v_mfma_f32_16x16x32_bf16 a[36:39], a[212:215], v[76:79], a[36:39]\n\t"
    "ds_read_b128 a[164:167], v197 offset:24576\n\t"
    "v_add_f32_e32 v215, v215, v28\n\t"
    "v_mfma_f32_16x16x32_bf16 a[52:55], a[216:219], v[72:75], 0\n\t"
    "ds_read_b128 a[168:171], v198 offset:24576\n\t"
    "v_add_f32_e32 v215, v215, v29\n\t"
    "v_mfma_f32_16x16x32_bf16 a[52:55], a[220:223], v[76:79], a[52:55]\n\t"
    "ds_read_b128 a[172:175], v199 offset:24576\n\t"
    "v_add_f32_e32 v215, v215, v30\n\t"
    "v_mfma_f32_16x16x32_bf16 a[68:71], a[224:227], v[72:75], 0\n\t"
    "ds_read_b128 a[188:191], v199 offset:28672\n\t"
    "v_add_f32_e32 v215, v215, v31\n\t"
    "v_mfma_f32_16x16x32_bf16 a[68:71], a[228:231], v[76:79], a[68:71]\n\t"
    "s_mov_b32 m0, s38\n\t"
    "v_add_f32_e32 v216, v216, v40\n\t"
    "v_mfma_f32_16x16x32_bf16 a[84:87], a[232:235], v[72:75], 0\n\t"
    "ds_read_b128 a[184:187], v198 offset:28672\n\t"
    "v_add_f32_e32 v216, v216, v41\n\t"
    "v_mfma_f32_16x16x32_bf16 a[84:87], a[236:239], v[76:79], a[84:87]\n\t"
    "ds_read_b128 a[180:183], v197 offset:28672\n\t"
    "v_add_f32_e32 v216, v216, v42\n\t"
    "v_mfma_f32_16x16x32_bf16 a[100:103], a[240:243], v[72:75], 0\n\t"
    "ds_read_b128 a[176:179], v196 offset:28672\n\t"
    "v_add_f32_e32 v216, v216, v43\n\t"
    "v_mfma_f32_16x16x32_bf16 a[100:103], a[244:247], v[76:79], a[100:103]\n\t"
    "buffer_load_dwordx4 v212, s[12:15], s28 offen lds\n\t"
    "v_add_f32_e32 v216, v216, v44\n\t"
    "v_mfma_f32_16x16x32_bf16 a[116:119], a[248:251], v[72:75], 0\n\t"
    "v_exp_f32_e32 v48, v48\n\t"
    "v_mfma_f32_16x16x32_bf16 a[116:119], a[252:255], v[76:79], a[116:119]\n\t"
    "v_exp_f32_e32 v49, v49\n\t"
    "v_mfma_f32_16x16x32_bf16 v[180:183], v[192:195], v[72:75], 0\n\t"
    "v_cvt_pk_bf16_f32 v88, v48, v49\n\t"
    "s_waitcnt lgkmcnt(8)\n\t"
    "v_mfma_f32_16x16x32_bf16 v[0:3], a[128:131], v[96:99], v[160:163]\n\t"
    "v_exp_f32_e32 v50, v50\n\t"
    "v_mfma_f32_16x16x32_bf16 v[0:3], a[132:135], v[100:103], v[0:3]\n\t"
    "v_exp_f32_e32 v51, v51\n\t"
    "v_mfma_f32_16x16x32_bf16 v[0:3], a[136:139], v[104:107], v[0:3]\n\t"
    "v_exp_f32_e32 v52, v52\n\t"
    "v_mfma_f32_16x16x32_bf16 v[0:3], a[140:143], v[108:111], v[0:3]\n\t"
    "v_exp_f32_e32 v53, v53\n\t"
    "v_mfma_f32_16x16x32_bf16 v[4:7], a[156:159], v[108:111], v[160:163]\n\t"
    "buffer_load_dwordx4 v213, s[12:15], s28 offen offset:1024 lds\n\t"
    "v_cvt_pk_bf16_f32 v89, v50, v51\n\t"
    "v_mfma_f32_16x16x32_bf16 v[4:7], a[152:155], v[104:107], v[4:7]\n\t"
    "v_exp_f32_e32 v54, v54\n\t"
    "v_mfma_f32_16x16x32_bf16 v[4:7], a[148:151], v[100:103], v[4:7]\n\t"
    "v_exp_f32_e32 v55, v55\n\t"
    "v_mfma_f32_16x16x32_bf16 v[4:7], a[144:147], v[96:99], v[4:7]\n\t"
    "v_cvt_pk_bf16_f32 v90, v52, v53\n\t"
    "s_waitcnt lgkmcnt(0)\n\t"
    "v_mfma_f32_16x16x32_bf16 v[8:11], a[160:163], v[96:99], v[160:163]\n\t"
    "v_exp_f32_e32 v56, v56\n\t"
    "v_mfma_f32_16x16x32_bf16 v[8:11], a[164:167], v[100:103], v[8:11]\n\t"
    "v_exp_f32_e32 v57, v57\n\t"
    "v_mfma_f32_16x16x32_bf16 v[8:11], a[168:171], v[104:107], v[8:11]\n\t"
    "v_exp_f32_e32 v58, v58\n\t"
    "v_mfma_f32_16x16x32_bf16 v[8:11], a[172:175], v[108:111], v[8:11]\n\t"
    "v_exp_f32_e32 v59, v59\n\t"
    "v_mfma_f32_16x16x32_bf16 v[12:15], a[188:191], v[108:111], v[160:163]\n\t"
    "v_exp_f32_e32 v60, v60\n\t"
    "v_mfma_f32_16x16x32_bf16 v[12:15], a[184:187], v[104:107], v[12:15]\n\t"
    "v_exp_f32_e32 v61, v61\n\t"
    "v_mfma_f32_16x16x32_bf16 v[12:15], a[180:183], v[100:103], v[12:15]\n\t"
    "buffer_load_dwordx4 v212, s[12:15], s28 offen offset:2048 lds\n\t"
    "v_cvt_pk_bf16_f32 v91, v54, v55\n\t"
    "v_mfma_f32_16x16x32_bf16 v[12:15], a[176:179], v[96:99], v[12:15]\n\t"
    "v_exp_f32_e32 v62, v62\n\t"
    "v_mfma_f32_16x16x32_bf16 a[8:11], a[192:195], v[80:83], 0\n\t"
    "v_exp_f32_e32 v63, v63\n\t"
    "v_mfma_f32_16x16x32_bf16 a[8:11], a[196:199], v[84:87], a[8:11]\n\t"
    "v_exp_f32_e32 v0, v0\n\t"
    "v_mfma_f32_16x16x32_bf16 a[24:27], a[200:203], v[80:83], 0\n\t"
    "v_exp_f32_e32 v1, v1\n\t"
    "v_mfma_f32_16x16x32_bf16 a[24:27], a[204:207], v[84:87], a[24:27]\n\t"
    "v_exp_f32_e32 v2, v2\n\t"
    "v_mfma_f32_16x16x32_bf16 a[40:43], a[208:211], v[80:83], 0\n\t"
    "v_exp_f32_e32 v3, v3\n\t"
    "v_mfma_f32_16x16x32_bf16 a[40:43], a[212:215], v[84:87], a[40:43]\n\t"
    "v_exp_f32_e32 v4, v4\n\t"
    "v_mfma_f32_16x16x32_bf16 a[56:59], a[216:219], v[80:83], 0\n\t"
    "v_exp_f32_e32 v5, v5\n\t"
    "v_mfma_f32_16x16x32_bf16 a[56:59], a[220:223], v[84:87], a[56:59]\n\t"
    "v_exp_f32_e32 v6, v6\n\t"
    "v_mfma_f32_16x16x32_bf16 a[72:75], a[224:227], v[80:83], 0\n\t"
    "buffer_load_dwordx4 v213, s[12:15], s28 offen offset:3072 lds\n\t"
    "v_cvt_pk_bf16_f32 v92, v56, v57\n\t"
    "v_mfma_f32_16x16x32_bf16 a[72:75], a[228:231], v[84:87], a[72:75]\n\t"
    "v_exp_f32_e32 v7, v7\n\t"
    "v_mfma_f32_16x16x32_bf16 a[88:91], a[232:235], v[80:83], 0\n\t"
    "v_exp_f32_e32 v8, v8\n\t"
    "v_mfma_f32_16x16x32_bf16 a[88:91], a[236:239], v[84:87], a[88:91]\n\t"
    "v_exp_f32_e32 v9, v9\n\t"
    "v_mfma_f32_16x16x32_bf16 a[104:107], a[240:243], v[80:83], 0\n\t"
    "v_exp_f32_e32 v10, v10\n\t"
    "v_mfma_f32_16x16x32_bf16 a[104:107], a[244:247], v[84:87], a[104:107]\n\t"
    "v_exp_f32_e32 v11, v11\n\t"
    "v_mfma_f32_16x16x32_bf16 a[120:123], a[248:251], v[80:83], 0\n\t"
    "v_exp_f32_e32 v12, v12\n\t"
    "v_mfma_f32_16x16x32_bf16 a[120:123], a[252:255], v[84:87], a[120:123]\n\t"
    "v_exp_f32_e32 v13, v13\n\t"
    "v_mfma_f32_16x16x32_bf16 v[184:187], v[192:195], v[80:83], 0\n\t"
    "v_exp_f32_e32 v14, v14\n\t"
    "v_mfma_f32_16x16x32_bf16 v[16:19], a[128:131], v[112:115], v[164:167]\n\t"
    "v_exp_f32_e32 v15, v15\n\t"
    "v_mfma_f32_16x16x32_bf16 v[16:19], a[132:135], v[116:119], v[16:19]\n\t"
    "v_cvt_pk_bf16_f32 v93, v58, v59\n\t"
    "v_mfma_f32_16x16x32_bf16 v[16:19], a[136:139], v[120:123], v[16:19]\n\t"
    "v_cvt_pk_bf16_f32 v94, v60, v61\n\t"
    "v_mfma_f32_16x16x32_bf16 v[16:19], a[140:143], v[124:127], v[16:19]\n\t"
    "v_cvt_pk_bf16_f32 v95, v62, v63\n\t"
    "v_mfma_f32_16x16x32_bf16 v[20:23], a[156:159], v[124:127], v[164:167]\n\t"
    "s_add_u32 s28, s28, 16384\n\t"
    "v_add_f32_e32 v216, v216, v45\n\t"
    "v_mfma_f32_16x16x32_bf16 v[20:23], a[152:155], v[120:123], v[20:23]\n\t"
    "v_add_f32_e32 v216, v216, v46\n\t"
    "v_add_f32_e32 v216, v216, v47\n\t"
    "v_mfma_f32_16x16x32_bf16 v[20:23], a[148:151], v[116:119], v[20:23]\n\t"
    "v_cvt_pk_bf16_f32 v64, v0, v1\n\t"
    "v_cvt_pk_bf16_f32 v65, v2, v3\n\t"
    "v_mfma_f32_16x16x32_bf16 v[20:23], a[144:147], v[112:115], v[20:23]\n\t"
    "v_exp_f32_e32 v16, v16\n\t"
    "v_mfma_f32_16x16x32_bf16 v[24:27], a[160:163], v[112:115], v[164:167]\n\t"
    "v_exp_f32_e32 v17, v17\n\t"
    "v_mfma_f32_16x16x32_bf16 v[24:27], a[164:167], v[116:119], v[24:27]\n\t"
    "v_exp_f32_e32 v18, v18\n\t"
    "v_mfma_f32_16x16x32_bf16 v[24:27], a[168:171], v[120:123], v[24:27]\n\t"
    "v_exp_f32_e32 v19, v19\n\t"
    "v_mfma_f32_16x16x32_bf16 v[24:27], a[172:175], v[124:127], v[24:27]\n\t"
    "v_exp_f32_e32 v20, v20\n\t"
    "v_mfma_f32_16x16x32_bf16 v[28:31], a[188:191], v[124:127], v[164:167]\n\t"
    "v_exp_f32_e32 v21, v21\n\t"
    "v_mfma_f32_16x16x32_bf16 v[28:31], a[184:187], v[120:123], v[28:31]\n\t"
    "v_exp_f32_e32 v22, v22\n\t"
    "v_mfma_f32_16x16x32_bf16 v[28:31], a[180:183], v[116:119], v[28:31]\n\t"
    "v_exp_f32_e32 v23, v23\n\t"
    "v_mfma_f32_16x16x32_bf16 v[28:31], a[176:179], v[112:115], v[28:31]\n\t"
    "v_exp_f32_e32 v24, v24\n\t"
    "v_mfma_f32_16x16x32_bf16 a[12:15], a[192:195], v[88:91], 0\n\t"
    "s_waitcnt vmcnt(8)\n\t"
    "s_barrier\n\t"
    "v_mfma_f32_16x16x32_bf16 a[12:15], a[196:199], v[92:95], a[12:15]\n\t"
    "ds_read_b64_tr_b16 a[192:193], v200 offset:16384\n\t"
    "v_cvt_pk_bf16_f32 v66, v4, v5\n\t"
    "v_mfma_f32_16x16x32_bf16 a[28:31], a[200:203], v[88:91], 0\n\t"
    "ds_read_b64_tr_b16 a[194:195], v200 offset:20480\n\t"
    "v_cvt_pk_bf16_f32 v67, v6, v7\n\t"
    "v_mfma_f32_16x16x32_bf16 a[28:31], a[204:207], v[92:95], a[28:31]\n\t"
    "ds_read_b64_tr_b16 a[196:197], v200 offset:24576\n\t"
    "v_cvt_pk_bf16_f32 v68, v8, v9\n\t"
    "v_mfma_f32_16x16x32_bf16 a[44:47], a[208:211], v[88:91], 0\n\t"
    "ds_read_b64_tr_b16 a[198:199], v200 offset:28672\n\t"
    "v_cvt_pk_bf16_f32 v69, v10, v11\n\t"
    "v_mfma_f32_16x16x32_bf16 a[44:47], a[212:215], v[92:95], a[44:47]\n\t"
    "ds_read_b64_tr_b16 a[200:201], v201 offset:16384\n\t"
    "v_cvt_pk_bf16_f32 v70, v12, v13\n\t"
    "v_mfma_f32_16x16x32_bf16 a[60:63], a[216:219], v[88:91], 0\n\t"
    "ds_read_b64_tr_b16 a[202:203], v201 offset:20480\n\t"
    "v_cvt_pk_bf16_f32 v71, v14, v15\n\t"
    "v_mfma_f32_16x16x32_bf16 a[60:63], a[220:223], v[92:95], a[60:63]\n\t"
    "ds_read_b64_tr_b16 a[204:205], v201 offset:24576\n\t"
    "v_add_f32_e32 v217, v217, v56\n\t"
    "v_mfma_f32_16x16x32_bf16 a[76:79], a[224:227], v[88:91], 0\n\t"
    "ds_read_b64_tr_b16 a[206:207], v201 offset:28672\n\t"
    "v_add_f32_e32 v217, v217, v57\n\t"
    "v_mfma_f32_16x16x32_bf16 a[76:79], a[228:231], v[92:95], a[76:79]\n\t"
    "ds_read_b64_tr_b16 a[208:209], v202 offset:16384\n\t"
    "v_add_f32_e32 v217, v217, v58\n\t"
    "v_mfma_f32_16x16x32_bf16 a[92:95], a[232:235], v[88:91], 0\n\t"
    "ds_read_b64_tr_b16 a[210:211], v202 offset:20480\n\t"
    "v_add_f32_e32 v217, v217, v59\n\t"
    "v_mfma_f32_16x16x32_bf16 a[92:95], a[236:239], v[92:95], a[92:95]\n\t"
    "ds_read_b64_tr_b16 a[212:213], v202 offset:24576\n\t"
    "v_add_f32_e32 v217, v217, v60\n\t"
    "v_mfma_f32_16x16x32_bf16 a[108:111], a[240:243], v[88:91], 0\n\t"
    "ds_read_b64_tr_b16 a[214:215], v202 offset:28672\n\t"
    "v_add_f32_e32 v217, v217, v61\n\t"
    "v_mfma_f32_16x16x32_bf16 a[108:111], a[244:247], v[92:95], a[108:111]\n\t"
    "ds_read_b64_tr_b16 a[216:217], v203 offset:16384\n\t"
    "v_add_f32_e32 v217, v217, v62\n\t"
    "v_mfma_f32_16x16x32_bf16 a[124:127], a[248:251], v[88:91], 0\n\t"
    "ds_read_b64_tr_b16 a[218:219], v203 offset:20480\n\t"
    "v_add_f32_e32 v217, v217, v63\n\t"
    "v_mfma_f32_16x16x32_bf16 a[124:127], a[252:255], v[92:95], a[124:127]\n\t"
    "ds_read_b64_tr_b16 a[220:221], v203 offset:24576\n\t"
    "v_cvt_pk_bf16_f32 v72, v16, v17\n\t"
    "v_mfma_f32_16x16x32_bf16 v[188:191], v[192:195], v[88:91], 0\n\t"
    "ds_read_b64_tr_b16 a[222:223], v203 offset:28672\n\t"
    "v_cvt_pk_bf16_f32 v73, v18, v19\n\t"
    "v_mfma_f32_16x16x32_bf16 v[32:35], a[128:131], v[128:131], v[168:171]\n\t"
    "ds_read_b64_tr_b16 a[224:225], v204 offset:16384\n\t"
    "v_cvt_pk_bf16_f32 v74, v20, v21\n\t"
    "v_mfma_f32_16x16x32_bf16 v[32:35], a[132:135], v[132:135], v[32:35]\n\t"
    "ds_read_b64_tr_b16 a[226:227], v204 offset:20480\n\t"
    "v_cvt_pk_bf16_f32 v75, v22, v23\n\t"
    "v_mfma_f32_16x16x32_bf16 v[32:35], a[136:139], v[136:139], v[32:35]\n\t"
    "ds_read_b64_tr_b16 a[228:229], v204 offset:24576\n\t"
    "v_add_f32_e32 v214, v214, v8\n\t"
    "v_mfma_f32_16x16x32_bf16 v[32:35], a[140:143], v[140:143], v[32:35]\n\t"
    "ds_read_b64_tr_b16 a[230:231], v204 offset:28672\n\t"
    "v_add_f32_e32 v214, v214, v9\n\t"
    "v_mfma_f32_16x16x32_bf16 v[36:39], a[156:159], v[140:143], v[168:171]\n\t"
    "ds_read_b64_tr_b16 a[232:233], v205 offset:16384\n\t"
    "v_add_f32_e32 v214, v214, v10\n\t"
    "v_mfma_f32_16x16x32_bf16 v[36:39], a[152:155], v[136:139], v[36:39]\n\t"
    "ds_read_b64_tr_b16 a[234:235], v205 offset:20480\n\t"
    "v_add_f32_e32 v214, v214, v11\n\t"
    "v_mfma_f32_16x16x32_bf16 v[36:39], a[148:151], v[132:135], v[36:39]\n\t"
    "ds_read_b64_tr_b16 a[236:237], v205 offset:24576\n\t"
    "v_add_f32_e32 v214, v214, v12\n\t"
    "v_mfma_f32_16x16x32_bf16 v[36:39], a[144:147], v[128:131], v[36:39]\n\t"
    "ds_read_b64_tr_b16 a[238:239], v205 offset:28672\n\t"
    "v_add_f32_e32 v214, v214, v13\n\t"
    "v_mfma_f32_16x16x32_bf16 v[40:43], a[160:163], v[128:131], v[168:171]\n\t"
    "ds_read_b64_tr_b16 a[240:241], v206 offset:16384\n\t"
    "v_add_f32_e32 v214, v214, v14\n\t"
    "v_mfma_f32_16x16x32_bf16 v[40:43], a[164:167], v[132:135], v[40:43]\n\t"
    "ds_read_b64_tr_b16 a[242:243], v206 offset:20480\n\t"
    "v_add_f32_e32 v214, v214, v15\n\t"
    "v_mfma_f32_16x16x32_bf16 v[40:43], a[168:171], v[136:139], v[40:43]\n\t"
    "ds_read_b64_tr_b16 a[244:245], v206 offset:24576\n\t"
    "v_add_f32_e32 v215, v215, v24\n\t"
    "v_mfma_f32_16x16x32_bf16 v[40:43], a[172:175], v[140:143], v[40:43]\n\t"
    "ds_read_b64_tr_b16 a[246:247], v206 offset:28672\n\t"
    "v_mfma_f32_16x16x32_bf16 v[44:47], a[188:191], v[140:143], v[168:171]\n\t"
    "ds_read_b64_tr_b16 a[248:249], v207 offset:16384\n\t"
    "v_mfma_f32_16x16x32_bf16 v[44:47], a[184:187], v[136:139], v[44:47]\n\t"
    "ds_read_b64_tr_b16 a[250:251], v207 offset:20480\n\t"
    "v_mfma_f32_16x16x32_bf16 v[44:47], a[180:183], v[132:135], v[44:47]\n\t"
    "ds_read_b64_tr_b16 a[252:253], v207 offset:24576\n\t"
    "v_mfma_f32_16x16x32_bf16 v[44:47], a[176:179], v[128:131], v[44:47]\n\t"
    "ds_read_b64_tr_b16 a[254:255], v207 offset:28672\n\t"
    "s_waitcnt lgkmcnt(15)\n\t"
    "v_mfma_f32_16x16x32_bf16 a[0:3], a[192:195], v[64:67], a[0:3]\n\t"
    "v_exp_f32_e32 v25, v25\n\t"
    "v_mfma_f32_16x16x32_bf16 a[0:3], a[196:199], v[68:71], a[0:3]\n\t"
    "v_exp_f32_e32 v26, v26\n\t"
    "v_mfma_f32_16x16x32_bf16 a[16:19], a[200:203], v[64:67], a[16:19]\n\t"
    "v_exp_f32_e32 v27, v27\n\t"
    "v_mfma_f32_16x16x32_bf16 a[16:19], a[204:207], v[68:71], a[16:19]\n\t"
    "v_exp_f32_e32 v28, v28\n\t"
    "v_mfma_f32_16x16x32_bf16 a[32:35], a[208:211], v[64:67], a[32:35]\n\t"
    "v_exp_f32_e32 v29, v29\n\t"
    "v_mfma_f32_16x16x32_bf16 a[32:35], a[212:215], v[68:71], a[32:35]\n\t"
    "v_exp_f32_e32 v30, v30\n\t"
    "v_mfma_f32_16x16x32_bf16 a[48:51], a[216:219], v[64:67], a[48:51]\n\t"
    "v_exp_f32_e32 v31, v31\n\t"
    "v_mfma_f32_16x16x32_bf16 a[48:51], a[220:223], v[68:71], a[48:51]\n\t"
    "s_mov_b32 m0, s32\n\t"
    "s_waitcnt lgkmcnt(0)\n\t"
    "v_mfma_f32_16x16x32_bf16 a[64:67], a[224:227], v[64:67], a[64:67]\n\t"
    "buffer_load_dwordx4 v208, s[8:11], s28 offen lds\n\t"
    "v_cvt_pk_bf16_f32 v76, v24, v25\n\t"
    "v_mfma_f32_16x16x32_bf16 a[64:67], a[228:231], v[68:71], a[64:67]\n\t"
    "v_exp_f32_e32 v32, v32\n\t"
    "v_mfma_f32_16x16x32_bf16 a[80:83], a[232:235], v[64:67], a[80:83]\n\t"
    "v_exp_f32_e32 v33, v33\n\t"
    "v_mfma_f32_16x16x32_bf16 a[80:83], a[236:239], v[68:71], a[80:83]\n\t"
    "v_exp_f32_e32 v34, v34\n\t"
    "v_mfma_f32_16x16x32_bf16 a[96:99], a[240:243], v[64:67], a[96:99]\n\t"
    "v_exp_f32_e32 v35, v35\n\t"
    "v_mfma_f32_16x16x32_bf16 a[96:99], a[244:247], v[68:71], a[96:99]\n\t"
    "v_exp_f32_e32 v36, v36\n\t"
    "v_mfma_f32_16x16x32_bf16 a[112:115], a[248:251], v[64:67], a[112:115]\n\t"
    "buffer_load_dwordx4 v209, s[8:11], s28 offen offset:1024 lds\n\t"
    "v_cvt_pk_bf16_f32 v77, v26, v27\n\t"
    "v_mfma_f32_16x16x32_bf16 a[112:115], a[252:255], v[68:71], a[112:115]\n\t"
    "v_exp_f32_e32 v37, v37\n\t"
    "v_mfma_f32_16x16x32_bf16 v[176:179], v[192:195], v[64:67], v[176:179]\n\t"
    "v_exp_f32_e32 v38, v38\n\t"
    "v_mfma_f32_16x16x32_bf16 v[48:51], a[128:131], v[144:147], v[172:175]\n\t"
    "v_exp_f32_e32 v39, v39\n\t"
    "v_mfma_f32_16x16x32_bf16 v[48:51], a[132:135], v[148:151], v[48:51]\n\t"
    "v_exp_f32_e32 v40, v40\n\t"
    "v_mfma_f32_16x16x32_bf16 v[48:51], a[136:139], v[152:155], v[48:51]\n\t"
    "v_exp_f32_e32 v41, v41\n\t"
    "v_mfma_f32_16x16x32_bf16 v[48:51], a[140:143], v[156:159], v[48:51]\n\t"
    "v_exp_f32_e32 v42, v42\n\t"
    "v_mfma_f32_16x16x32_bf16 v[52:55], a[156:159], v[156:159], v[172:175]\n\t"
    "v_exp_f32_e32 v43, v43\n\t"
    "v_mfma_f32_16x16x32_bf16 v[52:55], a[152:155], v[152:155], v[52:55]\n\t"
    "v_exp_f32_e32 v44, v44\n\t"
    "v_mfma_f32_16x16x32_bf16 v[52:55], a[148:151], v[148:151], v[52:55]\n\t"
    "v_exp_f32_e32 v45, v45\n\t"
    "v_mfma_f32_16x16x32_bf16 v[52:55], a[144:147], v[144:147], v[52:55]\n\t"
    "v_exp_f32_e32 v46, v46\n\t"
    "v_mfma_f32_16x16x32_bf16 v[56:59], a[160:163], v[144:147], v[172:175]\n\t"
    "v_exp_f32_e32 v47, v47\n\t"
    "v_mfma_f32_16x16x32_bf16 v[56:59], a[164:167], v[148:151], v[56:59]\n\t"
    "v_cvt_pk_bf16_f32 v78, v28, v29\n\t"
    "v_cvt_pk_bf16_f32 v79, v30, v31\n\t"
    "v_mfma_f32_16x16x32_bf16 v[56:59], a[168:171], v[152:155], v[56:59]\n\t"
    "buffer_load_dwordx4 v210, s[8:11], s28 offen offset:2048 lds\n\t"
    "v_cvt_pk_bf16_f32 v80, v32, v33\n\t"
    "v_mfma_f32_16x16x32_bf16 v[56:59], a[172:175], v[156:159], v[56:59]\n\t"
    "ds_read_b128 a[128:131], v196 offset:32768\n\t"
    "v_cvt_pk_bf16_f32 v81, v34, v35\n\t"
    "v_mfma_f32_16x16x32_bf16 v[60:63], a[188:191], v[156:159], v[172:175]\n\t"
    "ds_read_b128 a[132:135], v197 offset:32768\n\t"
    "v_cvt_pk_bf16_f32 v82, v36, v37\n\t"
    "v_mfma_f32_16x16x32_bf16 v[60:63], a[184:187], v[152:155], v[60:63]\n\t"
    "ds_read_b128 a[136:139], v198 offset:32768\n\t"
    "v_cvt_pk_bf16_f32 v83, v38, v39\n\t"
    "v_mfma_f32_16x16x32_bf16 v[60:63], a[180:183], v[148:151], v[60:63]\n\t"
    "ds_read_b128 a[140:143], v199 offset:32768\n\t"
    "v_cvt_pk_bf16_f32 v84, v40, v41\n\t"
    "v_mfma_f32_16x16x32_bf16 v[60:63], a[176:179], v[144:147], v[60:63]\n\t"
    "ds_read_b128 a[156:159], v199 offset:36864\n\t"
    "v_cvt_pk_bf16_f32 v85, v42, v43\n\t"
    "v_mfma_f32_16x16x32_bf16 a[4:7], a[192:195], v[72:75], a[4:7]\n\t"
    "ds_read_b128 a[152:155], v198 offset:36864\n\t"
    "v_cvt_pk_bf16_f32 v86, v44, v45\n\t"
    "v_mfma_f32_16x16x32_bf16 a[4:7], a[196:199], v[76:79], a[4:7]\n\t"
    "buffer_load_dwordx4 v211, s[8:11], s28 offen offset:3072 lds\n\t"
    "v_cvt_pk_bf16_f32 v87, v46, v47\n\t"
    "v_mfma_f32_16x16x32_bf16 a[20:23], a[200:203], v[72:75], a[20:23]\n\t"
    "ds_read_b128 a[148:151], v197 offset:36864\n\t"
    "v_add_f32_e32 v215, v215, v25\n\t"
    "v_mfma_f32_16x16x32_bf16 a[20:23], a[204:207], v[76:79], a[20:23]\n\t"
    "ds_read_b128 a[144:147], v196 offset:36864\n\t"
    "v_add_f32_e32 v215, v215, v26\n\t"
    "v_mfma_f32_16x16x32_bf16 a[36:39], a[208:211], v[72:75], a[36:39]\n\t"
    "ds_read_b128 a[160:163], v196 offset:40960\n\t"
    "v_add_f32_e32 v215, v215, v27\n\t"
    "v_mfma_f32_16x16x32_bf16 a[36:39], a[212:215], v[76:79], a[36:39]\n\t"
    "ds_read_b128 a[164:167], v197 offset:40960\n\t"
    "v_add_f32_e32 v215, v215, v28\n\t"
    "v_mfma_f32_16x16x32_bf16 a[52:55], a[216:219], v[72:75], a[52:55]\n\t"
    "ds_read_b128 a[168:171], v198 offset:40960\n\t"
    "v_add_f32_e32 v215, v215, v29\n\t"
    "v_mfma_f32_16x16x32_bf16 a[52:55], a[220:223], v[76:79], a[52:55]\n\t"
    "ds_read_b128 a[172:175], v199 offset:40960\n\t"
    "v_add_f32_e32 v215, v215, v30\n\t"
    "v_mfma_f32_16x16x32_bf16 a[68:71], a[224:227], v[72:75], a[68:71]\n\t"
    "ds_read_b128 a[188:191], v199 offset:45056\n\t"
    "v_add_f32_e32 v215, v215, v31\n\t"
    "v_mfma_f32_16x16x32_bf16 a[68:71], a[228:231], v[76:79], a[68:71]\n\t"
    "s_mov_b32 m0, s39\n\t"
    "v_add_f32_e32 v216, v216, v40\n\t"
    "v_mfma_f32_16x16x32_bf16 a[84:87], a[232:235], v[72:75], a[84:87]\n\t"
    "ds_read_b128 a[184:187], v198 offset:45056\n\t"
    "v_add_f32_e32 v216, v216, v41\n\t"
    "v_mfma_f32_16x16x32_bf16 a[84:87], a[236:239], v[76:79], a[84:87]\n\t"
    "ds_read_b128 a[180:183], v197 offset:45056\n\t"
    "v_add_f32_e32 v216, v216, v42\n\t"
    "v_mfma_f32_16x16x32_bf16 a[100:103], a[240:243], v[72:75], a[100:103]\n\t"
    "ds_read_b128 a[176:179], v196 offset:45056\n\t"
    "v_add_f32_e32 v216, v216, v43\n\t"
    "v_mfma_f32_16x16x32_bf16 a[100:103], a[244:247], v[76:79], a[100:103]\n\t"
    "buffer_load_dwordx4 v212, s[12:15], s28 offen lds\n\t"
    "v_add_f32_e32 v216, v216, v44\n\t"
    "v_mfma_f32_16x16x32_bf16 a[116:119], a[248:251], v[72:75], a[116:119]\n\t"
    "v_exp_f32_e32 v48, v48\n\t"
    "v_mfma_f32_16x16x32_bf16 a[116:119], a[252:255], v[76:79], a[116:119]\n\t"
    "v_exp_f32_e32 v49, v49\n\t"
    "v_mfma_f32_16x16x32_bf16 v[180:183], v[192:195], v[72:75], v[180:183]\n\t"
    "v_cvt_pk_bf16_f32 v88, v48, v49\n\t"
    "s_mov_b32 s52, 7\n\t"
    ".p2align 3\n\t"
    "Lattn_loop_%=:\n\t"
    "s_waitcnt lgkmcnt(8)\n\t"
    "v_mfma_f32_16x16x32_bf16 v[0:3], a[128:131], v[96:99], v[160:163]\n\t"
    "v_exp_f32_e32 v50, v50\n\t"
    "v_mfma_f32_16x16x32_bf16 v[0:3], a[132:135], v[100:103], v[0:3]\n\t"
    "v_exp_f32_e32 v51, v51\n\t"
    "v_mfma_f32_16x16x32_bf16 v[0:3], a[136:139], v[104:107], v[0:3]\n\t"
    "v_exp_f32_e32 v52, v52\n\t"
    "v_mfma_f32_16x16x32_bf16 v[0:3], a[140:143], v[108:111], v[0:3]\n\t"
    "v_exp_f32_e32 v53, v53\n\t"
    "v_mfma_f32_16x16x32_bf16 v[4:7], a[156:159], v[108:111], v[160:163]\n\t"
    "buffer_load_dwordx4 v213, s[12:15], s28 offen offset:1024 lds\n\t"
    "v_cvt_pk_bf16_f32 v89, v50, v51\n\t"
    "v_mfma_f32_16x16x32_bf16 v[4:7], a[152:155], v[104:107], v[4:7]\n\t"
    "v_exp_f32_e32 v54, v54\n\t"
    "v_mfma_f32_16x16x32_bf16 v[4:7], a[148:151], v[100:103], v[4:7]\n\t"
    "v_exp_f32_e32 v55, v55\n\t"
    "v_mfma_f32_16x16x32_bf16 v[4:7], a[144:147], v[96:99], v[4:7]\n\t"
    "v_cvt_pk_bf16_f32 v90, v52, v53\n\t"
    "s_waitcnt lgkmcnt(0)\n\t"
    "v_mfma_f32_16x16x32_bf16 v[8:11], a[160:163], v[96:99], v[160:163]\n\t"
    "v_exp_f32_e32 v56, v56\n\t"
    "v_mfma_f32_16x16x32_bf16 v[8:11], a[164:167], v[100:103], v[8:11]\n\t"
    "v_exp_f32_e32 v57, v57\n\t"
    "v_mfma_f32_16x16x32_bf16 v[8:11], a[168:171], v[104:107], v[8:11]\n\t"
    "v_exp_f32_e32 v58, v58\n\t"
    "v_mfma_f32_16x16x32_bf16 v[8:11], a[172:175], v[108:111], v[8:11]\n\t"
    "v_exp_f32_e32 v59, v59\n\t"
    "v_mfma_f32_16x16x32_bf16 v[12:15], a[188:191], v[108:111], v[160:163]\n\t"
    "v_exp_f32_e32 v60, v60\n\t"
    "v_mfma_f32_16x16x32_bf16 v[12:15], a[184:187], v[104:107], v[12:15]\n\t"
    "v_exp_f32_e32 v61, v61\n\t"
    "v_mfma_f32_16x16x32_bf16 v[12:15], a[180:183], v[100:103], v[12:15]\n\t"
    "buffer_load_dwordx4 v212, s[12:15], s28 offen offset:2048 lds\n\t"
    "v_cvt_pk_bf16_f32 v91, v54, v55\n\t"
    "v_mfma_f32_16x16x32_bf16 v[12:15], a[176:179], v[96:99], v[12:15]\n\t"
    "v_exp_f32_e32 v62, v62\n\t"
    "v_mfma_f32_16x16x32_bf16 a[8:11], a[192:195], v[80:83], a[8:11]\n\t"
    "v_exp_f32_e32 v63, v63\n\t"
    "v_mfma_f32_16x16x32_bf16 a[8:11], a[196:199], v[84:87], a[8:11]\n\t"
    "v_exp_f32_e32 v0, v0\n\t"
    "v_mfma_f32_16x16x32_bf16 a[24:27], a[200:203], v[80:83], a[24:27]\n\t"
    "v_exp_f32_e32 v1, v1\n\t"
    "v_mfma_f32_16x16x32_bf16 a[24:27], a[204:207], v[84:87], a[24:27]\n\t"
    "v_exp_f32_e32 v2, v2\n\t"
    "v_mfma_f32_16x16x32_bf16 a[40:43], a[208:211], v[80:83], a[40:43]\n\t"
    "v_exp_f32_e32 v3, v3\n\t"
    "v_mfma_f32_16x16x32_bf16 a[40:43], a[212:215], v[84:87], a[40:43]\n\t"
    "v_exp_f32_e32 v4, v4\n\t"
    "v_mfma_f32_16x16x32_bf16 a[56:59], a[216:219], v[80:83], a[56:59]\n\t"
    "v_exp_f32_e32 v5, v5\n\t"
    "v_mfma_f32_16x16x32_bf16 a[56:59], a[220:223], v[84:87], a[56:59]\n\t"
    "v_exp_f32_e32 v6, v6\n\t"
    "v_mfma_f32_16x16x32_bf16 a[72:75], a[224:227], v[80:83], a[72:75]\n\t"
    "buffer_load_dwordx4 v213, s[12:15], s28 offen offset:3072 lds\n\t"
    "v_cvt_pk_bf16_f32 v92, v56, v57\n\t"
    "v_mfma_f32_16x16x32_bf16 a[72:75], a[228:231], v[84:87], a[72:75]\n\t"
    "v_exp_f32_e32 v7, v7\n\t"
    "v_mfma_f32_16x16x32_bf16 a[88:91], a[232:235], v[80:83], a[88:91]\n\t"
    "v_exp_f32_e32 v8, v8\n\t"
    "v_mfma_f32_16x16x32_bf16 a[88:91], a[236:239], v[84:87], a[88:91]\n\t"
    "v_exp_f32_e32 v9, v9\n\t"
    "v_mfma_f32_16x16x32_bf16 a[104:107], a[240:243], v[80:83], a[104:107]\n\t"
    "v_exp_f32_e32 v10, v10\n\t"
    "v_mfma_f32_16x16x32_bf16 a[104:107], a[244:247], v[84:87], a[104:107]\n\t"
    "v_exp_f32_e32 v11, v11\n\t"
    "v_mfma_f32_16x16x32_bf16 a[120:123], a[248:251], v[80:83], a[120:123]\n\t"
    "v_exp_f32_e32 v12, v12\n\t"
    "v_mfma_f32_16x16x32_bf16 a[120:123], a[252:255], v[84:87], a[120:123]\n\t"
    "v_exp_f32_e32 v13, v13\n\t"
    "v_mfma_f32_16x16x32_bf16 v[184:187], v[192:195], v[80:83], v[184:187]\n\t"
    "v_exp_f32_e32 v14, v14\n\t"
    "v_mfma_f32_16x16x32_bf16 v[16:19], a[128:131], v[112:115], v[164:167]\n\t"
    "v_exp_f32_e32 v15, v15\n\t"
    "v_mfma_f32_16x16x32_bf16 v[16:19], a[132:135], v[116:119], v[16:19]\n\t"
    "v_cvt_pk_bf16_f32 v93, v58, v59\n\t"
    "v_mfma_f32_16x16x32_bf16 v[16:19], a[136:139], v[120:123], v[16:19]\n\t"
    "v_cvt_pk_bf16_f32 v94, v60, v61\n\t"
    "v_mfma_f32_16x16x32_bf16 v[16:19], a[140:143], v[124:127], v[16:19]\n\t"
    "v_cvt_pk_bf16_f32 v95, v62, v63\n\t"
    "v_mfma_f32_16x16x32_bf16 v[20:23], a[156:159], v[124:127], v[164:167]\n\t"
    "s_add_u32 s28, s28, 16384\n\t"
    "v_add_f32_e32 v216, v216, v45\n\t"
    "v_mfma_f32_16x16x32_bf16 v[20:23], a[152:155], v[120:123], v[20:23]\n\t"
    "v_add_f32_e32 v216, v216, v46\n\t"
    "v_add_f32_e32 v216, v216, v47\n\t"
    "v_mfma_f32_16x16x32_bf16 v[20:23], a[148:151], v[116:119], v[20:23]\n\t"
    "v_cvt_pk_bf16_f32 v64, v0, v1\n\t"
    "v_cvt_pk_bf16_f32 v65, v2, v3\n\t"
    "v_mfma_f32_16x16x32_bf16 v[20:23], a[144:147], v[112:115], v[20:23]\n\t"
    "v_exp_f32_e32 v16, v16\n\t"
    "v_mfma_f32_16x16x32_bf16 v[24:27], a[160:163], v[112:115], v[164:167]\n\t"
    "v_exp_f32_e32 v17, v17\n\t"
    "v_mfma_f32_16x16x32_bf16 v[24:27], a[164:167], v[116:119], v[24:27]\n\t"
    "v_exp_f32_e32 v18, v18\n\t"
    "v_mfma_f32_16x16x32_bf16 v[24:27], a[168:171], v[120:123], v[24:27]\n\t"
    "v_exp_f32_e32 v19, v19\n\t"
    "v_mfma_f32_16x16x32_bf16 v[24:27], a[172:175], v[124:127], v[24:27]\n\t"
    "v_exp_f32_e32 v20, v20\n\t"
    "v_mfma_f32_16x16x32_bf16 v[28:31], a[188:191], v[124:127], v[164:167]\n\t"
    "v_exp_f32_e32 v21, v21\n\t"
    "v_mfma_f32_16x16x32_bf16 v[28:31], a[184:187], v[120:123], v[28:31]\n\t"
    "v_exp_f32_e32 v22, v22\n\t"
    "v_mfma_f32_16x16x32_bf16 v[28:31], a[180:183], v[116:119], v[28:31]\n\t"
    "v_exp_f32_e32 v23, v23\n\t"
    "v_mfma_f32_16x16x32_bf16 v[28:31], a[176:179], v[112:115], v[28:31]\n\t"
    "v_exp_f32_e32 v24, v24\n\t"
    "v_mfma_f32_16x16x32_bf16 a[12:15], a[192:195], v[88:91], a[12:15]\n\t"
    "s_waitcnt vmcnt(8)\n\t"
    "s_barrier\n\t"
    "v_mfma_f32_16x16x32_bf16 a[12:15], a[196:199], v[92:95], a[12:15]\n\t"
    "ds_read_b64_tr_b16 a[192:193], v200 offset:32768\n\t"
    "v_cvt_pk_bf16_f32 v66, v4, v5\n\t"
    "v_mfma_f32_16x16x32_bf16 a[28:31], a[200:203], v[88:91], a[28:31]\n\t"
    "ds_read_b64_tr_b16 a[194:195], v200 offset:36864\n\t"
    "v_cvt_pk_bf16_f32 v67, v6, v7\n\t"
    "v_mfma_f32_16x16x32_bf16 a[28:31], a[204:207], v[92:95], a[28:31]\n\t"
    "ds_read_b64_tr_b16 a[196:197], v200 offset:40960\n\t"
    "v_cvt_pk_bf16_f32 v68, v8, v9\n\t"
    "v_mfma_f32_16x16x32_bf16 a[44:47], a[208:211], v[88:91], a[44:47]\n\t"
    "ds_read_b64_tr_b16 a[198:199], v200 offset:45056\n\t"
    "v_cvt_pk_bf16_f32 v69, v10, v11\n\t"
    "v_mfma_f32_16x16x32_bf16 a[44:47], a[212:215], v[92:95], a[44:47]\n\t"
    "ds_read_b64_tr_b16 a[200:201], v201 offset:32768\n\t"
    "v_cvt_pk_bf16_f32 v70, v12, v13\n\t"
    "v_mfma_f32_16x16x32_bf16 a[60:63], a[216:219], v[88:91], a[60:63]\n\t"
    "ds_read_b64_tr_b16 a[202:203], v201 offset:36864\n\t"
    "v_cvt_pk_bf16_f32 v71, v14, v15\n\t"
    "v_mfma_f32_16x16x32_bf16 a[60:63], a[220:223], v[92:95], a[60:63]\n\t"
    "ds_read_b64_tr_b16 a[204:205], v201 offset:40960\n\t"
    "v_add_f32_e32 v217, v217, v56\n\t"
    "v_mfma_f32_16x16x32_bf16 a[76:79], a[224:227], v[88:91], a[76:79]\n\t"
    "ds_read_b64_tr_b16 a[206:207], v201 offset:45056\n\t"
    "v_add_f32_e32 v217, v217, v57\n\t"
    "v_mfma_f32_16x16x32_bf16 a[76:79], a[228:231], v[92:95], a[76:79]\n\t"
    "ds_read_b64_tr_b16 a[208:209], v202 offset:32768\n\t"
    "v_add_f32_e32 v217, v217, v58\n\t"
    "v_mfma_f32_16x16x32_bf16 a[92:95], a[232:235], v[88:91], a[92:95]\n\t"
    "ds_read_b64_tr_b16 a[210:211], v202 offset:36864\n\t"
    "v_add_f32_e32 v217, v217, v59\n\t"
    "v_mfma_f32_16x16x32_bf16 a[92:95], a[236:239], v[92:95], a[92:95]\n\t"
    "ds_read_b64_tr_b16 a[212:213], v202 offset:40960\n\t"
    "v_add_f32_e32 v217, v217, v60\n\t"
    "v_mfma_f32_16x16x32_bf16 a[108:111], a[240:243], v[88:91], a[108:111]\n\t"
    "ds_read_b64_tr_b16 a[214:215], v202 offset:45056\n\t"
    "v_add_f32_e32 v217, v217, v61\n\t"
    "v_mfma_f32_16x16x32_bf16 a[108:111], a[244:247], v[92:95], a[108:111]\n\t"
    "ds_read_b64_tr_b16 a[216:217], v203 offset:32768\n\t"
    "v_add_f32_e32 v217, v217, v62\n\t"
    "v_mfma_f32_16x16x32_bf16 a[124:127], a[248:251], v[88:91], a[124:127]\n\t"
    "ds_read_b64_tr_b16 a[218:219], v203 offset:36864\n\t"
    "v_add_f32_e32 v217, v217, v63\n\t"
    "v_mfma_f32_16x16x32_bf16 a[124:127], a[252:255], v[92:95], a[124:127]\n\t"
    "ds_read_b64_tr_b16 a[220:221], v203 offset:40960\n\t"
    "v_cvt_pk_bf16_f32 v72, v16, v17\n\t"
    "v_mfma_f32_16x16x32_bf16 v[188:191], v[192:195], v[88:91], v[188:191]\n\t"
    "ds_read_b64_tr_b16 a[222:223], v203 offset:45056\n\t"
    "v_cvt_pk_bf16_f32 v73, v18, v19\n\t"
    "v_mfma_f32_16x16x32_bf16 v[32:35], a[128:131], v[128:131], v[168:171]\n\t"
    "ds_read_b64_tr_b16 a[224:225], v204 offset:32768\n\t"
    "v_cvt_pk_bf16_f32 v74, v20, v21\n\t"
    "v_mfma_f32_16x16x32_bf16 v[32:35], a[132:135], v[132:135], v[32:35]\n\t"
    "ds_read_b64_tr_b16 a[226:227], v204 offset:36864\n\t"
    "v_cvt_pk_bf16_f32 v75, v22, v23\n\t"
    "v_mfma_f32_16x16x32_bf16 v[32:35], a[136:139], v[136:139], v[32:35]\n\t"
    "ds_read_b64_tr_b16 a[228:229], v204 offset:40960\n\t"
    "v_add_f32_e32 v214, v214, v8\n\t"
    "v_mfma_f32_16x16x32_bf16 v[32:35], a[140:143], v[140:143], v[32:35]\n\t"
    "ds_read_b64_tr_b16 a[230:231], v204 offset:45056\n\t"
    "v_add_f32_e32 v214, v214, v9\n\t"
    "v_mfma_f32_16x16x32_bf16 v[36:39], a[156:159], v[140:143], v[168:171]\n\t"
    "ds_read_b64_tr_b16 a[232:233], v205 offset:32768\n\t"
    "v_add_f32_e32 v214, v214, v10\n\t"
    "v_mfma_f32_16x16x32_bf16 v[36:39], a[152:155], v[136:139], v[36:39]\n\t"
    "ds_read_b64_tr_b16 a[234:235], v205 offset:36864\n\t"
    "v_add_f32_e32 v214, v214, v11\n\t"
    "v_mfma_f32_16x16x32_bf16 v[36:39], a[148:151], v[132:135], v[36:39]\n\t"
    "ds_read_b64_tr_b16 a[236:237], v205 offset:40960\n\t"
    "v_add_f32_e32 v214, v214, v12\n\t"
    "v_mfma_f32_16x16x32_bf16 v[36:39], a[144:147], v[128:131], v[36:39]\n\t"
    "ds_read_b64_tr_b16 a[238:239], v205 offset:45056\n\t"
    "v_add_f32_e32 v214, v214, v13\n\t"
    "v_mfma_f32_16x16x32_bf16 v[40:43], a[160:163], v[128:131], v[168:171]\n\t"
    "ds_read_b64_tr_b16 a[240:241], v206 offset:32768\n\t"
    "v_add_f32_e32 v214, v214, v14\n\t"
    "v_mfma_f32_16x16x32_bf16 v[40:43], a[164:167], v[132:135], v[40:43]\n\t"
    "ds_read_b64_tr_b16 a[242:243], v206 offset:36864\n\t"
    "v_add_f32_e32 v214, v214, v15\n\t"
    "v_mfma_f32_16x16x32_bf16 v[40:43], a[168:171], v[136:139], v[40:43]\n\t"
    "ds_read_b64_tr_b16 a[244:245], v206 offset:40960\n\t"
    "v_add_f32_e32 v215, v215, v24\n\t"
    "v_mfma_f32_16x16x32_bf16 v[40:43], a[172:175], v[140:143], v[40:43]\n\t"
    "ds_read_b64_tr_b16 a[246:247], v206 offset:45056\n\t"
    "v_mfma_f32_16x16x32_bf16 v[44:47], a[188:191], v[140:143], v[168:171]\n\t"
    "ds_read_b64_tr_b16 a[248:249], v207 offset:32768\n\t"
    "v_mfma_f32_16x16x32_bf16 v[44:47], a[184:187], v[136:139], v[44:47]\n\t"
    "ds_read_b64_tr_b16 a[250:251], v207 offset:36864\n\t"
    "v_mfma_f32_16x16x32_bf16 v[44:47], a[180:183], v[132:135], v[44:47]\n\t"
    "ds_read_b64_tr_b16 a[252:253], v207 offset:40960\n\t"
    "v_mfma_f32_16x16x32_bf16 v[44:47], a[176:179], v[128:131], v[44:47]\n\t"
    "ds_read_b64_tr_b16 a[254:255], v207 offset:45056\n\t"
    "s_waitcnt lgkmcnt(15)\n\t"
    "v_mfma_f32_16x16x32_bf16 a[0:3], a[192:195], v[64:67], a[0:3]\n\t"
    "v_exp_f32_e32 v25, v25\n\t"
    "v_mfma_f32_16x16x32_bf16 a[0:3], a[196:199], v[68:71], a[0:3]\n\t"
    "v_exp_f32_e32 v26, v26\n\t"
    "v_mfma_f32_16x16x32_bf16 a[16:19], a[200:203], v[64:67], a[16:19]\n\t"
    "v_exp_f32_e32 v27, v27\n\t"
    "v_mfma_f32_16x16x32_bf16 a[16:19], a[204:207], v[68:71], a[16:19]\n\t"
    "v_exp_f32_e32 v28, v28\n\t"
    "v_mfma_f32_16x16x32_bf16 a[32:35], a[208:211], v[64:67], a[32:35]\n\t"
    "v_exp_f32_e32 v29, v29\n\t"
    "v_mfma_f32_16x16x32_bf16 a[32:35], a[212:215], v[68:71], a[32:35]\n\t"
    "v_exp_f32_e32 v30, v30\n\t"
    "v_mfma_f32_16x16x32_bf16 a[48:51], a[216:219], v[64:67], a[48:51]\n\t"
    "v_exp_f32_e32 v31, v31\n\t"
    "v_mfma_f32_16x16x32_bf16 a[48:51], a[220:223], v[68:71], a[48:51]\n\t"
    "s_mov_b32 m0, s33\n\t"
    "s_waitcnt lgkmcnt(0)\n\t"
    "v_mfma_f32_16x16x32_bf16 a[64:67], a[224:227], v[64:67], a[64:67]\n\t"
    "buffer_load_dwordx4 v208, s[8:11], s28 offen lds\n\t"
    "v_cvt_pk_bf16_f32 v76, v24, v25\n\t"
    "v_mfma_f32_16x16x32_bf16 a[64:67], a[228:231], v[68:71], a[64:67]\n\t"
    "v_exp_f32_e32 v32, v32\n\t"
    "v_mfma_f32_16x16x32_bf16 a[80:83], a[232:235], v[64:67], a[80:83]\n\t"
    "v_exp_f32_e32 v33, v33\n\t"
    "v_mfma_f32_16x16x32_bf16 a[80:83], a[236:239], v[68:71], a[80:83]\n\t"
    "v_exp_f32_e32 v34, v34\n\t"
    "v_mfma_f32_16x16x32_bf16 a[96:99], a[240:243], v[64:67], a[96:99]\n\t"
    "v_exp_f32_e32 v35, v35\n\t"
    "v_mfma_f32_16x16x32_bf16 a[96:99], a[244:247], v[68:71], a[96:99]\n\t"
    "v_exp_f32_e32 v36, v36\n\t"
    "v_mfma_f32_16x16x32_bf16 a[112:115], a[248:251], v[64:67], a[112:115]\n\t"
    "buffer_load_dwordx4 v209, s[8:11], s28 offen offset:1024 lds\n\t"
    "v_cvt_pk_bf16_f32 v77, v26, v27\n\t"
    "v_mfma_f32_16x16x32_bf16 a[112:115], a[252:255], v[68:71], a[112:115]\n\t"
    "v_exp_f32_e32 v37, v37\n\t"
    "v_mfma_f32_16x16x32_bf16 v[176:179], v[192:195], v[64:67], v[176:179]\n\t"
    "v_exp_f32_e32 v38, v38\n\t"
    "v_mfma_f32_16x16x32_bf16 v[48:51], a[128:131], v[144:147], v[172:175]\n\t"
    "v_exp_f32_e32 v39, v39\n\t"
    "v_mfma_f32_16x16x32_bf16 v[48:51], a[132:135], v[148:151], v[48:51]\n\t"
    "v_exp_f32_e32 v40, v40\n\t"
    "v_mfma_f32_16x16x32_bf16 v[48:51], a[136:139], v[152:155], v[48:51]\n\t"
    "v_exp_f32_e32 v41, v41\n\t"
    "v_mfma_f32_16x16x32_bf16 v[48:51], a[140:143], v[156:159], v[48:51]\n\t"
    "v_exp_f32_e32 v42, v42\n\t"
    "v_mfma_f32_16x16x32_bf16 v[52:55], a[156:159], v[156:159], v[172:175]\n\t"
    "v_exp_f32_e32 v43, v43\n\t"
    "v_mfma_f32_16x16x32_bf16 v[52:55], a[152:155], v[152:155], v[52:55]\n\t"
    "v_exp_f32_e32 v44, v44\n\t"
    "v_mfma_f32_16x16x32_bf16 v[52:55], a[148:151], v[148:151], v[52:55]\n\t"
    "v_exp_f32_e32 v45, v45\n\t"
    "v_mfma_f32_16x16x32_bf16 v[52:55], a[144:147], v[144:147], v[52:55]\n\t"
    "v_exp_f32_e32 v46, v46\n\t"
    "v_mfma_f32_16x16x32_bf16 v[56:59], a[160:163], v[144:147], v[172:175]\n\t"
    "v_exp_f32_e32 v47, v47\n\t"
    "v_mfma_f32_16x16x32_bf16 v[56:59], a[164:167], v[148:151], v[56:59]\n\t"
    "v_cvt_pk_bf16_f32 v78, v28, v29\n\t"
    "v_cvt_pk_bf16_f32 v79, v30, v31\n\t"
    "v_mfma_f32_16x16x32_bf16 v[56:59], a[168:171], v[152:155], v[56:59]\n\t"
    "buffer_load_dwordx4 v210, s[8:11], s28 offen offset:2048 lds\n\t"
    "v_cvt_pk_bf16_f32 v80, v32, v33\n\t"
    "v_mfma_f32_16x16x32_bf16 v[56:59], a[172:175], v[156:159], v[56:59]\n\t"
    "ds_read_b128 a[128:131], v196 offset:49152\n\t"
    "v_cvt_pk_bf16_f32 v81, v34, v35\n\t"
    "v_mfma_f32_16x16x32_bf16 v[60:63], a[188:191], v[156:159], v[172:175]\n\t"
    "ds_read_b128 a[132:135], v197 offset:49152\n\t"
    "v_cvt_pk_bf16_f32 v82, v36, v37\n\t"
    "v_mfma_f32_16x16x32_bf16 v[60:63], a[184:187], v[152:155], v[60:63]\n\t"
    "ds_read_b128 a[136:139], v198 offset:49152\n\t"
    "v_cvt_pk_bf16_f32 v83, v38, v39\n\t"
    "v_mfma_f32_16x16x32_bf16 v[60:63], a[180:183], v[148:151], v[60:63]\n\t"
    "ds_read_b128 a[140:143], v199 offset:49152\n\t"
    "v_cvt_pk_bf16_f32 v84, v40, v41\n\t"
    "v_mfma_f32_16x16x32_bf16 v[60:63], a[176:179], v[144:147], v[60:63]\n\t"
    "ds_read_b128 a[156:159], v199 offset:53248\n\t"
    "v_cvt_pk_bf16_f32 v85, v42, v43\n\t"
    "v_mfma_f32_16x16x32_bf16 a[4:7], a[192:195], v[72:75], a[4:7]\n\t"
    "ds_read_b128 a[152:155], v198 offset:53248\n\t"
    "v_cvt_pk_bf16_f32 v86, v44, v45\n\t"
    "v_mfma_f32_16x16x32_bf16 a[4:7], a[196:199], v[76:79], a[4:7]\n\t"
    "buffer_load_dwordx4 v211, s[8:11], s28 offen offset:3072 lds\n\t"
    "v_cvt_pk_bf16_f32 v87, v46, v47\n\t"
    "v_mfma_f32_16x16x32_bf16 a[20:23], a[200:203], v[72:75], a[20:23]\n\t"
    "ds_read_b128 a[148:151], v197 offset:53248\n\t"
    "v_add_f32_e32 v215, v215, v25\n\t"
    "v_mfma_f32_16x16x32_bf16 a[20:23], a[204:207], v[76:79], a[20:23]\n\t"
    "ds_read_b128 a[144:147], v196 offset:53248\n\t"
    "v_add_f32_e32 v215, v215, v26\n\t"
    "v_mfma_f32_16x16x32_bf16 a[36:39], a[208:211], v[72:75], a[36:39]\n\t"
    "ds_read_b128 a[160:163], v196 offset:57344\n\t"
    "v_add_f32_e32 v215, v215, v27\n\t"
    "v_mfma_f32_16x16x32_bf16 a[36:39], a[212:215], v[76:79], a[36:39]\n\t"
    "ds_read_b128 a[164:167], v197 offset:57344\n\t"
    "v_add_f32_e32 v215, v215, v28\n\t"
    "v_mfma_f32_16x16x32_bf16 a[52:55], a[216:219], v[72:75], a[52:55]\n\t"
    "ds_read_b128 a[168:171], v198 offset:57344\n\t"
    "v_add_f32_e32 v215, v215, v29\n\t"
    "v_mfma_f32_16x16x32_bf16 a[52:55], a[220:223], v[76:79], a[52:55]\n\t"
    "ds_read_b128 a[172:175], v199 offset:57344\n\t"
    "v_add_f32_e32 v215, v215, v30\n\t"
    "v_mfma_f32_16x16x32_bf16 a[68:71], a[224:227], v[72:75], a[68:71]\n\t"
    "ds_read_b128 a[188:191], v199 offset:61440\n\t"
    "v_add_f32_e32 v215, v215, v31\n\t"
    "v_mfma_f32_16x16x32_bf16 a[68:71], a[228:231], v[76:79], a[68:71]\n\t"
    "s_mov_b32 m0, s36\n\t"
    "v_add_f32_e32 v216, v216, v40\n\t"
    "v_mfma_f32_16x16x32_bf16 a[84:87], a[232:235], v[72:75], a[84:87]\n\t"
    "ds_read_b128 a[184:187], v198 offset:61440\n\t"
    "v_add_f32_e32 v216, v216, v41\n\t"
    "v_mfma_f32_16x16x32_bf16 a[84:87], a[236:239], v[76:79], a[84:87]\n\t"
    "ds_read_b128 a[180:183], v197 offset:61440\n\t"
    "v_add_f32_e32 v216, v216, v42\n\t"
    "v_mfma_f32_16x16x32_bf16 a[100:103], a[240:243], v[72:75], a[100:103]\n\t"
    "ds_read_b128 a[176:179], v196 offset:61440\n\t"
    "v_add_f32_e32 v216, v216, v43\n\t"
    "v_mfma_f32_16x16x32_bf16 a[100:103], a[244:247], v[76:79], a[100:103]\n\t"
    "buffer_load_dwordx4 v212, s[12:15], s28 offen lds\n\t"
    "v_add_f32_e32 v216, v216, v44\n\t"
    "v_mfma_f32_16x16x32_bf16 a[116:119], a[248:251], v[72:75], a[116:119]\n\t"
    "v_exp_f32_e32 v48, v48\n\t"
    "v_mfma_f32_16x16x32_bf16 a[116:119], a[252:255], v[76:79], a[116:119]\n\t"
    "v_exp_f32_e32 v49, v49\n\t"
    "v_mfma_f32_16x16x32_bf16 v[180:183], v[192:195], v[72:75], v[180:183]\n\t"
    "v_cvt_pk_bf16_f32 v88, v48, v49\n\t"
    "s_waitcnt lgkmcnt(8)\n\t"
    "v_mfma_f32_16x16x32_bf16 v[0:3], a[128:131], v[96:99], v[160:163]\n\t"
    "v_exp_f32_e32 v50, v50\n\t"
    "v_mfma_f32_16x16x32_bf16 v[0:3], a[132:135], v[100:103], v[0:3]\n\t"
    "v_exp_f32_e32 v51, v51\n\t"
    "v_mfma_f32_16x16x32_bf16 v[0:3], a[136:139], v[104:107], v[0:3]\n\t"
    "v_exp_f32_e32 v52, v52\n\t"
    "v_mfma_f32_16x16x32_bf16 v[0:3], a[140:143], v[108:111], v[0:3]\n\t"
    "v_exp_f32_e32 v53, v53\n\t"
    "v_mfma_f32_16x16x32_bf16 v[4:7], a[156:159], v[108:111], v[160:163]\n\t"
    "buffer_load_dwordx4 v213, s[12:15], s28 offen offset:1024 lds\n\t"
    "v_cvt_pk_bf16_f32 v89, v50, v51\n\t"
    "v_mfma_f32_16x16x32_bf16 v[4:7], a[152:155], v[104:107], v[4:7]\n\t"
    "v_exp_f32_e32 v54, v54\n\t"
    "v_mfma_f32_16x16x32_bf16 v[4:7], a[148:151], v[100:103], v[4:7]\n\t"
    "v_exp_f32_e32 v55, v55\n\t"
    "v_mfma_f32_16x16x32_bf16 v[4:7], a[144:147], v[96:99], v[4:7]\n\t"
    "v_cvt_pk_bf16_f32 v90, v52, v53\n\t"
    "s_waitcnt lgkmcnt(0)\n\t"
    "v_mfma_f32_16x16x32_bf16 v[8:11], a[160:163], v[96:99], v[160:163]\n\t"
    "v_exp_f32_e32 v56, v56\n\t"
    "v_mfma_f32_16x16x32_bf16 v[8:11], a[164:167], v[100:103], v[8:11]\n\t"
    "v_exp_f32_e32 v57, v57\n\t"
    "v_mfma_f32_16x16x32_bf16 v[8:11], a[168:171], v[104:107], v[8:11]\n\t"
    "v_exp_f32_e32 v58, v58\n\t"
    "v_mfma_f32_16x16x32_bf16 v[8:11], a[172:175], v[108:111], v[8:11]\n\t"
    "v_exp_f32_e32 v59, v59\n\t"
    "v_mfma_f32_16x16x32_bf16 v[12:15], a[188:191], v[108:111], v[160:163]\n\t"
    "v_exp_f32_e32 v60, v60\n\t"
    "v_mfma_f32_16x16x32_bf16 v[12:15], a[184:187], v[104:107], v[12:15]\n\t"
    "v_exp_f32_e32 v61, v61\n\t"
    "v_mfma_f32_16x16x32_bf16 v[12:15], a[180:183], v[100:103], v[12:15]\n\t"
    "buffer_load_dwordx4 v212, s[12:15], s28 offen offset:2048 lds\n\t"
    "v_cvt_pk_bf16_f32 v91, v54, v55\n\t"
    "v_mfma_f32_16x16x32_bf16 v[12:15], a[176:179], v[96:99], v[12:15]\n\t"
    "v_exp_f32_e32 v62, v62\n\t"
    "v_mfma_f32_16x16x32_bf16 a[8:11], a[192:195], v[80:83], a[8:11]\n\t"
    "v_exp_f32_e32 v63, v63\n\t"
    "v_mfma_f32_16x16x32_bf16 a[8:11], a[196:199], v[84:87], a[8:11]\n\t"
    "v_exp_f32_e32 v0, v0\n\t"
    "v_mfma_f32_16x16x32_bf16 a[24:27], a[200:203], v[80:83], a[24:27]\n\t"
    "v_exp_f32_e32 v1, v1\n\t"
    "v_mfma_f32_16x16x32_bf16 a[24:27], a[204:207], v[84:87], a[24:27]\n\t"
    "v_exp_f32_e32 v2, v2\n\t"
    "v_mfma_f32_16x16x32_bf16 a[40:43], a[208:211], v[80:83], a[40:43]\n\t"
    "v_exp_f32_e32 v3, v3\n\t"
    "v_mfma_f32_16x16x32_bf16 a[40:43], a[212:215], v[84:87], a[40:43]\n\t"
    "v_exp_f32_e32 v4, v4\n\t"
    "v_mfma_f32_16x16x32_bf16 a[56:59], a[216:219], v[80:83], a[56:59]\n\t"
    "v_exp_f32_e32 v5, v5\n\t"
    "v_mfma_f32_16x16x32_bf16 a[56:59], a[220:223], v[84:87], a[56:59]\n\t"
    "v_exp_f32_e32 v6, v6\n\t"
    "v_mfma_f32_16x16x32_bf16 a[72:75], a[224:227], v[80:83], a[72:75]\n\t"
    "buffer_load_dwordx4 v213, s[12:15], s28 offen offset:3072 lds\n\t"
    "v_cvt_pk_bf16_f32 v92, v56, v57\n\t"
    "v_mfma_f32_16x16x32_bf16 a[72:75], a[228:231], v[84:87], a[72:75]\n\t"
    "v_exp_f32_e32 v7, v7\n\t"
    "v_mfma_f32_16x16x32_bf16 a[88:91], a[232:235], v[80:83], a[88:91]\n\t"
    "v_exp_f32_e32 v8, v8\n\t"
    "v_mfma_f32_16x16x32_bf16 a[88:91], a[236:239], v[84:87], a[88:91]\n\t"
    "v_exp_f32_e32 v9, v9\n\t"
    "v_mfma_f32_16x16x32_bf16 a[104:107], a[240:243], v[80:83], a[104:107]\n\t"
    "v_exp_f32_e32 v10, v10\n\t"
    "v_mfma_f32_16x16x32_bf16 a[104:107], a[244:247], v[84:87], a[104:107]\n\t"
    "v_exp_f32_e32 v11, v11\n\t"
    "v_mfma_f32_16x16x32_bf16 a[120:123], a[248:251], v[80:83], a[120:123]\n\t"
    "v_exp_f32_e32 v12, v12\n\t"
    "v_mfma_f32_16x16x32_bf16 a[120:123], a[252:255], v[84:87], a[120:123]\n\t"
    "v_exp_f32_e32 v13, v13\n\t"
    "v_mfma_f32_16x16x32_bf16 v[184:187], v[192:195], v[80:83], v[184:187]\n\t"
    "v_exp_f32_e32 v14, v14\n\t"
    "v_mfma_f32_16x16x32_bf16 v[16:19], a[128:131], v[112:115], v[164:167]\n\t"
    "v_exp_f32_e32 v15, v15\n\t"
    "v_mfma_f32_16x16x32_bf16 v[16:19], a[132:135], v[116:119], v[16:19]\n\t"
    "s_mov_b32 m0, s49\n\t"
    "v_cvt_pk_bf16_f32 v93, v58, v59\n\t"
    "v_mfma_f32_16x16x32_bf16 v[16:19], a[136:139], v[120:123], v[16:19]\n\t"
    "buffer_load_dwordx4 v232, s[44:47], s48 offen lds\n\t"
    "v_cvt_pk_bf16_f32 v94, v60, v61\n\t"
    "v_mfma_f32_16x16x32_bf16 v[16:19], a[140:143], v[124:127], v[16:19]\n\t"
    "v_cvt_pk_bf16_f32 v95, v62, v63\n\t"
    "s_add_u32 s48, s48, 1024\n\t"
    "v_mfma_f32_16x16x32_bf16 v[20:23], a[156:159], v[124:127], v[164:167]\n\t"
    "s_add_u32 s28, s28, 16384\n\t"
    "v_add_f32_e32 v216, v216, v45\n\t"
    "v_mfma_f32_16x16x32_bf16 v[20:23], a[152:155], v[120:123], v[20:23]\n\t"
    "v_add_f32_e32 v216, v216, v46\n\t"
    "v_add_f32_e32 v216, v216, v47\n\t"
    "v_mfma_f32_16x16x32_bf16 v[20:23], a[148:151], v[116:119], v[20:23]\n\t"
    "v_cvt_pk_bf16_f32 v64, v0, v1\n\t"
    "v_cvt_pk_bf16_f32 v65, v2, v3\n\t"
    "v_mfma_f32_16x16x32_bf16 v[20:23], a[144:147], v[112:115], v[20:23]\n\t"
    "v_exp_f32_e32 v16, v16\n\t"
    "v_mfma_f32_16x16x32_bf16 v[24:27], a[160:163], v[112:115], v[164:167]\n\t"
    "v_exp_f32_e32 v17, v17\n\t"
    "v_mfma_f32_16x16x32_bf16 v[24:27], a[164:167], v[116:119], v[24:27]\n\t"
    "v_exp_f32_e32 v18, v18\n\t"
    "v_mfma_f32_16x16x32_bf16 v[24:27], a[168:171], v[120:123], v[24:27]\n\t"
    "v_exp_f32_e32 v19, v19\n\t"
    "v_mfma_f32_16x16x32_bf16 v[24:27], a[172:175], v[124:127], v[24:27]\n\t"
    "v_exp_f32_e32 v20, v20\n\t"
    "v_mfma_f32_16x16x32_bf16 v[28:31], a[188:191], v[124:127], v[164:167]\n\t"
    "v_exp_f32_e32 v21, v21\n\t"
    "v_mfma_f32_16x16x32_bf16 v[28:31], a[184:187], v[120:123], v[28:31]\n\t"
    "v_exp_f32_e32 v22, v22\n\t"
    "v_mfma_f32_16x16x32_bf16 v[28:31], a[180:183], v[116:119], v[28:31]\n\t"
    "v_exp_f32_e32 v23, v23\n\t"
    "v_mfma_f32_16x16x32_bf16 v[28:31], a[176:179], v[112:115], v[28:31]\n\t"
    "v_exp_f32_e32 v24, v24\n\t"
    "v_mfma_f32_16x16x32_bf16 a[12:15], a[192:195], v[88:91], a[12:15]\n\t"
    "s_waitcnt vmcnt(9)\n\t"
    "s_barrier\n\t"
    "v_mfma_f32_16x16x32_bf16 a[12:15], a[196:199], v[92:95], a[12:15]\n\t"
    "ds_read_b64_tr_b16 a[192:193], v200 offset:49152\n\t"
    "v_cvt_pk_bf16_f32 v66, v4, v5\n\t"
    "v_mfma_f32_16x16x32_bf16 a[28:31], a[200:203], v[88:91], a[28:31]\n\t"
    "ds_read_b64_tr_b16 a[194:195], v200 offset:53248\n\t"
    "v_cvt_pk_bf16_f32 v67, v6, v7\n\t"
    "v_mfma_f32_16x16x32_bf16 a[28:31], a[204:207], v[92:95], a[28:31]\n\t"
    "ds_read_b64_tr_b16 a[196:197], v200 offset:57344\n\t"
    "v_cvt_pk_bf16_f32 v68, v8, v9\n\t"
    "v_mfma_f32_16x16x32_bf16 a[44:47], a[208:211], v[88:91], a[44:47]\n\t"
    "ds_read_b64_tr_b16 a[198:199], v200 offset:61440\n\t"
    "v_cvt_pk_bf16_f32 v69, v10, v11\n\t"
    "v_mfma_f32_16x16x32_bf16 a[44:47], a[212:215], v[92:95], a[44:47]\n\t"
    "ds_read_b64_tr_b16 a[200:201], v201 offset:49152\n\t"
    "v_cvt_pk_bf16_f32 v70, v12, v13\n\t"
    "v_mfma_f32_16x16x32_bf16 a[60:63], a[216:219], v[88:91], a[60:63]\n\t"
    "ds_read_b64_tr_b16 a[202:203], v201 offset:53248\n\t"
    "v_cvt_pk_bf16_f32 v71, v14, v15\n\t"
    "v_mfma_f32_16x16x32_bf16 a[60:63], a[220:223], v[92:95], a[60:63]\n\t"
    "ds_read_b64_tr_b16 a[204:205], v201 offset:57344\n\t"
    "v_add_f32_e32 v217, v217, v56\n\t"
    "v_mfma_f32_16x16x32_bf16 a[76:79], a[224:227], v[88:91], a[76:79]\n\t"
    "ds_read_b64_tr_b16 a[206:207], v201 offset:61440\n\t"
    "v_add_f32_e32 v217, v217, v57\n\t"
    "v_mfma_f32_16x16x32_bf16 a[76:79], a[228:231], v[92:95], a[76:79]\n\t"
    "ds_read_b64_tr_b16 a[208:209], v202 offset:49152\n\t"
    "v_add_f32_e32 v217, v217, v58\n\t"
    "v_mfma_f32_16x16x32_bf16 a[92:95], a[232:235], v[88:91], a[92:95]\n\t"
    "ds_read_b64_tr_b16 a[210:211], v202 offset:53248\n\t"
    "v_add_f32_e32 v217, v217, v59\n\t"
    "v_mfma_f32_16x16x32_bf16 a[92:95], a[236:239], v[92:95], a[92:95]\n\t"
    "ds_read_b64_tr_b16 a[212:213], v202 offset:57344\n\t"
    "v_add_f32_e32 v217, v217, v60\n\t"
    "v_mfma_f32_16x16x32_bf16 a[108:111], a[240:243], v[88:91], a[108:111]\n\t"
    "ds_read_b64_tr_b16 a[214:215], v202 offset:61440\n\t"
    "v_add_f32_e32 v217, v217, v61\n\t"
    "v_mfma_f32_16x16x32_bf16 a[108:111], a[244:247], v[92:95], a[108:111]\n\t"
    "ds_read_b64_tr_b16 a[216:217], v203 offset:49152\n\t"
    "v_add_f32_e32 v217, v217, v62\n\t"
    "v_mfma_f32_16x16x32_bf16 a[124:127], a[248:251], v[88:91], a[124:127]\n\t"
    "ds_read_b64_tr_b16 a[218:219], v203 offset:53248\n\t"
    "v_add_f32_e32 v217, v217, v63\n\t"
    "v_mfma_f32_16x16x32_bf16 a[124:127], a[252:255], v[92:95], a[124:127]\n\t"
    "ds_read_b64_tr_b16 a[220:221], v203 offset:57344\n\t"
    "v_cvt_pk_bf16_f32 v72, v16, v17\n\t"
    "v_mfma_f32_16x16x32_bf16 v[188:191], v[192:195], v[88:91], v[188:191]\n\t"
    "ds_read_b64_tr_b16 a[222:223], v203 offset:61440\n\t"
    "v_cvt_pk_bf16_f32 v73, v18, v19\n\t"
    "v_mfma_f32_16x16x32_bf16 v[32:35], a[128:131], v[128:131], v[168:171]\n\t"
    "ds_read_b64_tr_b16 a[224:225], v204 offset:49152\n\t"
    "v_cvt_pk_bf16_f32 v74, v20, v21\n\t"
    "v_mfma_f32_16x16x32_bf16 v[32:35], a[132:135], v[132:135], v[32:35]\n\t"
    "ds_read_b64_tr_b16 a[226:227], v204 offset:53248\n\t"
    "v_cvt_pk_bf16_f32 v75, v22, v23\n\t"
    "v_mfma_f32_16x16x32_bf16 v[32:35], a[136:139], v[136:139], v[32:35]\n\t"
    "ds_read_b64_tr_b16 a[228:229], v204 offset:57344\n\t"
    "v_add_f32_e32 v214, v214, v8\n\t"
    "v_mfma_f32_16x16x32_bf16 v[32:35], a[140:143], v[140:143], v[32:35]\n\t"
    "ds_read_b64_tr_b16 a[230:231], v204 offset:61440\n\t"
    "v_add_f32_e32 v214, v214, v9\n\t"
    "v_mfma_f32_16x16x32_bf16 v[36:39], a[156:159], v[140:143], v[168:171]\n\t"
    "ds_read_b64_tr_b16 a[232:233], v205 offset:49152\n\t"
    "v_add_f32_e32 v214, v214, v10\n\t"
    "v_mfma_f32_16x16x32_bf16 v[36:39], a[152:155], v[136:139], v[36:39]\n\t"
    "ds_read_b64_tr_b16 a[234:235], v205 offset:53248\n\t"
    "v_add_f32_e32 v214, v214, v11\n\t"
    "v_mfma_f32_16x16x32_bf16 v[36:39], a[148:151], v[132:135], v[36:39]\n\t"
    "ds_read_b64_tr_b16 a[236:237], v205 offset:57344\n\t"
    "v_add_f32_e32 v214, v214, v12\n\t"
    "v_mfma_f32_16x16x32_bf16 v[36:39], a[144:147], v[128:131], v[36:39]\n\t"
    "ds_read_b64_tr_b16 a[238:239], v205 offset:61440\n\t"
    "v_add_f32_e32 v214, v214, v13\n\t"
    "v_mfma_f32_16x16x32_bf16 v[40:43], a[160:163], v[128:131], v[168:171]\n\t"
    "ds_read_b64_tr_b16 a[240:241], v206 offset:49152\n\t"
    "v_add_f32_e32 v214, v214, v14\n\t"
    "v_mfma_f32_16x16x32_bf16 v[40:43], a[164:167], v[132:135], v[40:43]\n\t"
    "ds_read_b64_tr_b16 a[242:243], v206 offset:53248\n\t"
    "v_add_f32_e32 v214, v214, v15\n\t"
    "v_mfma_f32_16x16x32_bf16 v[40:43], a[168:171], v[136:139], v[40:43]\n\t"
    "ds_read_b64_tr_b16 a[244:245], v206 offset:57344\n\t"
    "v_add_f32_e32 v215, v215, v24\n\t"
    "v_mfma_f32_16x16x32_bf16 v[40:43], a[172:175], v[140:143], v[40:43]\n\t"
    "ds_read_b64_tr_b16 a[246:247], v206 offset:61440\n\t"
    "v_mfma_f32_16x16x32_bf16 v[44:47], a[188:191], v[140:143], v[168:171]\n\t"
    "ds_read_b64_tr_b16 a[248:249], v207 offset:49152\n\t"
    "v_mfma_f32_16x16x32_bf16 v[44:47], a[184:187], v[136:139], v[44:47]\n\t"
    "ds_read_b64_tr_b16 a[250:251], v207 offset:53248\n\t"
    "v_mfma_f32_16x16x32_bf16 v[44:47], a[180:183], v[132:135], v[44:47]\n\t"
    "ds_read_b64_tr_b16 a[252:253], v207 offset:57344\n\t"
    "v_mfma_f32_16x16x32_bf16 v[44:47], a[176:179], v[128:131], v[44:47]\n\t"
    "ds_read_b64_tr_b16 a[254:255], v207 offset:61440\n\t"
    "s_waitcnt lgkmcnt(15)\n\t"
    "v_mfma_f32_16x16x32_bf16 a[0:3], a[192:195], v[64:67], a[0:3]\n\t"
    "v_exp_f32_e32 v25, v25\n\t"
    "v_mfma_f32_16x16x32_bf16 a[0:3], a[196:199], v[68:71], a[0:3]\n\t"
    "v_exp_f32_e32 v26, v26\n\t"
    "v_mfma_f32_16x16x32_bf16 a[16:19], a[200:203], v[64:67], a[16:19]\n\t"
    "v_exp_f32_e32 v27, v27\n\t"
    "v_mfma_f32_16x16x32_bf16 a[16:19], a[204:207], v[68:71], a[16:19]\n\t"
    "v_exp_f32_e32 v28, v28\n\t"
    "v_mfma_f32_16x16x32_bf16 a[32:35], a[208:211], v[64:67], a[32:35]\n\t"
    "v_exp_f32_e32 v29, v29\n\t"
    "v_mfma_f32_16x16x32_bf16 a[32:35], a[212:215], v[68:71], a[32:35]\n\t"
    "v_exp_f32_e32 v30, v30\n\t"
    "v_mfma_f32_16x16x32_bf16 a[48:51], a[216:219], v[64:67], a[48:51]\n\t"
    "v_exp_f32_e32 v31, v31\n\t"
    "v_mfma_f32_16x16x32_bf16 a[48:51], a[220:223], v[68:71], a[48:51]\n\t"
    "s_mov_b32 m0, s34\n\t"
    "s_waitcnt lgkmcnt(0)\n\t"
    "v_mfma_f32_16x16x32_bf16 a[64:67], a[224:227], v[64:67], a[64:67]\n\t"
    "buffer_load_dwordx4 v208, s[8:11], s28 offen lds\n\t"
    "v_cvt_pk_bf16_f32 v76, v24, v25\n\t"
    "v_mfma_f32_16x16x32_bf16 a[64:67], a[228:231], v[68:71], a[64:67]\n\t"
    "v_exp_f32_e32 v32, v32\n\t"
    "v_mfma_f32_16x16x32_bf16 a[80:83], a[232:235], v[64:67], a[80:83]\n\t"
    "v_exp_f32_e32 v33, v33\n\t"
    "v_mfma_f32_16x16x32_bf16 a[80:83], a[236:239], v[68:71], a[80:83]\n\t"
    "v_exp_f32_e32 v34, v34\n\t"
    "v_mfma_f32_16x16x32_bf16 a[96:99], a[240:243], v[64:67], a[96:99]\n\t"
    "v_exp_f32_e32 v35, v35\n\t"
    "v_mfma_f32_16x16x32_bf16 a[96:99], a[244:247], v[68:71], a[96:99]\n\t"
    "v_exp_f32_e32 v36, v36\n\t"
    "v_mfma_f32_16x16x32_bf16 a[112:115], a[248:251], v[64:67], a[112:115]\n\t"
    "buffer_load_dwordx4 v209, s[8:11], s28 offen offset:1024 lds\n\t"
    "v_cvt_pk_bf16_f32 v77, v26, v27\n\t"
    "v_mfma_f32_16x16x32_bf16 a[112:115], a[252:255], v[68:71], a[112:115]\n\t"
    "v_exp_f32_e32 v37, v37\n\t"
    "v_mfma_f32_16x16x32_bf16 v[176:179], v[192:195], v[64:67], v[176:179]\n\t"
    "v_exp_f32_e32 v38, v38\n\t"
    "v_mfma_f32_16x16x32_bf16 v[48:51], a[128:131], v[144:147], v[172:175]\n\t"
    "v_exp_f32_e32 v39, v39\n\t"
    "v_mfma_f32_16x16x32_bf16 v[48:51], a[132:135], v[148:151], v[48:51]\n\t"
    "v_exp_f32_e32 v40, v40\n\t"
    "v_mfma_f32_16x16x32_bf16 v[48:51], a[136:139], v[152:155], v[48:51]\n\t"
    "v_exp_f32_e32 v41, v41\n\t"
    "v_mfma_f32_16x16x32_bf16 v[48:51], a[140:143], v[156:159], v[48:51]\n\t"
    "v_exp_f32_e32 v42, v42\n\t"
    "v_mfma_f32_16x16x32_bf16 v[52:55], a[156:159], v[156:159], v[172:175]\n\t"
    "v_exp_f32_e32 v43, v43\n\t"
    "v_mfma_f32_16x16x32_bf16 v[52:55], a[152:155], v[152:155], v[52:55]\n\t"
    "v_exp_f32_e32 v44, v44\n\t"
    "v_mfma_f32_16x16x32_bf16 v[52:55], a[148:151], v[148:151], v[52:55]\n\t"
    "v_exp_f32_e32 v45, v45\n\t"
    "v_mfma_f32_16x16x32_bf16 v[52:55], a[144:147], v[144:147], v[52:55]\n\t"
    "v_exp_f32_e32 v46, v46\n\t"
    "v_mfma_f32_16x16x32_bf16 v[56:59], a[160:163], v[144:147], v[172:175]\n\t"
    "v_exp_f32_e32 v47, v47\n\t"
    "v_mfma_f32_16x16x32_bf16 v[56:59], a[164:167], v[148:151], v[56:59]\n\t"
    "v_cvt_pk_bf16_f32 v78, v28, v29\n\t"
    "v_cvt_pk_bf16_f32 v79, v30, v31\n\t"
    "v_mfma_f32_16x16x32_bf16 v[56:59], a[168:171], v[152:155], v[56:59]\n\t"
    "buffer_load_dwordx4 v210, s[8:11], s28 offen offset:2048 lds\n\t"
    "v_cvt_pk_bf16_f32 v80, v32, v33\n\t"
    "v_mfma_f32_16x16x32_bf16 v[56:59], a[172:175], v[156:159], v[56:59]\n\t"
    "ds_read_b128 a[128:131], v196 offset:0\n\t"
    "v_cvt_pk_bf16_f32 v81, v34, v35\n\t"
    "v_mfma_f32_16x16x32_bf16 v[60:63], a[188:191], v[156:159], v[172:175]\n\t"
    "ds_read_b128 a[132:135], v197 offset:0\n\t"
    "v_cvt_pk_bf16_f32 v82, v36, v37\n\t"
    "v_mfma_f32_16x16x32_bf16 v[60:63], a[184:187], v[152:155], v[60:63]\n\t"
    "ds_read_b128 a[136:139], v198 offset:0\n\t"
    "v_cvt_pk_bf16_f32 v83, v38, v39\n\t"
    "v_mfma_f32_16x16x32_bf16 v[60:63], a[180:183], v[148:151], v[60:63]\n\t"
    "ds_read_b128 a[140:143], v199 offset:0\n\t"
    "v_cvt_pk_bf16_f32 v84, v40, v41\n\t"
    "v_mfma_f32_16x16x32_bf16 v[60:63], a[176:179], v[144:147], v[60:63]\n\t"
    "ds_read_b128 a[156:159], v199 offset:4096\n\t"
    "v_cvt_pk_bf16_f32 v85, v42, v43\n\t"
    "v_mfma_f32_16x16x32_bf16 a[4:7], a[192:195], v[72:75], a[4:7]\n\t"
    "ds_read_b128 a[152:155], v198 offset:4096\n\t"
    "v_cvt_pk_bf16_f32 v86, v44, v45\n\t"
    "v_mfma_f32_16x16x32_bf16 a[4:7], a[196:199], v[76:79], a[4:7]\n\t"
    "buffer_load_dwordx4 v211, s[8:11], s28 offen offset:3072 lds\n\t"
    "v_cvt_pk_bf16_f32 v87, v46, v47\n\t"
    "v_mfma_f32_16x16x32_bf16 a[20:23], a[200:203], v[72:75], a[20:23]\n\t"
    "ds_read_b128 a[148:151], v197 offset:4096\n\t"
    "v_add_f32_e32 v215, v215, v25\n\t"
    "v_mfma_f32_16x16x32_bf16 a[20:23], a[204:207], v[76:79], a[20:23]\n\t"
    "ds_read_b128 a[144:147], v196 offset:4096\n\t"
    "v_add_f32_e32 v215, v215, v26\n\t"
    "v_mfma_f32_16x16x32_bf16 a[36:39], a[208:211], v[72:75], a[36:39]\n\t"
    "ds_read_b128 a[160:163], v196 offset:8192\n\t"
    "v_add_f32_e32 v215, v215, v27\n\t"
    "v_mfma_f32_16x16x32_bf16 a[36:39], a[212:215], v[76:79], a[36:39]\n\t"
    "ds_read_b128 a[164:167], v197 offset:8192\n\t"
    "v_add_f32_e32 v215, v215, v28\n\t"
    "v_mfma_f32_16x16x32_bf16 a[52:55], a[216:219], v[72:75], a[52:55]\n\t"
    "ds_read_b128 a[168:171], v198 offset:8192\n\t"
    "v_add_f32_e32 v215, v215, v29\n\t"
    "v_mfma_f32_16x16x32_bf16 a[52:55], a[220:223], v[76:79], a[52:55]\n\t"
    "ds_read_b128 a[172:175], v199 offset:8192\n\t"
    "v_add_f32_e32 v215, v215, v30\n\t"
    "v_mfma_f32_16x16x32_bf16 a[68:71], a[224:227], v[72:75], a[68:71]\n\t"
    "ds_read_b128 a[188:191], v199 offset:12288\n\t"
    "v_add_f32_e32 v215, v215, v31\n\t"
    "v_mfma_f32_16x16x32_bf16 a[68:71], a[228:231], v[76:79], a[68:71]\n\t"
    "s_mov_b32 m0, s37\n\t"
    "v_add_f32_e32 v216, v216, v40\n\t"
    "v_mfma_f32_16x16x32_bf16 a[84:87], a[232:235], v[72:75], a[84:87]\n\t"
    "ds_read_b128 a[184:187], v198 offset:12288\n\t"
    "v_add_f32_e32 v216, v216, v41\n\t"
    "v_mfma_f32_16x16x32_bf16 a[84:87], a[236:239], v[76:79], a[84:87]\n\t"
    "ds_read_b128 a[180:183], v197 offset:12288\n\t"
    "v_add_f32_e32 v216, v216, v42\n\t"
    "v_mfma_f32_16x16x32_bf16 a[100:103], a[240:243], v[72:75], a[100:103]\n\t"
    "ds_read_b128 a[176:179], v196 offset:12288\n\t"
    "v_add_f32_e32 v216, v216, v43\n\t"
    "v_mfma_f32_16x16x32_bf16 a[100:103], a[244:247], v[76:79], a[100:103]\n\t"
    "buffer_load_dwordx4 v212, s[12:15], s28 offen lds\n\t"
    "v_add_f32_e32 v216, v216, v44\n\t"
    "v_mfma_f32_16x16x32_bf16 a[116:119], a[248:251], v[72:75], a[116:119]\n\t"
    "v_exp_f32_e32 v48, v48\n\t"
    "v_mfma_f32_16x16x32_bf16 a[116:119], a[252:255], v[76:79], a[116:119]\n\t"
    "v_exp_f32_e32 v49, v49\n\t"
    "v_mfma_f32_16x16x32_bf16 v[180:183], v[192:195], v[72:75], v[180:183]\n\t"
    "v_cvt_pk_bf16_f32 v88, v48, v49\n\t"
    "s_waitcnt lgkmcnt(8)\n\t"
    "v_mfma_f32_16x16x32_bf16 v[0:3], a[128:131], v[96:99], v[160:163]\n\t"
    "v_exp_f32_e32 v50, v50\n\t"
    "v_mfma_f32_16x16x32_bf16 v[0:3], a[132:135], v[100:103], v[0:3]\n\t"
    "v_exp_f32_e32 v51, v51\n\t"
    "v_mfma_f32_16x16x32_bf16 v[0:3], a[136:139], v[104:107], v[0:3]\n\t"
    "v_exp_f32_e32 v52, v52\n\t"
    "v_mfma_f32_16x16x32_bf16 v[0:3], a[140:143], v[108:111], v[0:3]\n\t"
    "v_exp_f32_e32 v53, v53\n\t"
    "v_mfma_f32_16x16x32_bf16 v[4:7], a[156:159], v[108:111], v[160:163]\n\t"
    "buffer_load_dwordx4 v213, s[12:15], s28 offen offset:1024 lds\n\t"
    "v_cvt_pk_bf16_f32 v89, v50, v51\n\t"
    "v_mfma_f32_16x16x32_bf16 v[4:7], a[152:155], v[104:107], v[4:7]\n\t"
    "v_exp_f32_e32 v54, v54\n\t"
    "v_mfma_f32_16x16x32_bf16 v[4:7], a[148:151], v[100:103], v[4:7]\n\t"
    "v_exp_f32_e32 v55, v55\n\t"
    "v_mfma_f32_16x16x32_bf16 v[4:7], a[144:147], v[96:99], v[4:7]\n\t"
    "v_cvt_pk_bf16_f32 v90, v52, v53\n\t"
    "s_waitcnt lgkmcnt(0)\n\t"
    "v_mfma_f32_16x16x32_bf16 v[8:11], a[160:163], v[96:99], v[160:163]\n\t"
    "v_exp_f32_e32 v56, v56\n\t"
    "v_mfma_f32_16x16x32_bf16 v[8:11], a[164:167], v[100:103], v[8:11]\n\t"
    "v_exp_f32_e32 v57, v57\n\t"
    "v_mfma_f32_16x16x32_bf16 v[8:11], a[168:171], v[104:107], v[8:11]\n\t"
    "v_exp_f32_e32 v58, v58\n\t"
    "v_mfma_f32_16x16x32_bf16 v[8:11], a[172:175], v[108:111], v[8:11]\n\t"
    "v_exp_f32_e32 v59, v59\n\t"
    "v_mfma_f32_16x16x32_bf16 v[12:15], a[188:191], v[108:111], v[160:163]\n\t"
    "v_exp_f32_e32 v60, v60\n\t"
    "v_mfma_f32_16x16x32_bf16 v[12:15], a[184:187], v[104:107], v[12:15]\n\t"
    "v_exp_f32_e32 v61, v61\n\t"
    "v_mfma_f32_16x16x32_bf16 v[12:15], a[180:183], v[100:103], v[12:15]\n\t"
    "buffer_load_dwordx4 v212, s[12:15], s28 offen offset:2048 lds\n\t"
    "v_cvt_pk_bf16_f32 v91, v54, v55\n\t"
    "v_mfma_f32_16x16x32_bf16 v[12:15], a[176:179], v[96:99], v[12:15]\n\t"
    "v_exp_f32_e32 v62, v62\n\t"
    "v_mfma_f32_16x16x32_bf16 a[8:11], a[192:195], v[80:83], a[8:11]\n\t"
    "v_exp_f32_e32 v63, v63\n\t"
    "v_mfma_f32_16x16x32_bf16 a[8:11], a[196:199], v[84:87], a[8:11]\n\t"
    "v_exp_f32_e32 v0, v0\n\t"
    "v_mfma_f32_16x16x32_bf16 a[24:27], a[200:203], v[80:83], a[24:27]\n\t"
    "v_exp_f32_e32 v1, v1\n\t"
    "v_mfma_f32_16x16x32_bf16 a[24:27], a[204:207], v[84:87], a[24:27]\n\t"
    "v_exp_f32_e32 v2, v2\n\t"
    "v_mfma_f32_16x16x32_bf16 a[40:43], a[208:211], v[80:83], a[40:43]\n\t"
    "v_exp_f32_e32 v3, v3\n\t"
    "v_mfma_f32_16x16x32_bf16 a[40:43], a[212:215], v[84:87], a[40:43]\n\t"
    "v_exp_f32_e32 v4, v4\n\t"
    "v_mfma_f32_16x16x32_bf16 a[56:59], a[216:219], v[80:83], a[56:59]\n\t"
    "v_exp_f32_e32 v5, v5\n\t"
    "v_mfma_f32_16x16x32_bf16 a[56:59], a[220:223], v[84:87], a[56:59]\n\t"
    "v_exp_f32_e32 v6, v6\n\t"
    "v_mfma_f32_16x16x32_bf16 a[72:75], a[224:227], v[80:83], a[72:75]\n\t"
    "buffer_load_dwordx4 v213, s[12:15], s28 offen offset:3072 lds\n\t"
    "v_cvt_pk_bf16_f32 v92, v56, v57\n\t"
    "v_mfma_f32_16x16x32_bf16 a[72:75], a[228:231], v[84:87], a[72:75]\n\t"
    "v_exp_f32_e32 v7, v7\n\t"
    "v_mfma_f32_16x16x32_bf16 a[88:91], a[232:235], v[80:83], a[88:91]\n\t"
    "v_exp_f32_e32 v8, v8\n\t"
    "v_mfma_f32_16x16x32_bf16 a[88:91], a[236:239], v[84:87], a[88:91]\n\t"
    "v_exp_f32_e32 v9, v9\n\t"
    "v_mfma_f32_16x16x32_bf16 a[104:107], a[240:243], v[80:83], a[104:107]\n\t"
    "v_exp_f32_e32 v10, v10\n\t"
    "v_mfma_f32_16x16x32_bf16 a[104:107], a[244:247], v[84:87], a[104:107]\n\t"
    "v_exp_f32_e32 v11, v11\n\t"
    "v_mfma_f32_16x16x32_bf16 a[120:123], a[248:251], v[80:83], a[120:123]\n\t"
    "v_exp_f32_e32 v12, v12\n\t"
    "v_mfma_f32_16x16x32_bf16 a[120:123], a[252:255], v[84:87], a[120:123]\n\t"
    "v_exp_f32_e32 v13, v13\n\t"
    "v_mfma_f32_16x16x32_bf16 v[184:187], v[192:195], v[80:83], v[184:187]\n\t"
    "v_exp_f32_e32 v14, v14\n\t"
    "v_mfma_f32_16x16x32_bf16 v[16:19], a[128:131], v[112:115], v[164:167]\n\t"
    "v_exp_f32_e32 v15, v15\n\t"
    "v_mfma_f32_16x16x32_bf16 v[16:19], a[132:135], v[116:119], v[16:19]\n\t"
    "v_cvt_pk_bf16_f32 v93, v58, v59\n\t"
    "v_mfma_f32_16x16x32_bf16 v[16:19], a[136:139], v[120:123], v[16:19]\n\t"
    "v_cvt_pk_bf16_f32 v94, v60, v61\n\t"
    "v_mfma_f32_16x16x32_bf16 v[16:19], a[140:143], v[124:127], v[16:19]\n\t"
    "v_cvt_pk_bf16_f32 v95, v62, v63\n\t"
    "v_mfma_f32_16x16x32_bf16 v[20:23], a[156:159], v[124:127], v[164:167]\n\t"
    "s_add_u32 s28, s28, 16384\n\t"
    "v_add_f32_e32 v216, v216, v45\n\t"
    "v_mfma_f32_16x16x32_bf16 v[20:23], a[152:155], v[120:123], v[20:23]\n\t"
    "v_add_f32_e32 v216, v216, v46\n\t"
    "v_add_f32_e32 v216, v216, v47\n\t"
    "v_mfma_f32_16x16x32_bf16 v[20:23], a[148:151], v[116:119], v[20:23]\n\t"
    "v_cvt_pk_bf16_f32 v64, v0, v1\n\t"
    "v_cvt_pk_bf16_f32 v65, v2, v3\n\t"
    "v_mfma_f32_16x16x32_bf16 v[20:23], a[144:147], v[112:115], v[20:23]\n\t"
    "v_exp_f32_e32 v16, v16\n\t"
    "v_mfma_f32_16x16x32_bf16 v[24:27], a[160:163], v[112:115], v[164:167]\n\t"
    "v_exp_f32_e32 v17, v17\n\t"
    "v_mfma_f32_16x16x32_bf16 v[24:27], a[164:167], v[116:119], v[24:27]\n\t"
    "v_exp_f32_e32 v18, v18\n\t"
    "v_mfma_f32_16x16x32_bf16 v[24:27], a[168:171], v[120:123], v[24:27]\n\t"
    "v_exp_f32_e32 v19, v19\n\t"
    "v_mfma_f32_16x16x32_bf16 v[24:27], a[172:175], v[124:127], v[24:27]\n\t"
    "v_exp_f32_e32 v20, v20\n\t"
    "v_mfma_f32_16x16x32_bf16 v[28:31], a[188:191], v[124:127], v[164:167]\n\t"
    "v_exp_f32_e32 v21, v21\n\t"
    "v_mfma_f32_16x16x32_bf16 v[28:31], a[184:187], v[120:123], v[28:31]\n\t"
    "v_exp_f32_e32 v22, v22\n\t"
    "v_mfma_f32_16x16x32_bf16 v[28:31], a[180:183], v[116:119], v[28:31]\n\t"
    "v_exp_f32_e32 v23, v23\n\t"
    "v_mfma_f32_16x16x32_bf16 v[28:31], a[176:179], v[112:115], v[28:31]\n\t"
    "v_exp_f32_e32 v24, v24\n\t"
    "v_mfma_f32_16x16x32_bf16 a[12:15], a[192:195], v[88:91], a[12:15]\n\t"
    "s_waitcnt vmcnt(9)\n\t"
    "s_barrier\n\t"
    "v_mfma_f32_16x16x32_bf16 a[12:15], a[196:199], v[92:95], a[12:15]\n\t"
    "ds_read_b64_tr_b16 a[192:193], v200 offset:0\n\t"
    "v_cvt_pk_bf16_f32 v66, v4, v5\n\t"
    "v_mfma_f32_16x16x32_bf16 a[28:31], a[200:203], v[88:91], a[28:31]\n\t"
    "ds_read_b64_tr_b16 a[194:195], v200 offset:4096\n\t"
    "v_cvt_pk_bf16_f32 v67, v6, v7\n\t"
    "v_mfma_f32_16x16x32_bf16 a[28:31], a[204:207], v[92:95], a[28:31]\n\t"
    "ds_read_b64_tr_b16 a[196:197], v200 offset:8192\n\t"
    "v_cvt_pk_bf16_f32 v68, v8, v9\n\t"
    "v_mfma_f32_16x16x32_bf16 a[44:47], a[208:211], v[88:91], a[44:47]\n\t"
    "ds_read_b64_tr_b16 a[198:199], v200 offset:12288\n\t"
    "v_cvt_pk_bf16_f32 v69, v10, v11\n\t"
    "v_mfma_f32_16x16x32_bf16 a[44:47], a[212:215], v[92:95], a[44:47]\n\t"
    "ds_read_b64_tr_b16 a[200:201], v201 offset:0\n\t"
    "v_cvt_pk_bf16_f32 v70, v12, v13\n\t"
    "v_mfma_f32_16x16x32_bf16 a[60:63], a[216:219], v[88:91], a[60:63]\n\t"
    "ds_read_b64_tr_b16 a[202:203], v201 offset:4096\n\t"
    "v_cvt_pk_bf16_f32 v71, v14, v15\n\t"
    "v_mfma_f32_16x16x32_bf16 a[60:63], a[220:223], v[92:95], a[60:63]\n\t"
    "ds_read_b64_tr_b16 a[204:205], v201 offset:8192\n\t"
    "v_add_f32_e32 v217, v217, v56\n\t"
    "v_mfma_f32_16x16x32_bf16 a[76:79], a[224:227], v[88:91], a[76:79]\n\t"
    "ds_read_b64_tr_b16 a[206:207], v201 offset:12288\n\t"
    "v_add_f32_e32 v217, v217, v57\n\t"
    "v_mfma_f32_16x16x32_bf16 a[76:79], a[228:231], v[92:95], a[76:79]\n\t"
    "ds_read_b64_tr_b16 a[208:209], v202 offset:0\n\t"
    "v_add_f32_e32 v217, v217, v58\n\t"
    "v_mfma_f32_16x16x32_bf16 a[92:95], a[232:235], v[88:91], a[92:95]\n\t"
    "ds_read_b64_tr_b16 a[210:211], v202 offset:4096\n\t"
    "v_add_f32_e32 v217, v217, v59\n\t"
    "v_mfma_f32_16x16x32_bf16 a[92:95], a[236:239], v[92:95], a[92:95]\n\t"
    "ds_read_b64_tr_b16 a[212:213], v202 offset:8192\n\t"
    "v_add_f32_e32 v217, v217, v60\n\t"
    "v_mfma_f32_16x16x32_bf16 a[108:111], a[240:243], v[88:91], a[108:111]\n\t"
    "ds_read_b64_tr_b16 a[214:215], v202 offset:12288\n\t"
    "v_add_f32_e32 v217, v217, v61\n\t"
    "v_mfma_f32_16x16x32_bf16 a[108:111], a[244:247], v[92:95], a[108:111]\n\t"
    "ds_read_b64_tr_b16 a[216:217], v203 offset:0\n\t"
    "v_add_f32_e32 v217, v217, v62\n\t"
    "v_mfma_f32_16x16x32_bf16 a[124:127], a[248:251], v[88:91], a[124:127]\n\t"
    "ds_read_b64_tr_b16 a[218:219], v203 offset:4096\n\t"
    "v_add_f32_e32 v217, v217, v63\n\t"
    "v_mfma_f32_16x16x32_bf16 a[124:127], a[252:255], v[92:95], a[124:127]\n\t"
    "ds_read_b64_tr_b16 a[220:221], v203 offset:8192\n\t"
    "v_cvt_pk_bf16_f32 v72, v16, v17\n\t"
    "v_mfma_f32_16x16x32_bf16 v[188:191], v[192:195], v[88:91], v[188:191]\n\t"
    "ds_read_b64_tr_b16 a[222:223], v203 offset:12288\n\t"
    "v_cvt_pk_bf16_f32 v73, v18, v19\n\t"
    "v_mfma_f32_16x16x32_bf16 v[32:35], a[128:131], v[128:131], v[168:171]\n\t"
    "ds_read_b64_tr_b16 a[224:225], v204 offset:0\n\t"
    "v_cvt_pk_bf16_f32 v74, v20, v21\n\t"
    "v_mfma_f32_16x16x32_bf16 v[32:35], a[132:135], v[132:135], v[32:35]\n\t"
    "ds_read_b64_tr_b16 a[226:227], v204 offset:4096\n\t"
    "v_cvt_pk_bf16_f32 v75, v22, v23\n\t"
    "v_mfma_f32_16x16x32_bf16 v[32:35], a[136:139], v[136:139], v[32:35]\n\t"
    "ds_read_b64_tr_b16 a[228:229], v204 offset:8192\n\t"
    "v_add_f32_e32 v214, v214, v8\n\t"
    "v_mfma_f32_16x16x32_bf16 v[32:35], a[140:143], v[140:143], v[32:35]\n\t"
    "ds_read_b64_tr_b16 a[230:231], v204 offset:12288\n\t"
    "v_add_f32_e32 v214, v214, v9\n\t"
    "v_mfma_f32_16x16x32_bf16 v[36:39], a[156:159], v[140:143], v[168:171]\n\t"
    "ds_read_b64_tr_b16 a[232:233], v205 offset:0\n\t"
    "v_add_f32_e32 v214, v214, v10\n\t"
    "v_mfma_f32_16x16x32_bf16 v[36:39], a[152:155], v[136:139], v[36:39]\n\t"
    "ds_read_b64_tr_b16 a[234:235], v205 offset:4096\n\t"
    "v_add_f32_e32 v214, v214, v11\n\t"
    "v_mfma_f32_16x16x32_bf16 v[36:39], a[148:151], v[132:135], v[36:39]\n\t"
    "ds_read_b64_tr_b16 a[236:237], v205 offset:8192\n\t"
    "v_add_f32_e32 v214, v214, v12\n\t"
    "v_mfma_f32_16x16x32_bf16 v[36:39], a[144:147], v[128:131], v[36:39]\n\t"
    "ds_read_b64_tr_b16 a[238:239], v205 offset:12288\n\t"
    "v_add_f32_e32 v214, v214, v13\n\t"
    "v_mfma_f32_16x16x32_bf16 v[40:43], a[160:163], v[128:131], v[168:171]\n\t"
    "ds_read_b64_tr_b16 a[240:241], v206 offset:0\n\t"
    "v_add_f32_e32 v214, v214, v14\n\t"
    "v_mfma_f32_16x16x32_bf16 v[40:43], a[164:167], v[132:135], v[40:43]\n\t"
    "ds_read_b64_tr_b16 a[242:243], v206 offset:4096\n\t"
    "v_add_f32_e32 v214, v214, v15\n\t"
    "v_mfma_f32_16x16x32_bf16 v[40:43], a[168:171], v[136:139], v[40:43]\n\t"
    "ds_read_b64_tr_b16 a[244:245], v206 offset:8192\n\t"
    "v_add_f32_e32 v215, v215, v24\n\t"
    "v_mfma_f32_16x16x32_bf16 v[40:43], a[172:175], v[140:143], v[40:43]\n\t"
    "ds_read_b64_tr_b16 a[246:247], v206 offset:12288\n\t"
    "v_mfma_f32_16x16x32_bf16 v[44:47], a[188:191], v[140:143], v[168:171]\n\t"
    "ds_read_b64_tr_b16 a[248:249], v207 offset:0\n\t"
    "v_mfma_f32_16x16x32_bf16 v[44:47], a[184:187], v[136:139], v[44:47]\n\t"
    "ds_read_b64_tr_b16 a[250:251], v207 offset:4096\n\t"
    "v_mfma_f32_16x16x32_bf16 v[44:47], a[180:183], v[132:135], v[44:47]\n\t"
    "ds_read_b64_tr_b16 a[252:253], v207 offset:8192\n\t"
    "v_mfma_f32_16x16x32_bf16 v[44:47], a[176:179], v[128:131], v[44:47]\n\t"
    "ds_read_b64_tr_b16 a[254:255], v207 offset:12288\n\t"
    "s_waitcnt lgkmcnt(15)\n\t"
    "v_mfma_f32_16x16x32_bf16 a[0:3], a[192:195], v[64:67], a[0:3]\n\t"
    "v_exp_f32_e32 v25, v25\n\t"
    "v_mfma_f32_16x16x32_bf16 a[0:3], a[196:199], v[68:71], a[0:3]\n\t"
    "v_exp_f32_e32 v26, v26\n\t"
    "v_mfma_f32_16x16x32_bf16 a[16:19], a[200:203], v[64:67], a[16:19]\n\t"
    "v_exp_f32_e32 v27, v27\n\t"
    "v_mfma_f32_16x16x32_bf16 a[16:19], a[204:207], v[68:71], a[16:19]\n\t"
    "v_exp_f32_e32 v28, v28\n\t"
    "v_mfma_f32_16x16x32_bf16 a[32:35], a[208:211], v[64:67], a[32:35]\n\t"
    "v_exp_f32_e32 v29, v29\n\t"
    "v_mfma_f32_16x16x32_bf16 a[32:35], a[212:215], v[68:71], a[32:35]\n\t"
    "v_exp_f32_e32 v30, v30\n\t"
    "v_mfma_f32_16x16x32_bf16 a[48:51], a[216:219], v[64:67], a[48:51]\n\t"
    "v_exp_f32_e32 v31, v31\n\t"
    "v_mfma_f32_16x16x32_bf16 a[48:51], a[220:223], v[68:71], a[48:51]\n\t"
    "s_mov_b32 m0, s35\n\t"
    "s_waitcnt lgkmcnt(0)\n\t"
    "v_mfma_f32_16x16x32_bf16 a[64:67], a[224:227], v[64:67], a[64:67]\n\t"
    "buffer_load_dwordx4 v208, s[8:11], s28 offen lds\n\t"
    "v_cvt_pk_bf16_f32 v76, v24, v25\n\t"
    "v_mfma_f32_16x16x32_bf16 a[64:67], a[228:231], v[68:71], a[64:67]\n\t"
    "v_exp_f32_e32 v32, v32\n\t"
    "v_mfma_f32_16x16x32_bf16 a[80:83], a[232:235], v[64:67], a[80:83]\n\t"
    "v_exp_f32_e32 v33, v33\n\t"
    "v_mfma_f32_16x16x32_bf16 a[80:83], a[236:239], v[68:71], a[80:83]\n\t"
    "v_exp_f32_e32 v34, v34\n\t"
    "v_mfma_f32_16x16x32_bf16 a[96:99], a[240:243], v[64:67], a[96:99]\n\t"
    "v_exp_f32_e32 v35, v35\n\t"
    "v_mfma_f32_16x16x32_bf16 a[96:99], a[244:247], v[68:71], a[96:99]\n\t"
    "v_exp_f32_e32 v36, v36\n\t"
    "v_mfma_f32_16x16x32_bf16 a[112:115], a[248:251], v[64:67], a[112:115]\n\t"
    "buffer_load_dwordx4 v209, s[8:11], s28 offen offset:1024 lds\n\t"
    "v_cvt_pk_bf16_f32 v77, v26, v27\n\t"
    "v_mfma_f32_16x16x32_bf16 a[112:115], a[252:255], v[68:71], a[112:115]\n\t"
    "v_exp_f32_e32 v37, v37\n\t"
    "v_mfma_f32_16x16x32_bf16 v[176:179], v[192:195], v[64:67], v[176:179]\n\t"
    "v_exp_f32_e32 v38, v38\n\t"
    "v_mfma_f32_16x16x32_bf16 v[48:51], a[128:131], v[144:147], v[172:175]\n\t"
    "v_exp_f32_e32 v39, v39\n\t"
    "v_mfma_f32_16x16x32_bf16 v[48:51], a[132:135], v[148:151], v[48:51]\n\t"
    "v_exp_f32_e32 v40, v40\n\t"
    "v_mfma_f32_16x16x32_bf16 v[48:51], a[136:139], v[152:155], v[48:51]\n\t"
    "v_exp_f32_e32 v41, v41\n\t"
    "v_mfma_f32_16x16x32_bf16 v[48:51], a[140:143], v[156:159], v[48:51]\n\t"
    "v_exp_f32_e32 v42, v42\n\t"
    "v_mfma_f32_16x16x32_bf16 v[52:55], a[156:159], v[156:159], v[172:175]\n\t"
    "v_exp_f32_e32 v43, v43\n\t"
    "v_mfma_f32_16x16x32_bf16 v[52:55], a[152:155], v[152:155], v[52:55]\n\t"
    "v_exp_f32_e32 v44, v44\n\t"
    "v_mfma_f32_16x16x32_bf16 v[52:55], a[148:151], v[148:151], v[52:55]\n\t"
    "v_exp_f32_e32 v45, v45\n\t"
    "v_mfma_f32_16x16x32_bf16 v[52:55], a[144:147], v[144:147], v[52:55]\n\t"
    "v_exp_f32_e32 v46, v46\n\t"
    "v_mfma_f32_16x16x32_bf16 v[56:59], a[160:163], v[144:147], v[172:175]\n\t"
    "v_exp_f32_e32 v47, v47\n\t"
    "v_mfma_f32_16x16x32_bf16 v[56:59], a[164:167], v[148:151], v[56:59]\n\t"
    "v_cvt_pk_bf16_f32 v78, v28, v29\n\t"
    "v_cvt_pk_bf16_f32 v79, v30, v31\n\t"
    "v_mfma_f32_16x16x32_bf16 v[56:59], a[168:171], v[152:155], v[56:59]\n\t"
    "buffer_load_dwordx4 v210, s[8:11], s28 offen offset:2048 lds\n\t"
    "v_cvt_pk_bf16_f32 v80, v32, v33\n\t"
    "v_mfma_f32_16x16x32_bf16 v[56:59], a[172:175], v[156:159], v[56:59]\n\t"
    "ds_read_b128 a[128:131], v196 offset:16384\n\t"
    "v_cvt_pk_bf16_f32 v81, v34, v35\n\t"
    "v_mfma_f32_16x16x32_bf16 v[60:63], a[188:191], v[156:159], v[172:175]\n\t"
    "ds_read_b128 a[132:135], v197 offset:16384\n\t"
    "v_cvt_pk_bf16_f32 v82, v36, v37\n\t"
    "v_mfma_f32_16x16x32_bf16 v[60:63], a[184:187], v[152:155], v[60:63]\n\t"
    "ds_read_b128 a[136:139], v198 offset:16384\n\t"
    "v_cvt_pk_bf16_f32 v83, v38, v39\n\t"
    "v_mfma_f32_16x16x32_bf16 v[60:63], a[180:183], v[148:151], v[60:63]\n\t"
    "ds_read_b128 a[140:143], v199 offset:16384\n\t"
    "v_cvt_pk_bf16_f32 v84, v40, v41\n\t"
    "v_mfma_f32_16x16x32_bf16 v[60:63], a[176:179], v[144:147], v[60:63]\n\t"
    "ds_read_b128 a[156:159], v199 offset:20480\n\t"
    "v_cvt_pk_bf16_f32 v85, v42, v43\n\t"
    "v_mfma_f32_16x16x32_bf16 a[4:7], a[192:195], v[72:75], a[4:7]\n\t"
    "ds_read_b128 a[152:155], v198 offset:20480\n\t"
    "v_cvt_pk_bf16_f32 v86, v44, v45\n\t"
    "v_mfma_f32_16x16x32_bf16 a[4:7], a[196:199], v[76:79], a[4:7]\n\t"
    "buffer_load_dwordx4 v211, s[8:11], s28 offen offset:3072 lds\n\t"
    "v_cvt_pk_bf16_f32 v87, v46, v47\n\t"
    "v_mfma_f32_16x16x32_bf16 a[20:23], a[200:203], v[72:75], a[20:23]\n\t"
    "ds_read_b128 a[148:151], v197 offset:20480\n\t"
    "v_add_f32_e32 v215, v215, v25\n\t"
    "v_mfma_f32_16x16x32_bf16 a[20:23], a[204:207], v[76:79], a[20:23]\n\t"
    "ds_read_b128 a[144:147], v196 offset:20480\n\t"
    "v_add_f32_e32 v215, v215, v26\n\t"
    "v_mfma_f32_16x16x32_bf16 a[36:39], a[208:211], v[72:75], a[36:39]\n\t"
    "ds_read_b128 a[160:163], v196 offset:24576\n\t"
    "v_add_f32_e32 v215, v215, v27\n\t"
    "v_mfma_f32_16x16x32_bf16 a[36:39], a[212:215], v[76:79], a[36:39]\n\t"
    "ds_read_b128 a[164:167], v197 offset:24576\n\t"
    "v_add_f32_e32 v215, v215, v28\n\t"
    "v_mfma_f32_16x16x32_bf16 a[52:55], a[216:219], v[72:75], a[52:55]\n\t"
    "ds_read_b128 a[168:171], v198 offset:24576\n\t"
    "v_add_f32_e32 v215, v215, v29\n\t"
    "v_mfma_f32_16x16x32_bf16 a[52:55], a[220:223], v[76:79], a[52:55]\n\t"
    "ds_read_b128 a[172:175], v199 offset:24576\n\t"
    "v_add_f32_e32 v215, v215, v30\n\t"
    "v_mfma_f32_16x16x32_bf16 a[68:71], a[224:227], v[72:75], a[68:71]\n\t"
    "ds_read_b128 a[188:191], v199 offset:28672\n\t"
    "v_add_f32_e32 v215, v215, v31\n\t"
    "v_mfma_f32_16x16x32_bf16 a[68:71], a[228:231], v[76:79], a[68:71]\n\t"
    "s_mov_b32 m0, s38\n\t"
    "v_add_f32_e32 v216, v216, v40\n\t"
    "v_mfma_f32_16x16x32_bf16 a[84:87], a[232:235], v[72:75], a[84:87]\n\t"
    "ds_read_b128 a[184:187], v198 offset:28672\n\t"
    "v_add_f32_e32 v216, v216, v41\n\t"
    "v_mfma_f32_16x16x32_bf16 a[84:87], a[236:239], v[76:79], a[84:87]\n\t"
    "ds_read_b128 a[180:183], v197 offset:28672\n\t"
    "v_add_f32_e32 v216, v216, v42\n\t"
    "v_mfma_f32_16x16x32_bf16 a[100:103], a[240:243], v[72:75], a[100:103]\n\t"
    "ds_read_b128 a[176:179], v196 offset:28672\n\t"
    "v_add_f32_e32 v216, v216, v43\n\t"
    "v_mfma_f32_16x16x32_bf16 a[100:103], a[244:247], v[76:79], a[100:103]\n\t"
    "buffer_load_dwordx4 v212, s[12:15], s28 offen lds\n\t"
    "v_add_f32_e32 v216, v216, v44\n\t"
    "v_mfma_f32_16x16x32_bf16 a[116:119], a[248:251], v[72:75], a[116:119]\n\t"
    "v_exp_f32_e32 v48, v48\n\t"
    "v_mfma_f32_16x16x32_bf16 a[116:119], a[252:255], v[76:79], a[116:119]\n\t"
    "v_exp_f32_e32 v49, v49\n\t"
    "v_mfma_f32_16x16x32_bf16 v[180:183], v[192:195], v[72:75], v[180:183]\n\t"
    "v_cvt_pk_bf16_f32 v88, v48, v49\n\t"
    "s_waitcnt lgkmcnt(8)\n\t"
    "v_mfma_f32_16x16x32_bf16 v[0:3], a[128:131], v[96:99], v[160:163]\n\t"
    "v_exp_f32_e32 v50, v50\n\t"
    "v_mfma_f32_16x16x32_bf16 v[0:3], a[132:135], v[100:103], v[0:3]\n\t"
    "v_exp_f32_e32 v51, v51\n\t"
    "v_mfma_f32_16x16x32_bf16 v[0:3], a[136:139], v[104:107], v[0:3]\n\t"
    "v_exp_f32_e32 v52, v52\n\t"
    "v_mfma_f32_16x16x32_bf16 v[0:3], a[140:143], v[108:111], v[0:3]\n\t"
    "v_exp_f32_e32 v53, v53\n\t"
    "v_mfma_f32_16x16x32_bf16 v[4:7], a[156:159], v[108:111], v[160:163]\n\t"
    "buffer_load_dwordx4 v213, s[12:15], s28 offen offset:1024 lds\n\t"
    "v_cvt_pk_bf16_f32 v89, v50, v51\n\t"
    "v_mfma_f32_16x16x32_bf16 v[4:7], a[152:155], v[104:107], v[4:7]\n\t"
    "v_exp_f32_e32 v54, v54\n\t"
    "v_mfma_f32_16x16x32_bf16 v[4:7], a[148:151], v[100:103], v[4:7]\n\t"
    "v_exp_f32_e32 v55, v55\n\t"
    "v_mfma_f32_16x16x32_bf16 v[4:7], a[144:147], v[96:99], v[4:7]\n\t"
    "v_cvt_pk_bf16_f32 v90, v52, v53\n\t"
    "s_waitcnt lgkmcnt(0)\n\t"
    "v_mfma_f32_16x16x32_bf16 v[8:11], a[160:163], v[96:99], v[160:163]\n\t"
    "v_exp_f32_e32 v56, v56\n\t"
    "v_mfma_f32_16x16x32_bf16 v[8:11], a[164:167], v[100:103], v[8:11]\n\t"
    "v_exp_f32_e32 v57, v57\n\t"
    "v_mfma_f32_16x16x32_bf16 v[8:11], a[168:171], v[104:107], v[8:11]\n\t"
    "v_exp_f32_e32 v58, v58\n\t"
    "v_mfma_f32_16x16x32_bf16 v[8:11], a[172:175], v[108:111], v[8:11]\n\t"
    "v_exp_f32_e32 v59, v59\n\t"
    "v_mfma_f32_16x16x32_bf16 v[12:15], a[188:191], v[108:111], v[160:163]\n\t"
    "v_exp_f32_e32 v60, v60\n\t"
    "v_mfma_f32_16x16x32_bf16 v[12:15], a[184:187], v[104:107], v[12:15]\n\t"
    "v_exp_f32_e32 v61, v61\n\t"
    "v_mfma_f32_16x16x32_bf16 v[12:15], a[180:183], v[100:103], v[12:15]\n\t"
    "buffer_load_dwordx4 v212, s[12:15], s28 offen offset:2048 lds\n\t"
    "v_cvt_pk_bf16_f32 v91, v54, v55\n\t"
    "v_mfma_f32_16x16x32_bf16 v[12:15], a[176:179], v[96:99], v[12:15]\n\t"
    "v_exp_f32_e32 v62, v62\n\t"
    "v_mfma_f32_16x16x32_bf16 a[8:11], a[192:195], v[80:83], a[8:11]\n\t"
    "v_exp_f32_e32 v63, v63\n\t"
    "v_mfma_f32_16x16x32_bf16 a[8:11], a[196:199], v[84:87], a[8:11]\n\t"
    "v_exp_f32_e32 v0, v0\n\t"
    "v_mfma_f32_16x16x32_bf16 a[24:27], a[200:203], v[80:83], a[24:27]\n\t"
    "v_exp_f32_e32 v1, v1\n\t"
    "v_mfma_f32_16x16x32_bf16 a[24:27], a[204:207], v[84:87], a[24:27]\n\t"
    "v_exp_f32_e32 v2, v2\n\t"
    "v_mfma_f32_16x16x32_bf16 a[40:43], a[208:211], v[80:83], a[40:43]\n\t"
    "v_exp_f32_e32 v3, v3\n\t"
    "v_mfma_f32_16x16x32_bf16 a[40:43], a[212:215], v[84:87], a[40:43]\n\t"
    "v_exp_f32_e32 v4, v4\n\t"
    "v_mfma_f32_16x16x32_bf16 a[56:59], a[216:219], v[80:83], a[56:59]\n\t"
    "v_exp_f32_e32 v5, v5\n\t"
    "v_mfma_f32_16x16x32_bf16 a[56:59], a[220:223], v[84:87], a[56:59]\n\t"
    "v_exp_f32_e32 v6, v6\n\t"
    "v_mfma_f32_16x16x32_bf16 a[72:75], a[224:227], v[80:83], a[72:75]\n\t"
    "buffer_load_dwordx4 v213, s[12:15], s28 offen offset:3072 lds\n\t"
    "v_cvt_pk_bf16_f32 v92, v56, v57\n\t"
    "v_mfma_f32_16x16x32_bf16 a[72:75], a[228:231], v[84:87], a[72:75]\n\t"
    "v_exp_f32_e32 v7, v7\n\t"
    "v_mfma_f32_16x16x32_bf16 a[88:91], a[232:235], v[80:83], a[88:91]\n\t"
    "v_exp_f32_e32 v8, v8\n\t"
    "v_mfma_f32_16x16x32_bf16 a[88:91], a[236:239], v[84:87], a[88:91]\n\t"
    "v_exp_f32_e32 v9, v9\n\t"
    "v_mfma_f32_16x16x32_bf16 a[104:107], a[240:243], v[80:83], a[104:107]\n\t"
    "v_exp_f32_e32 v10, v10\n\t"
    "v_mfma_f32_16x16x32_bf16 a[104:107], a[244:247], v[84:87], a[104:107]\n\t"
    "v_exp_f32_e32 v11, v11\n\t"
    "v_mfma_f32_16x16x32_bf16 a[120:123], a[248:251], v[80:83], a[120:123]\n\t"
    "v_exp_f32_e32 v12, v12\n\t"
    "v_mfma_f32_16x16x32_bf16 a[120:123], a[252:255], v[84:87], a[120:123]\n\t"
    "v_exp_f32_e32 v13, v13\n\t"
    "v_mfma_f32_16x16x32_bf16 v[184:187], v[192:195], v[80:83], v[184:187]\n\t"
    "v_exp_f32_e32 v14, v14\n\t"
    "v_mfma_f32_16x16x32_bf16 v[16:19], a[128:131], v[112:115], v[164:167]\n\t"
    "v_exp_f32_e32 v15, v15\n\t"
    "v_mfma_f32_16x16x32_bf16 v[16:19], a[132:135], v[116:119], v[16:19]\n\t"
    "s_mov_b32 m0, s49\n\t"
    "v_cvt_pk_bf16_f32 v93, v58, v59\n\t"
    "v_mfma_f32_16x16x32_bf16 v[16:19], a[136:139], v[120:123], v[16:19]\n\t"
    "buffer_load_dwordx4 v232, s[44:47], s48 offen lds\n\t"
    "v_cvt_pk_bf16_f32 v94, v60, v61\n\t"
    "v_mfma_f32_16x16x32_bf16 v[16:19], a[140:143], v[124:127], v[16:19]\n\t"
    "v_cvt_pk_bf16_f32 v95, v62, v63\n\t"
    "s_add_u32 s48, s48, 1024\n\t"
    "v_mfma_f32_16x16x32_bf16 v[20:23], a[156:159], v[124:127], v[164:167]\n\t"
    "s_add_u32 s28, s28, 16384\n\t"
    "v_add_f32_e32 v216, v216, v45\n\t"
    "v_mfma_f32_16x16x32_bf16 v[20:23], a[152:155], v[120:123], v[20:23]\n\t"
    "v_add_f32_e32 v216, v216, v46\n\t"
    "v_add_f32_e32 v216, v216, v47\n\t"
    "v_mfma_f32_16x16x32_bf16 v[20:23], a[148:151], v[116:119], v[20:23]\n\t"
    "v_cvt_pk_bf16_f32 v64, v0, v1\n\t"
    "v_cvt_pk_bf16_f32 v65, v2, v3\n\t"
    "v_mfma_f32_16x16x32_bf16 v[20:23], a[144:147], v[112:115], v[20:23]\n\t"
    "v_exp_f32_e32 v16, v16\n\t"
    "v_mfma_f32_16x16x32_bf16 v[24:27], a[160:163], v[112:115], v[164:167]\n\t"
    "v_exp_f32_e32 v17, v17\n\t"
    "v_mfma_f32_16x16x32_bf16 v[24:27], a[164:167], v[116:119], v[24:27]\n\t"
    "v_exp_f32_e32 v18, v18\n\t"
    "v_mfma_f32_16x16x32_bf16 v[24:27], a[168:171], v[120:123], v[24:27]\n\t"
    "v_exp_f32_e32 v19, v19\n\t"
    "v_mfma_f32_16x16x32_bf16 v[24:27], a[172:175], v[124:127], v[24:27]\n\t"
    "v_exp_f32_e32 v20, v20\n\t"
    "v_mfma_f32_16x16x32_bf16 v[28:31], a[188:191], v[124:127], v[164:167]\n\t"
    "v_exp_f32_e32 v21, v21\n\t"
    "v_mfma_f32_16x16x32_bf16 v[28:31], a[184:187], v[120:123], v[28:31]\n\t"
    "v_exp_f32_e32 v22, v22\n\t"
    "v_mfma_f32_16x16x32_bf16 v[28:31], a[180:183], v[116:119], v[28:31]\n\t"
    "v_exp_f32_e32 v23, v23\n\t"
    "v_mfma_f32_16x16x32_bf16 v[28:31], a[176:179], v[112:115], v[28:31]\n\t"
    "v_exp_f32_e32 v24, v24\n\t"
    "v_mfma_f32_16x16x32_bf16 a[12:15], a[192:195], v[88:91], a[12:15]\n\t"
    "s_waitcnt vmcnt(9)\n\t"
    "s_barrier\n\t"
    "v_mfma_f32_16x16x32_bf16 a[12:15], a[196:199], v[92:95], a[12:15]\n\t"
    "ds_read_b64_tr_b16 a[192:193], v200 offset:16384\n\t"
    "v_cvt_pk_bf16_f32 v66, v4, v5\n\t"
    "v_mfma_f32_16x16x32_bf16 a[28:31], a[200:203], v[88:91], a[28:31]\n\t"
    "ds_read_b64_tr_b16 a[194:195], v200 offset:20480\n\t"
    "v_cvt_pk_bf16_f32 v67, v6, v7\n\t"
    "v_mfma_f32_16x16x32_bf16 a[28:31], a[204:207], v[92:95], a[28:31]\n\t"
    "ds_read_b64_tr_b16 a[196:197], v200 offset:24576\n\t"
    "v_cvt_pk_bf16_f32 v68, v8, v9\n\t"
    "v_mfma_f32_16x16x32_bf16 a[44:47], a[208:211], v[88:91], a[44:47]\n\t"
    "ds_read_b64_tr_b16 a[198:199], v200 offset:28672\n\t"
    "v_cvt_pk_bf16_f32 v69, v10, v11\n\t"
    "v_mfma_f32_16x16x32_bf16 a[44:47], a[212:215], v[92:95], a[44:47]\n\t"
    "ds_read_b64_tr_b16 a[200:201], v201 offset:16384\n\t"
    "v_cvt_pk_bf16_f32 v70, v12, v13\n\t"
    "v_mfma_f32_16x16x32_bf16 a[60:63], a[216:219], v[88:91], a[60:63]\n\t"
    "ds_read_b64_tr_b16 a[202:203], v201 offset:20480\n\t"
    "v_cvt_pk_bf16_f32 v71, v14, v15\n\t"
    "v_mfma_f32_16x16x32_bf16 a[60:63], a[220:223], v[92:95], a[60:63]\n\t"
    "ds_read_b64_tr_b16 a[204:205], v201 offset:24576\n\t"
    "v_add_f32_e32 v217, v217, v56\n\t"
    "v_mfma_f32_16x16x32_bf16 a[76:79], a[224:227], v[88:91], a[76:79]\n\t"
    "ds_read_b64_tr_b16 a[206:207], v201 offset:28672\n\t"
    "v_add_f32_e32 v217, v217, v57\n\t"
    "v_mfma_f32_16x16x32_bf16 a[76:79], a[228:231], v[92:95], a[76:79]\n\t"
    "ds_read_b64_tr_b16 a[208:209], v202 offset:16384\n\t"
    "v_add_f32_e32 v217, v217, v58\n\t"
    "v_mfma_f32_16x16x32_bf16 a[92:95], a[232:235], v[88:91], a[92:95]\n\t"
    "ds_read_b64_tr_b16 a[210:211], v202 offset:20480\n\t"
    "v_add_f32_e32 v217, v217, v59\n\t"
    "v_mfma_f32_16x16x32_bf16 a[92:95], a[236:239], v[92:95], a[92:95]\n\t"
    "ds_read_b64_tr_b16 a[212:213], v202 offset:24576\n\t"
    "v_add_f32_e32 v217, v217, v60\n\t"
    "v_mfma_f32_16x16x32_bf16 a[108:111], a[240:243], v[88:91], a[108:111]\n\t"
    "ds_read_b64_tr_b16 a[214:215], v202 offset:28672\n\t"
    "v_add_f32_e32 v217, v217, v61\n\t"
    "v_mfma_f32_16x16x32_bf16 a[108:111], a[244:247], v[92:95], a[108:111]\n\t"
    "ds_read_b64_tr_b16 a[216:217], v203 offset:16384\n\t"
    "v_add_f32_e32 v217, v217, v62\n\t"
    "v_mfma_f32_16x16x32_bf16 a[124:127], a[248:251], v[88:91], a[124:127]\n\t"
    "ds_read_b64_tr_b16 a[218:219], v203 offset:20480\n\t"
    "v_add_f32_e32 v217, v217, v63\n\t"
    "v_mfma_f32_16x16x32_bf16 a[124:127], a[252:255], v[92:95], a[124:127]\n\t"
    "ds_read_b64_tr_b16 a[220:221], v203 offset:24576\n\t"
    "v_cvt_pk_bf16_f32 v72, v16, v17\n\t"
    "v_mfma_f32_16x16x32_bf16 v[188:191], v[192:195], v[88:91], v[188:191]\n\t"
    "ds_read_b64_tr_b16 a[222:223], v203 offset:28672\n\t"
    "v_cvt_pk_bf16_f32 v73, v18, v19\n\t"
    "v_mfma_f32_16x16x32_bf16 v[32:35], a[128:131], v[128:131], v[168:171]\n\t"
    "ds_read_b64_tr_b16 a[224:225], v204 offset:16384\n\t"
    "v_cvt_pk_bf16_f32 v74, v20, v21\n\t"
    "v_mfma_f32_16x16x32_bf16 v[32:35], a[132:135], v[132:135], v[32:35]\n\t"
    "ds_read_b64_tr_b16 a[226:227], v204 offset:20480\n\t"
    "v_cvt_pk_bf16_f32 v75, v22, v23\n\t"
    "v_mfma_f32_16x16x32_bf16 v[32:35], a[136:139], v[136:139], v[32:35]\n\t"
    "ds_read_b64_tr_b16 a[228:229], v204 offset:24576\n\t"
    "v_add_f32_e32 v214, v214, v8\n\t"
    "v_mfma_f32_16x16x32_bf16 v[32:35], a[140:143], v[140:143], v[32:35]\n\t"
    "ds_read_b64_tr_b16 a[230:231], v204 offset:28672\n\t"
    "v_add_f32_e32 v214, v214, v9\n\t"
    "v_mfma_f32_16x16x32_bf16 v[36:39], a[156:159], v[140:143], v[168:171]\n\t"
    "ds_read_b64_tr_b16 a[232:233], v205 offset:16384\n\t"
    "v_add_f32_e32 v214, v214, v10\n\t"
    "v_mfma_f32_16x16x32_bf16 v[36:39], a[152:155], v[136:139], v[36:39]\n\t"
    "ds_read_b64_tr_b16 a[234:235], v205 offset:20480\n\t"
    "v_add_f32_e32 v214, v214, v11\n\t"
    "v_mfma_f32_16x16x32_bf16 v[36:39], a[148:151], v[132:135], v[36:39]\n\t"
    "ds_read_b64_tr_b16 a[236:237], v205 offset:24576\n\t"
    "v_add_f32_e32 v214, v214, v12\n\t"
    "v_mfma_f32_16x16x32_bf16 v[36:39], a[144:147], v[128:131], v[36:39]\n\t"
    "ds_read_b64_tr_b16 a[238:239], v205 offset:28672\n\t"
    "v_add_f32_e32 v214, v214, v13\n\t"
    "v_mfma_f32_16x16x32_bf16 v[40:43], a[160:163], v[128:131], v[168:171]\n\t"
    "ds_read_b64_tr_b16 a[240:241], v206 offset:16384\n\t"
    "v_add_f32_e32 v214, v214, v14\n\t"
    "v_mfma_f32_16x16x32_bf16 v[40:43], a[164:167], v[132:135], v[40:43]\n\t"
    "ds_read_b64_tr_b16 a[242:243], v206 offset:20480\n\t"
    "v_add_f32_e32 v214, v214, v15\n\t"
    "v_mfma_f32_16x16x32_bf16 v[40:43], a[168:171], v[136:139], v[40:43]\n\t"
    "ds_read_b64_tr_b16 a[244:245], v206 offset:24576\n\t"
    "v_add_f32_e32 v215, v215, v24\n\t"
    "v_mfma_f32_16x16x32_bf16 v[40:43], a[172:175], v[140:143], v[40:43]\n\t"
    "ds_read_b64_tr_b16 a[246:247], v206 offset:28672\n\t"
    "v_mfma_f32_16x16x32_bf16 v[44:47], a[188:191], v[140:143], v[168:171]\n\t"
    "ds_read_b64_tr_b16 a[248:249], v207 offset:16384\n\t"
    "v_mfma_f32_16x16x32_bf16 v[44:47], a[184:187], v[136:139], v[44:47]\n\t"
    "ds_read_b64_tr_b16 a[250:251], v207 offset:20480\n\t"
    "v_mfma_f32_16x16x32_bf16 v[44:47], a[180:183], v[132:135], v[44:47]\n\t"
    "ds_read_b64_tr_b16 a[252:253], v207 offset:24576\n\t"
    "v_mfma_f32_16x16x32_bf16 v[44:47], a[176:179], v[128:131], v[44:47]\n\t"
    "ds_read_b64_tr_b16 a[254:255], v207 offset:28672\n\t"
    "s_waitcnt lgkmcnt(15)\n\t"
    "v_mfma_f32_16x16x32_bf16 a[0:3], a[192:195], v[64:67], a[0:3]\n\t"
    "v_exp_f32_e32 v25, v25\n\t"
    "v_mfma_f32_16x16x32_bf16 a[0:3], a[196:199], v[68:71], a[0:3]\n\t"
    "v_exp_f32_e32 v26, v26\n\t"
    "v_mfma_f32_16x16x32_bf16 a[16:19], a[200:203], v[64:67], a[16:19]\n\t"
    "v_exp_f32_e32 v27, v27\n\t"
    "v_mfma_f32_16x16x32_bf16 a[16:19], a[204:207], v[68:71], a[16:19]\n\t"
    "v_exp_f32_e32 v28, v28\n\t"
    "v_mfma_f32_16x16x32_bf16 a[32:35], a[208:211], v[64:67], a[32:35]\n\t"
    "v_exp_f32_e32 v29, v29\n\t"
    "v_mfma_f32_16x16x32_bf16 a[32:35], a[212:215], v[68:71], a[32:35]\n\t"
    "v_exp_f32_e32 v30, v30\n\t"
    "v_mfma_f32_16x16x32_bf16 a[48:51], a[216:219], v[64:67], a[48:51]\n\t"
    "v_exp_f32_e32 v31, v31\n\t"
    "v_mfma_f32_16x16x32_bf16 a[48:51], a[220:223], v[68:71], a[48:51]\n\t"
    "s_mov_b32 m0, s32\n\t"
    "s_waitcnt lgkmcnt(0)\n\t"
    "v_mfma_f32_16x16x32_bf16 a[64:67], a[224:227], v[64:67], a[64:67]\n\t"
    "buffer_load_dwordx4 v208, s[8:11], s28 offen lds\n\t"
    "v_cvt_pk_bf16_f32 v76, v24, v25\n\t"
    "v_mfma_f32_16x16x32_bf16 a[64:67], a[228:231], v[68:71], a[64:67]\n\t"
    "v_exp_f32_e32 v32, v32\n\t"
    "v_mfma_f32_16x16x32_bf16 a[80:83], a[232:235], v[64:67], a[80:83]\n\t"
    "v_exp_f32_e32 v33, v33\n\t"
    "v_mfma_f32_16x16x32_bf16 a[80:83], a[236:239], v[68:71], a[80:83]\n\t"
    "v_exp_f32_e32 v34, v34\n\t"
    "v_mfma_f32_16x16x32_bf16 a[96:99], a[240:243], v[64:67], a[96:99]\n\t"
    "v_exp_f32_e32 v35, v35\n\t"
    "v_mfma_f32_16x16x32_bf16 a[96:99], a[244:247], v[68:71], a[96:99]\n\t"
    "v_exp_f32_e32 v36, v36\n\t"
    "v_mfma_f32_16x16x32_bf16 a[112:115], a[248:251], v[64:67], a[112:115]\n\t"
    "buffer_load_dwordx4 v209, s[8:11], s28 offen offset:1024 lds\n\t"
    "v_cvt_pk_bf16_f32 v77, v26, v27\n\t"
    "v_mfma_f32_16x16x32_bf16 a[112:115], a[252:255], v[68:71], a[112:115]\n\t"
    "v_exp_f32_e32 v37, v37\n\t"
    "v_mfma_f32_16x16x32_bf16 v[176:179], v[192:195], v[64:67], v[176:179]\n\t"
    "v_exp_f32_e32 v38, v38\n\t"
    "v_mfma_f32_16x16x32_bf16 v[48:51], a[128:131], v[144:147], v[172:175]\n\t"
    "v_exp_f32_e32 v39, v39\n\t"
    "v_mfma_f32_16x16x32_bf16 v[48:51], a[132:135], v[148:151], v[48:51]\n\t"
    "v_exp_f32_e32 v40, v40\n\t"
    "v_mfma_f32_16x16x32_bf16 v[48:51], a[136:139], v[152:155], v[48:51]\n\t"
    "v_exp_f32_e32 v41, v41\n\t"
    "v_mfma_f32_16x16x32_bf16 v[48:51], a[140:143], v[156:159], v[48:51]\n\t"
    "v_exp_f32_e32 v42, v42\n\t"
    "v_mfma_f32_16x16x32_bf16 v[52:55], a[156:159], v[156:159], v[172:175]\n\t"
    "v_exp_f32_e32 v43, v43\n\t"
    "v_mfma_f32_16x16x32_bf16 v[52:55], a[152:155], v[152:155], v[52:55]\n\t"
    "v_exp_f32_e32 v44, v44\n\t"
    "v_mfma_f32_16x16x32_bf16 v[52:55], a[148:151], v[148:151], v[52:55]\n\t"
    "v_exp_f32_e32 v45, v45\n\t"
    "v_mfma_f32_16x16x32_bf16 v[52:55], a[144:147], v[144:147], v[52:55]\n\t"
    "v_exp_f32_e32 v46, v46\n\t"
    "v_mfma_f32_16x16x32_bf16 v[56:59], a[160:163], v[144:147], v[172:175]\n\t"
    "v_exp_f32_e32 v47, v47\n\t"
    "v_mfma_f32_16x16x32_bf16 v[56:59], a[164:167], v[148:151], v[56:59]\n\t"
    "v_cvt_pk_bf16_f32 v78, v28, v29\n\t"
    "v_cvt_pk_bf16_f32 v79, v30, v31\n\t"
    "v_mfma_f32_16x16x32_bf16 v[56:59], a[168:171], v[152:155], v[56:59]\n\t"
    "buffer_load_dwordx4 v210, s[8:11], s28 offen offset:2048 lds\n\t"
    "v_cvt_pk_bf16_f32 v80, v32, v33\n\t"
    "v_mfma_f32_16x16x32_bf16 v[56:59], a[172:175], v[156:159], v[56:59]\n\t"
    "ds_read_b128 a[128:131], v196 offset:32768\n\t"
    "v_cvt_pk_bf16_f32 v81, v34, v35\n\t"
    "v_mfma_f32_16x16x32_bf16 v[60:63], a[188:191], v[156:159], v[172:175]\n\t"
    "ds_read_b128 a[132:135], v197 offset:32768\n\t"
    "v_cvt_pk_bf16_f32 v82, v36, v37\n\t"
    "v_mfma_f32_16x16x32_bf16 v[60:63], a[184:187], v[152:155], v[60:63]\n\t"
    "ds_read_b128 a[136:139], v198 offset:32768\n\t"
    "v_cvt_pk_bf16_f32 v83, v38, v39\n\t"
    "v_mfma_f32_16x16x32_bf16 v[60:63], a[180:183], v[148:151], v[60:63]\n\t"
    "ds_read_b128 a[140:143], v199 offset:32768\n\t"
    "v_cvt_pk_bf16_f32 v84, v40, v41\n\t"
    "v_mfma_f32_16x16x32_bf16 v[60:63], a[176:179], v[144:147], v[60:63]\n\t"
    "ds_read_b128 a[156:159], v199 offset:36864\n\t"
    "v_cvt_pk_bf16_f32 v85, v42, v43\n\t"
    "v_mfma_f32_16x16x32_bf16 a[4:7], a[192:195], v[72:75], a[4:7]\n\t"
    "ds_read_b128 a[152:155], v198 offset:36864\n\t"
    "v_cvt_pk_bf16_f32 v86, v44, v45\n\t"
    "v_mfma_f32_16x16x32_bf16 a[4:7], a[196:199], v[76:79], a[4:7]\n\t"
    "buffer_load_dwordx4 v211, s[8:11], s28 offen offset:3072 lds\n\t"
    "v_cvt_pk_bf16_f32 v87, v46, v47\n\t"
    "v_mfma_f32_16x16x32_bf16 a[20:23], a[200:203], v[72:75], a[20:23]\n\t"
    "ds_read_b128 a[148:151], v197 offset:36864\n\t"
    "v_add_f32_e32 v215, v215, v25\n\t"
    "v_mfma_f32_16x16x32_bf16 a[20:23], a[204:207], v[76:79], a[20:23]\n\t"
    "ds_read_b128 a[144:147], v196 offset:36864\n\t"
    "v_add_f32_e32 v215, v215, v26\n\t"
    "v_mfma_f32_16x16x32_bf16 a[36:39], a[208:211], v[72:75], a[36:39]\n\t"
    "ds_read_b128 a[160:163], v196 offset:40960\n\t"
    "v_add_f32_e32 v215, v215, v27\n\t"
    "v_mfma_f32_16x16x32_bf16 a[36:39], a[212:215], v[76:79], a[36:39]\n\t"
    "ds_read_b128 a[164:167], v197 offset:40960\n\t"
    "v_add_f32_e32 v215, v215, v28\n\t"
    "v_mfma_f32_16x16x32_bf16 a[52:55], a[216:219], v[72:75], a[52:55]\n\t"
    "ds_read_b128 a[168:171], v198 offset:40960\n\t"
    "v_add_f32_e32 v215, v215, v29\n\t"
    "v_mfma_f32_16x16x32_bf16 a[52:55], a[220:223], v[76:79], a[52:55]\n\t"
    "ds_read_b128 a[172:175], v199 offset:40960\n\t"
    "v_add_f32_e32 v215, v215, v30\n\t"
    "v_mfma_f32_16x16x32_bf16 a[68:71], a[224:227], v[72:75], a[68:71]\n\t"
    "ds_read_b128 a[188:191], v199 offset:45056\n\t"
    "v_add_f32_e32 v215, v215, v31\n\t"
    "v_mfma_f32_16x16x32_bf16 a[68:71], a[228:231], v[76:79], a[68:71]\n\t"
    "s_mov_b32 m0, s39\n\t"
    "v_add_f32_e32 v216, v216, v40\n\t"
    "v_mfma_f32_16x16x32_bf16 a[84:87], a[232:235], v[72:75], a[84:87]\n\t"
    "ds_read_b128 a[184:187], v198 offset:45056\n\t"
    "v_add_f32_e32 v216, v216, v41\n\t"
    "v_mfma_f32_16x16x32_bf16 a[84:87], a[236:239], v[76:79], a[84:87]\n\t"
    "ds_read_b128 a[180:183], v197 offset:45056\n\t"
    "v_add_f32_e32 v216, v216, v42\n\t"
    "v_mfma_f32_16x16x32_bf16 a[100:103], a[240:243], v[72:75], a[100:103]\n\t"
    "ds_read_b128 a[176:179], v196 offset:45056\n\t"
    "v_add_f32_e32 v216, v216, v43\n\t"
    "v_mfma_f32_16x16x32_bf16 a[100:103], a[244:247], v[76:79], a[100:103]\n\t"
    "buffer_load_dwordx4 v212, s[12:15], s28 offen lds\n\t"
    "v_add_f32_e32 v216, v216, v44\n\t"
    "v_mfma_f32_16x16x32_bf16 a[116:119], a[248:251], v[72:75], a[116:119]\n\t"
    "v_exp_f32_e32 v48, v48\n\t"
    "v_mfma_f32_16x16x32_bf16 a[116:119], a[252:255], v[76:79], a[116:119]\n\t"
    "v_exp_f32_e32 v49, v49\n\t"
    "v_mfma_f32_16x16x32_bf16 v[180:183], v[192:195], v[72:75], v[180:183]\n\t"
    "v_cvt_pk_bf16_f32 v88, v48, v49\n\t"
    "s_sub_u32 s52, s52, 1\n\t"
    "s_cmp_lg_u32 s52, 0\n\t"
    "s_cbranch_scc1 Lattn_loop_%=\n\t"
    "Lcode_tail_begin:\n\t"
    "s_waitcnt lgkmcnt(8)\n\t"
    "v_mfma_f32_16x16x32_bf16 v[0:3], a[128:131], v[96:99], v[160:163]\n\t"
    "v_exp_f32_e32 v50, v50\n\t"
    "v_mfma_f32_16x16x32_bf16 v[0:3], a[132:135], v[100:103], v[0:3]\n\t"
    "v_exp_f32_e32 v51, v51\n\t"
    "v_mfma_f32_16x16x32_bf16 v[0:3], a[136:139], v[104:107], v[0:3]\n\t"
    "v_exp_f32_e32 v52, v52\n\t"
    "v_mfma_f32_16x16x32_bf16 v[0:3], a[140:143], v[108:111], v[0:3]\n\t"
    "v_exp_f32_e32 v53, v53\n\t"
    "v_mfma_f32_16x16x32_bf16 v[4:7], a[156:159], v[108:111], v[160:163]\n\t"
    "buffer_load_dwordx4 v213, s[12:15], s28 offen offset:1024 lds\n\t"
    "v_cvt_pk_bf16_f32 v89, v50, v51\n\t"
    "v_mfma_f32_16x16x32_bf16 v[4:7], a[152:155], v[104:107], v[4:7]\n\t"
    "v_exp_f32_e32 v54, v54\n\t"
    "v_mfma_f32_16x16x32_bf16 v[4:7], a[148:151], v[100:103], v[4:7]\n\t"
    "v_exp_f32_e32 v55, v55\n\t"
    "v_mfma_f32_16x16x32_bf16 v[4:7], a[144:147], v[96:99], v[4:7]\n\t"
    "v_cvt_pk_bf16_f32 v90, v52, v53\n\t"
    "s_waitcnt lgkmcnt(0)\n\t"
    "v_mfma_f32_16x16x32_bf16 v[8:11], a[160:163], v[96:99], v[160:163]\n\t"
    "v_exp_f32_e32 v56, v56\n\t"
    "v_mfma_f32_16x16x32_bf16 v[8:11], a[164:167], v[100:103], v[8:11]\n\t"
    "v_exp_f32_e32 v57, v57\n\t"
    "v_mfma_f32_16x16x32_bf16 v[8:11], a[168:171], v[104:107], v[8:11]\n\t"
    "v_exp_f32_e32 v58, v58\n\t"
    "v_mfma_f32_16x16x32_bf16 v[8:11], a[172:175], v[108:111], v[8:11]\n\t"
    "v_exp_f32_e32 v59, v59\n\t"
    "v_mfma_f32_16x16x32_bf16 v[12:15], a[188:191], v[108:111], v[160:163]\n\t"
    "v_exp_f32_e32 v60, v60\n\t"
    "v_mfma_f32_16x16x32_bf16 v[12:15], a[184:187], v[104:107], v[12:15]\n\t"
    "v_exp_f32_e32 v61, v61\n\t"
    "v_mfma_f32_16x16x32_bf16 v[12:15], a[180:183], v[100:103], v[12:15]\n\t"
    "buffer_load_dwordx4 v212, s[12:15], s28 offen offset:2048 lds\n\t"
    "v_cvt_pk_bf16_f32 v91, v54, v55\n\t"
    "v_mfma_f32_16x16x32_bf16 v[12:15], a[176:179], v[96:99], v[12:15]\n\t"
    "v_exp_f32_e32 v62, v62\n\t"
    "v_mfma_f32_16x16x32_bf16 a[8:11], a[192:195], v[80:83], a[8:11]\n\t"
    "v_exp_f32_e32 v63, v63\n\t"
    "v_mfma_f32_16x16x32_bf16 a[8:11], a[196:199], v[84:87], a[8:11]\n\t"
    "v_exp_f32_e32 v0, v0\n\t"
    "v_mfma_f32_16x16x32_bf16 a[24:27], a[200:203], v[80:83], a[24:27]\n\t"
    "v_exp_f32_e32 v1, v1\n\t"
    "v_mfma_f32_16x16x32_bf16 a[24:27], a[204:207], v[84:87], a[24:27]\n\t"
    "v_exp_f32_e32 v2, v2\n\t"
    "v_mfma_f32_16x16x32_bf16 a[40:43], a[208:211], v[80:83], a[40:43]\n\t"
    "v_exp_f32_e32 v3, v3\n\t"
    "v_mfma_f32_16x16x32_bf16 a[40:43], a[212:215], v[84:87], a[40:43]\n\t"
    "v_exp_f32_e32 v4, v4\n\t"
    "v_mfma_f32_16x16x32_bf16 a[56:59], a[216:219], v[80:83], a[56:59]\n\t"
    "v_exp_f32_e32 v5, v5\n\t"
    "v_mfma_f32_16x16x32_bf16 a[56:59], a[220:223], v[84:87], a[56:59]\n\t"
    "v_exp_f32_e32 v6, v6\n\t"
    "v_mfma_f32_16x16x32_bf16 a[72:75], a[224:227], v[80:83], a[72:75]\n\t"
    "buffer_load_dwordx4 v213, s[12:15], s28 offen offset:3072 lds\n\t"
    "v_cvt_pk_bf16_f32 v92, v56, v57\n\t"
    "v_mfma_f32_16x16x32_bf16 a[72:75], a[228:231], v[84:87], a[72:75]\n\t"
    "v_exp_f32_e32 v7, v7\n\t"
    "v_mfma_f32_16x16x32_bf16 a[88:91], a[232:235], v[80:83], a[88:91]\n\t"
    "v_exp_f32_e32 v8, v8\n\t"
    "v_mfma_f32_16x16x32_bf16 a[88:91], a[236:239], v[84:87], a[88:91]\n\t"
    "v_exp_f32_e32 v9, v9\n\t"
    "v_mfma_f32_16x16x32_bf16 a[104:107], a[240:243], v[80:83], a[104:107]\n\t"
    "v_exp_f32_e32 v10, v10\n\t"
    "v_mfma_f32_16x16x32_bf16 a[104:107], a[244:247], v[84:87], a[104:107]\n\t"
    "v_exp_f32_e32 v11, v11\n\t"
    "v_mfma_f32_16x16x32_bf16 a[120:123], a[248:251], v[80:83], a[120:123]\n\t"
    "v_exp_f32_e32 v12, v12\n\t"
    "v_mfma_f32_16x16x32_bf16 a[120:123], a[252:255], v[84:87], a[120:123]\n\t"
    "v_exp_f32_e32 v13, v13\n\t"
    "v_mfma_f32_16x16x32_bf16 v[184:187], v[192:195], v[80:83], v[184:187]\n\t"
    "v_exp_f32_e32 v14, v14\n\t"
    "v_mfma_f32_16x16x32_bf16 v[16:19], a[128:131], v[112:115], v[164:167]\n\t"
    "v_exp_f32_e32 v15, v15\n\t"
    "v_mfma_f32_16x16x32_bf16 v[16:19], a[132:135], v[116:119], v[16:19]\n\t"
    "v_cvt_pk_bf16_f32 v93, v58, v59\n\t"
    "v_mfma_f32_16x16x32_bf16 v[16:19], a[136:139], v[120:123], v[16:19]\n\t"
    "v_cvt_pk_bf16_f32 v94, v60, v61\n\t"
    "v_mfma_f32_16x16x32_bf16 v[16:19], a[140:143], v[124:127], v[16:19]\n\t"
    "v_cvt_pk_bf16_f32 v95, v62, v63\n\t"
    "v_mfma_f32_16x16x32_bf16 v[20:23], a[156:159], v[124:127], v[164:167]\n\t"
    "s_add_u32 s28, s28, 16384\n\t"
    "v_add_f32_e32 v216, v216, v45\n\t"
    "v_mfma_f32_16x16x32_bf16 v[20:23], a[152:155], v[120:123], v[20:23]\n\t"
    "v_add_f32_e32 v216, v216, v46\n\t"
    "v_add_f32_e32 v216, v216, v47\n\t"
    "v_mfma_f32_16x16x32_bf16 v[20:23], a[148:151], v[116:119], v[20:23]\n\t"
    "v_cvt_pk_bf16_f32 v64, v0, v1\n\t"
    "v_cvt_pk_bf16_f32 v65, v2, v3\n\t"
    "v_mfma_f32_16x16x32_bf16 v[20:23], a[144:147], v[112:115], v[20:23]\n\t"
    "v_exp_f32_e32 v16, v16\n\t"
    "v_mfma_f32_16x16x32_bf16 v[24:27], a[160:163], v[112:115], v[164:167]\n\t"
    "v_exp_f32_e32 v17, v17\n\t"
    "v_mfma_f32_16x16x32_bf16 v[24:27], a[164:167], v[116:119], v[24:27]\n\t"
    "v_exp_f32_e32 v18, v18\n\t"
    "v_mfma_f32_16x16x32_bf16 v[24:27], a[168:171], v[120:123], v[24:27]\n\t"
    "v_exp_f32_e32 v19, v19\n\t"
    "v_mfma_f32_16x16x32_bf16 v[24:27], a[172:175], v[124:127], v[24:27]\n\t"
    "v_exp_f32_e32 v20, v20\n\t"
    "v_mfma_f32_16x16x32_bf16 v[28:31], a[188:191], v[124:127], v[164:167]\n\t"
    "v_exp_f32_e32 v21, v21\n\t"
    "v_mfma_f32_16x16x32_bf16 v[28:31], a[184:187], v[120:123], v[28:31]\n\t"
    "v_exp_f32_e32 v22, v22\n\t"
    "v_mfma_f32_16x16x32_bf16 v[28:31], a[180:183], v[116:119], v[28:31]\n\t"
    "v_exp_f32_e32 v23, v23\n\t"
    "v_mfma_f32_16x16x32_bf16 v[28:31], a[176:179], v[112:115], v[28:31]\n\t"
    "v_exp_f32_e32 v24, v24\n\t"
    "v_mfma_f32_16x16x32_bf16 a[12:15], a[192:195], v[88:91], a[12:15]\n\t"
    "s_waitcnt vmcnt(9)\n\t"
    "s_barrier\n\t"
    "v_mfma_f32_16x16x32_bf16 a[12:15], a[196:199], v[92:95], a[12:15]\n\t"
    "ds_read_b64_tr_b16 a[192:193], v200 offset:32768\n\t"
    "v_cvt_pk_bf16_f32 v66, v4, v5\n\t"
    "v_mfma_f32_16x16x32_bf16 a[28:31], a[200:203], v[88:91], a[28:31]\n\t"
    "ds_read_b64_tr_b16 a[194:195], v200 offset:36864\n\t"
    "v_cvt_pk_bf16_f32 v67, v6, v7\n\t"
    "v_mfma_f32_16x16x32_bf16 a[28:31], a[204:207], v[92:95], a[28:31]\n\t"
    "ds_read_b64_tr_b16 a[196:197], v200 offset:40960\n\t"
    "v_cvt_pk_bf16_f32 v68, v8, v9\n\t"
    "v_mfma_f32_16x16x32_bf16 a[44:47], a[208:211], v[88:91], a[44:47]\n\t"
    "ds_read_b64_tr_b16 a[198:199], v200 offset:45056\n\t"
    "v_cvt_pk_bf16_f32 v69, v10, v11\n\t"
    "v_mfma_f32_16x16x32_bf16 a[44:47], a[212:215], v[92:95], a[44:47]\n\t"
    "ds_read_b64_tr_b16 a[200:201], v201 offset:32768\n\t"
    "v_cvt_pk_bf16_f32 v70, v12, v13\n\t"
    "v_mfma_f32_16x16x32_bf16 a[60:63], a[216:219], v[88:91], a[60:63]\n\t"
    "ds_read_b64_tr_b16 a[202:203], v201 offset:36864\n\t"
    "v_cvt_pk_bf16_f32 v71, v14, v15\n\t"
    "v_mfma_f32_16x16x32_bf16 a[60:63], a[220:223], v[92:95], a[60:63]\n\t"
    "ds_read_b64_tr_b16 a[204:205], v201 offset:40960\n\t"
    "v_add_f32_e32 v217, v217, v56\n\t"
    "v_mfma_f32_16x16x32_bf16 a[76:79], a[224:227], v[88:91], a[76:79]\n\t"
    "ds_read_b64_tr_b16 a[206:207], v201 offset:45056\n\t"
    "v_add_f32_e32 v217, v217, v57\n\t"
    "v_mfma_f32_16x16x32_bf16 a[76:79], a[228:231], v[92:95], a[76:79]\n\t"
    "ds_read_b64_tr_b16 a[208:209], v202 offset:32768\n\t"
    "v_add_f32_e32 v217, v217, v58\n\t"
    "v_mfma_f32_16x16x32_bf16 a[92:95], a[232:235], v[88:91], a[92:95]\n\t"
    "ds_read_b64_tr_b16 a[210:211], v202 offset:36864\n\t"
    "v_add_f32_e32 v217, v217, v59\n\t"
    "v_mfma_f32_16x16x32_bf16 a[92:95], a[236:239], v[92:95], a[92:95]\n\t"
    "ds_read_b64_tr_b16 a[212:213], v202 offset:40960\n\t"
    "v_add_f32_e32 v217, v217, v60\n\t"
    "v_mfma_f32_16x16x32_bf16 a[108:111], a[240:243], v[88:91], a[108:111]\n\t"
    "ds_read_b64_tr_b16 a[214:215], v202 offset:45056\n\t"
    "v_add_f32_e32 v217, v217, v61\n\t"
    "v_mfma_f32_16x16x32_bf16 a[108:111], a[244:247], v[92:95], a[108:111]\n\t"
    "ds_read_b64_tr_b16 a[216:217], v203 offset:32768\n\t"
    "v_add_f32_e32 v217, v217, v62\n\t"
    "v_mfma_f32_16x16x32_bf16 a[124:127], a[248:251], v[88:91], a[124:127]\n\t"
    "ds_read_b64_tr_b16 a[218:219], v203 offset:36864\n\t"
    "v_add_f32_e32 v217, v217, v63\n\t"
    "v_mfma_f32_16x16x32_bf16 a[124:127], a[252:255], v[92:95], a[124:127]\n\t"
    "ds_read_b64_tr_b16 a[220:221], v203 offset:40960\n\t"
    "v_cvt_pk_bf16_f32 v72, v16, v17\n\t"
    "v_mfma_f32_16x16x32_bf16 v[188:191], v[192:195], v[88:91], v[188:191]\n\t"
    "ds_read_b64_tr_b16 a[222:223], v203 offset:45056\n\t"
    "v_cvt_pk_bf16_f32 v73, v18, v19\n\t"
    "v_mfma_f32_16x16x32_bf16 v[32:35], a[128:131], v[128:131], v[168:171]\n\t"
    "ds_read_b64_tr_b16 a[224:225], v204 offset:32768\n\t"
    "v_cvt_pk_bf16_f32 v74, v20, v21\n\t"
    "v_mfma_f32_16x16x32_bf16 v[32:35], a[132:135], v[132:135], v[32:35]\n\t"
    "ds_read_b64_tr_b16 a[226:227], v204 offset:36864\n\t"
    "v_cvt_pk_bf16_f32 v75, v22, v23\n\t"
    "v_mfma_f32_16x16x32_bf16 v[32:35], a[136:139], v[136:139], v[32:35]\n\t"
    "ds_read_b64_tr_b16 a[228:229], v204 offset:40960\n\t"
    "v_add_f32_e32 v214, v214, v8\n\t"
    "v_mfma_f32_16x16x32_bf16 v[32:35], a[140:143], v[140:143], v[32:35]\n\t"
    "ds_read_b64_tr_b16 a[230:231], v204 offset:45056\n\t"
    "v_add_f32_e32 v214, v214, v9\n\t"
    "v_mfma_f32_16x16x32_bf16 v[36:39], a[156:159], v[140:143], v[168:171]\n\t"
    "ds_read_b64_tr_b16 a[232:233], v205 offset:32768\n\t"
    "v_add_f32_e32 v214, v214, v10\n\t"
    "v_mfma_f32_16x16x32_bf16 v[36:39], a[152:155], v[136:139], v[36:39]\n\t"
    "ds_read_b64_tr_b16 a[234:235], v205 offset:36864\n\t"
    "v_add_f32_e32 v214, v214, v11\n\t"
    "v_mfma_f32_16x16x32_bf16 v[36:39], a[148:151], v[132:135], v[36:39]\n\t"
    "ds_read_b64_tr_b16 a[236:237], v205 offset:40960\n\t"
    "v_add_f32_e32 v214, v214, v12\n\t"
    "v_mfma_f32_16x16x32_bf16 v[36:39], a[144:147], v[128:131], v[36:39]\n\t"
    "ds_read_b64_tr_b16 a[238:239], v205 offset:45056\n\t"
    "v_add_f32_e32 v214, v214, v13\n\t"
    "v_mfma_f32_16x16x32_bf16 v[40:43], a[160:163], v[128:131], v[168:171]\n\t"
    "ds_read_b64_tr_b16 a[240:241], v206 offset:32768\n\t"
    "v_add_f32_e32 v214, v214, v14\n\t"
    "v_mfma_f32_16x16x32_bf16 v[40:43], a[164:167], v[132:135], v[40:43]\n\t"
    "ds_read_b64_tr_b16 a[242:243], v206 offset:36864\n\t"
    "v_add_f32_e32 v214, v214, v15\n\t"
    "v_mfma_f32_16x16x32_bf16 v[40:43], a[168:171], v[136:139], v[40:43]\n\t"
    "ds_read_b64_tr_b16 a[244:245], v206 offset:40960\n\t"
    "v_add_f32_e32 v215, v215, v24\n\t"
    "v_mfma_f32_16x16x32_bf16 v[40:43], a[172:175], v[140:143], v[40:43]\n\t"
    "ds_read_b64_tr_b16 a[246:247], v206 offset:45056\n\t"
    "v_mfma_f32_16x16x32_bf16 v[44:47], a[188:191], v[140:143], v[168:171]\n\t"
    "ds_read_b64_tr_b16 a[248:249], v207 offset:32768\n\t"
    "v_mfma_f32_16x16x32_bf16 v[44:47], a[184:187], v[136:139], v[44:47]\n\t"
    "ds_read_b64_tr_b16 a[250:251], v207 offset:36864\n\t"
    "v_mfma_f32_16x16x32_bf16 v[44:47], a[180:183], v[132:135], v[44:47]\n\t"
    "ds_read_b64_tr_b16 a[252:253], v207 offset:40960\n\t"
    "v_mfma_f32_16x16x32_bf16 v[44:47], a[176:179], v[128:131], v[44:47]\n\t"
    "ds_read_b64_tr_b16 a[254:255], v207 offset:45056\n\t"
    "s_waitcnt lgkmcnt(15)\n\t"
    "v_mfma_f32_16x16x32_bf16 a[0:3], a[192:195], v[64:67], a[0:3]\n\t"
    "v_exp_f32_e32 v25, v25\n\t"
    "v_mfma_f32_16x16x32_bf16 a[0:3], a[196:199], v[68:71], a[0:3]\n\t"
    "v_exp_f32_e32 v26, v26\n\t"
    "v_mfma_f32_16x16x32_bf16 a[16:19], a[200:203], v[64:67], a[16:19]\n\t"
    "v_exp_f32_e32 v27, v27\n\t"
    "v_mfma_f32_16x16x32_bf16 a[16:19], a[204:207], v[68:71], a[16:19]\n\t"
    "v_exp_f32_e32 v28, v28\n\t"
    "v_mfma_f32_16x16x32_bf16 a[32:35], a[208:211], v[64:67], a[32:35]\n\t"
    "v_exp_f32_e32 v29, v29\n\t"
    "v_mfma_f32_16x16x32_bf16 a[32:35], a[212:215], v[68:71], a[32:35]\n\t"
    "v_exp_f32_e32 v30, v30\n\t"
    "v_mfma_f32_16x16x32_bf16 a[48:51], a[216:219], v[64:67], a[48:51]\n\t"
    "v_exp_f32_e32 v31, v31\n\t"
    "v_mfma_f32_16x16x32_bf16 a[48:51], a[220:223], v[68:71], a[48:51]\n\t"
    "s_waitcnt lgkmcnt(0)\n\t"
    "v_mfma_f32_16x16x32_bf16 a[64:67], a[224:227], v[64:67], a[64:67]\n\t"
    "v_cvt_pk_bf16_f32 v76, v24, v25\n\t"
    "v_mfma_f32_16x16x32_bf16 a[64:67], a[228:231], v[68:71], a[64:67]\n\t"
    "v_exp_f32_e32 v32, v32\n\t"
    "v_mfma_f32_16x16x32_bf16 a[80:83], a[232:235], v[64:67], a[80:83]\n\t"
    "v_exp_f32_e32 v33, v33\n\t"
    "v_mfma_f32_16x16x32_bf16 a[80:83], a[236:239], v[68:71], a[80:83]\n\t"
    "v_exp_f32_e32 v34, v34\n\t"
    "v_mfma_f32_16x16x32_bf16 a[96:99], a[240:243], v[64:67], a[96:99]\n\t"
    "v_exp_f32_e32 v35, v35\n\t"
    "v_mfma_f32_16x16x32_bf16 a[96:99], a[244:247], v[68:71], a[96:99]\n\t"
    "v_exp_f32_e32 v36, v36\n\t"
    "v_mfma_f32_16x16x32_bf16 a[112:115], a[248:251], v[64:67], a[112:115]\n\t"
    "v_cvt_pk_bf16_f32 v77, v26, v27\n\t"
    "v_mfma_f32_16x16x32_bf16 a[112:115], a[252:255], v[68:71], a[112:115]\n\t"
    "v_exp_f32_e32 v37, v37\n\t"
    "v_mfma_f32_16x16x32_bf16 v[176:179], v[192:195], v[64:67], v[176:179]\n\t"
    "v_exp_f32_e32 v38, v38\n\t"
    "v_mfma_f32_16x16x32_bf16 v[48:51], a[128:131], v[144:147], v[172:175]\n\t"
    "v_exp_f32_e32 v39, v39\n\t"
    "v_mfma_f32_16x16x32_bf16 v[48:51], a[132:135], v[148:151], v[48:51]\n\t"
    "v_exp_f32_e32 v40, v40\n\t"
    "v_mfma_f32_16x16x32_bf16 v[48:51], a[136:139], v[152:155], v[48:51]\n\t"
    "v_exp_f32_e32 v41, v41\n\t"
    "v_mfma_f32_16x16x32_bf16 v[48:51], a[140:143], v[156:159], v[48:51]\n\t"
    "v_exp_f32_e32 v42, v42\n\t"
    "v_mfma_f32_16x16x32_bf16 v[52:55], a[156:159], v[156:159], v[172:175]\n\t"
    "v_exp_f32_e32 v43, v43\n\t"
    "v_mfma_f32_16x16x32_bf16 v[52:55], a[152:155], v[152:155], v[52:55]\n\t"
    "v_exp_f32_e32 v44, v44\n\t"
    "v_mfma_f32_16x16x32_bf16 v[52:55], a[148:151], v[148:151], v[52:55]\n\t"
    "v_exp_f32_e32 v45, v45\n\t"
    "v_mfma_f32_16x16x32_bf16 v[52:55], a[144:147], v[144:147], v[52:55]\n\t"
    "v_exp_f32_e32 v46, v46\n\t"
    "v_mfma_f32_16x16x32_bf16 v[56:59], a[160:163], v[144:147], v[172:175]\n\t"
    "v_exp_f32_e32 v47, v47\n\t"
    "v_mfma_f32_16x16x32_bf16 v[56:59], a[164:167], v[148:151], v[56:59]\n\t"
    "v_cvt_pk_bf16_f32 v78, v28, v29\n\t"
    "v_cvt_pk_bf16_f32 v79, v30, v31\n\t"
    "v_mfma_f32_16x16x32_bf16 v[56:59], a[168:171], v[152:155], v[56:59]\n\t"
    "v_cvt_pk_bf16_f32 v80, v32, v33\n\t"
    "v_mfma_f32_16x16x32_bf16 v[56:59], a[172:175], v[156:159], v[56:59]\n\t"
    "ds_read_b128 a[128:131], v196 offset:49152\n\t"
    "v_cvt_pk_bf16_f32 v81, v34, v35\n\t"
    "v_mfma_f32_16x16x32_bf16 v[60:63], a[188:191], v[156:159], v[172:175]\n\t"
    "ds_read_b128 a[132:135], v197 offset:49152\n\t"
    "v_cvt_pk_bf16_f32 v82, v36, v37\n\t"
    "v_mfma_f32_16x16x32_bf16 v[60:63], a[184:187], v[152:155], v[60:63]\n\t"
    "ds_read_b128 a[136:139], v198 offset:49152\n\t"
    "v_cvt_pk_bf16_f32 v83, v38, v39\n\t"
    "v_mfma_f32_16x16x32_bf16 v[60:63], a[180:183], v[148:151], v[60:63]\n\t"
    "ds_read_b128 a[140:143], v199 offset:49152\n\t"
    "v_cvt_pk_bf16_f32 v84, v40, v41\n\t"
    "v_mfma_f32_16x16x32_bf16 v[60:63], a[176:179], v[144:147], v[60:63]\n\t"
    "ds_read_b128 a[156:159], v199 offset:53248\n\t"
    "v_cvt_pk_bf16_f32 v85, v42, v43\n\t"
    "v_mfma_f32_16x16x32_bf16 a[4:7], a[192:195], v[72:75], a[4:7]\n\t"
    "ds_read_b128 a[152:155], v198 offset:53248\n\t"
    "v_cvt_pk_bf16_f32 v86, v44, v45\n\t"
    "v_mfma_f32_16x16x32_bf16 a[4:7], a[196:199], v[76:79], a[4:7]\n\t"
    "v_cvt_pk_bf16_f32 v87, v46, v47\n\t"
    "v_mfma_f32_16x16x32_bf16 a[20:23], a[200:203], v[72:75], a[20:23]\n\t"
    "ds_read_b128 a[148:151], v197 offset:53248\n\t"
    "v_add_f32_e32 v215, v215, v25\n\t"
    "v_mfma_f32_16x16x32_bf16 a[20:23], a[204:207], v[76:79], a[20:23]\n\t"
    "ds_read_b128 a[144:147], v196 offset:53248\n\t"
    "v_add_f32_e32 v215, v215, v26\n\t"
    "v_mfma_f32_16x16x32_bf16 a[36:39], a[208:211], v[72:75], a[36:39]\n\t"
    "ds_read_b128 a[160:163], v196 offset:57344\n\t"
    "v_add_f32_e32 v215, v215, v27\n\t"
    "v_mfma_f32_16x16x32_bf16 a[36:39], a[212:215], v[76:79], a[36:39]\n\t"
    "ds_read_b128 a[164:167], v197 offset:57344\n\t"
    "v_add_f32_e32 v215, v215, v28\n\t"
    "v_mfma_f32_16x16x32_bf16 a[52:55], a[216:219], v[72:75], a[52:55]\n\t"
    "ds_read_b128 a[168:171], v198 offset:57344\n\t"
    "v_add_f32_e32 v215, v215, v29\n\t"
    "v_mfma_f32_16x16x32_bf16 a[52:55], a[220:223], v[76:79], a[52:55]\n\t"
    "ds_read_b128 a[172:175], v199 offset:57344\n\t"
    "v_add_f32_e32 v215, v215, v30\n\t"
    "v_mfma_f32_16x16x32_bf16 a[68:71], a[224:227], v[72:75], a[68:71]\n\t"
    "ds_read_b128 a[188:191], v199 offset:61440\n\t"
    "v_add_f32_e32 v215, v215, v31\n\t"
    "v_mfma_f32_16x16x32_bf16 a[68:71], a[228:231], v[76:79], a[68:71]\n\t"
    "v_add_f32_e32 v216, v216, v40\n\t"
    "v_mfma_f32_16x16x32_bf16 a[84:87], a[232:235], v[72:75], a[84:87]\n\t"
    "ds_read_b128 a[184:187], v198 offset:61440\n\t"
    "v_add_f32_e32 v216, v216, v41\n\t"
    "v_mfma_f32_16x16x32_bf16 a[84:87], a[236:239], v[76:79], a[84:87]\n\t"
    "ds_read_b128 a[180:183], v197 offset:61440\n\t"
    "v_add_f32_e32 v216, v216, v42\n\t"
    "v_mfma_f32_16x16x32_bf16 a[100:103], a[240:243], v[72:75], a[100:103]\n\t"
    "ds_read_b128 a[176:179], v196 offset:61440\n\t"
    "v_add_f32_e32 v216, v216, v43\n\t"
    "v_mfma_f32_16x16x32_bf16 a[100:103], a[244:247], v[76:79], a[100:103]\n\t"
    "v_add_f32_e32 v216, v216, v44\n\t"
    "v_mfma_f32_16x16x32_bf16 a[116:119], a[248:251], v[72:75], a[116:119]\n\t"
    "v_exp_f32_e32 v48, v48\n\t"
    "v_mfma_f32_16x16x32_bf16 a[116:119], a[252:255], v[76:79], a[116:119]\n\t"
    "v_exp_f32_e32 v49, v49\n\t"
    "v_mfma_f32_16x16x32_bf16 v[180:183], v[192:195], v[72:75], v[180:183]\n\t"
    "v_cvt_pk_bf16_f32 v88, v48, v49\n\t"
    "s_waitcnt lgkmcnt(8)\n\t"
    "v_mfma_f32_16x16x32_bf16 v[0:3], a[128:131], v[96:99], v[160:163]\n\t"
    "s_add_u32 m0, s78, 65536\n\t"
    "s_nop 0\n\t"
    "buffer_load_dwordx4 v234, s[64:67], s76 offen lds\n\t"
    "v_exp_f32_e32 v50, v50\n\t"
    "v_mfma_f32_16x16x32_bf16 v[0:3], a[132:135], v[100:103], v[0:3]\n\t"
    "v_exp_f32_e32 v51, v51\n\t"
    "v_mfma_f32_16x16x32_bf16 v[0:3], a[136:139], v[104:107], v[0:3]\n\t"
    "v_exp_f32_e32 v52, v52\n\t"
    "v_mfma_f32_16x16x32_bf16 v[0:3], a[140:143], v[108:111], v[0:3]\n\t"
    "buffer_load_dwordx4 v235, s[64:67], s76 offen offset:1024 lds\n\t"
    "v_exp_f32_e32 v53, v53\n\t"
    "v_mfma_f32_16x16x32_bf16 v[4:7], a[156:159], v[108:111], v[160:163]\n\t"
    "v_cvt_pk_bf16_f32 v89, v50, v51\n\t"
    "v_mfma_f32_16x16x32_bf16 v[4:7], a[152:155], v[104:107], v[4:7]\n\t"
    "v_exp_f32_e32 v54, v54\n\t"
    "v_mfma_f32_16x16x32_bf16 v[4:7], a[148:151], v[100:103], v[4:7]\n\t"
    "buffer_load_dwordx4 v236, s[64:67], s76 offen offset:2048 lds\n\t"
    "v_exp_f32_e32 v55, v55\n\t"
    "v_mfma_f32_16x16x32_bf16 v[4:7], a[144:147], v[96:99], v[4:7]\n\t"
    "v_cvt_pk_bf16_f32 v90, v52, v53\n\t"
    "s_waitcnt lgkmcnt(0)\n\t"
    "v_mfma_f32_16x16x32_bf16 v[8:11], a[160:163], v[96:99], v[160:163]\n\t"
    "v_exp_f32_e32 v56, v56\n\t"
    "v_mfma_f32_16x16x32_bf16 v[8:11], a[164:167], v[100:103], v[8:11]\n\t"
    "buffer_load_dwordx4 v237, s[64:67], s76 offen offset:3072 lds\n\t"
    "s_add_u32 s76, s76, 65536\n\t"
    "s_and_b32 s76, s76, 0xfffff\n\t"
    "v_exp_f32_e32 v57, v57\n\t"
    "v_mfma_f32_16x16x32_bf16 v[8:11], a[168:171], v[104:107], v[8:11]\n\t"
    "v_exp_f32_e32 v58, v58\n\t"
    "v_mfma_f32_16x16x32_bf16 v[8:11], a[172:175], v[108:111], v[8:11]\n\t"
    "v_exp_f32_e32 v59, v59\n\t"
    "v_mfma_f32_16x16x32_bf16 v[12:15], a[188:191], v[108:111], v[160:163]\n\t"
    "s_add_u32 m0, s78, 81920\n\t"
    "s_nop 0\n\t"
    "buffer_load_dwordx4 v234, s[64:67], s76 offen lds\n\t"
    "v_exp_f32_e32 v60, v60\n\t"
    "v_mfma_f32_16x16x32_bf16 v[12:15], a[184:187], v[104:107], v[12:15]\n\t"
    "v_exp_f32_e32 v61, v61\n\t"
    "v_mfma_f32_16x16x32_bf16 v[12:15], a[180:183], v[100:103], v[12:15]\n\t"
    "v_cvt_pk_bf16_f32 v91, v54, v55\n\t"
    "v_mfma_f32_16x16x32_bf16 v[12:15], a[176:179], v[96:99], v[12:15]\n\t"
    "buffer_load_dwordx4 v235, s[64:67], s76 offen offset:1024 lds\n\t"
    "v_exp_f32_e32 v62, v62\n\t"
    "v_mfma_f32_16x16x32_bf16 a[8:11], a[192:195], v[80:83], a[8:11]\n\t"
    "v_exp_f32_e32 v63, v63\n\t"
    "v_mfma_f32_16x16x32_bf16 a[8:11], a[196:199], v[84:87], a[8:11]\n\t"
    "v_exp_f32_e32 v0, v0\n\t"
    "v_mfma_f32_16x16x32_bf16 a[24:27], a[200:203], v[80:83], a[24:27]\n\t"
    "buffer_load_dwordx4 v236, s[64:67], s76 offen offset:2048 lds\n\t"
    "v_exp_f32_e32 v1, v1\n\t"
    "v_mfma_f32_16x16x32_bf16 a[24:27], a[204:207], v[84:87], a[24:27]\n\t"
    "v_exp_f32_e32 v2, v2\n\t"
    "v_mfma_f32_16x16x32_bf16 a[40:43], a[208:211], v[80:83], a[40:43]\n\t"
    "v_exp_f32_e32 v3, v3\n\t"
    "v_mfma_f32_16x16x32_bf16 a[40:43], a[212:215], v[84:87], a[40:43]\n\t"
    "buffer_load_dwordx4 v237, s[64:67], s76 offen offset:3072 lds\n\t"
    "s_add_u32 s76, s76, 65536\n\t"
    "s_and_b32 s76, s76, 0xfffff\n\t"
    "v_exp_f32_e32 v4, v4\n\t"
    "v_mfma_f32_16x16x32_bf16 a[56:59], a[216:219], v[80:83], a[56:59]\n\t"
    "v_exp_f32_e32 v5, v5\n\t"
    "v_mfma_f32_16x16x32_bf16 a[56:59], a[220:223], v[84:87], a[56:59]\n\t"
    "v_exp_f32_e32 v6, v6\n\t"
    "v_mfma_f32_16x16x32_bf16 a[72:75], a[224:227], v[80:83], a[72:75]\n\t"
    "s_add_u32 m0, s78, 98304\n\t"
    "s_nop 0\n\t"
    "buffer_load_dwordx4 v234, s[64:67], s76 offen lds\n\t"
    "v_cvt_pk_bf16_f32 v92, v56, v57\n\t"
    "v_mfma_f32_16x16x32_bf16 a[72:75], a[228:231], v[84:87], a[72:75]\n\t"
    "v_exp_f32_e32 v7, v7\n\t"
    "v_mfma_f32_16x16x32_bf16 a[88:91], a[232:235], v[80:83], a[88:91]\n\t"
    "v_exp_f32_e32 v8, v8\n\t"
    "v_mfma_f32_16x16x32_bf16 a[88:91], a[236:239], v[84:87], a[88:91]\n\t"
    "buffer_load_dwordx4 v235, s[64:67], s76 offen offset:1024 lds\n\t"
    "v_exp_f32_e32 v9, v9\n\t"
    "v_mfma_f32_16x16x32_bf16 a[104:107], a[240:243], v[80:83], a[104:107]\n\t"
    "v_exp_f32_e32 v10, v10\n\t"
    "v_mfma_f32_16x16x32_bf16 a[104:107], a[244:247], v[84:87], a[104:107]\n\t"
    "v_exp_f32_e32 v11, v11\n\t"
    "v_mfma_f32_16x16x32_bf16 a[120:123], a[248:251], v[80:83], a[120:123]\n\t"
    "buffer_load_dwordx4 v236, s[64:67], s76 offen offset:2048 lds\n\t"
    "v_exp_f32_e32 v12, v12\n\t"
    "v_mfma_f32_16x16x32_bf16 a[120:123], a[252:255], v[84:87], a[120:123]\n\t"
    "v_exp_f32_e32 v13, v13\n\t"
    "v_mfma_f32_16x16x32_bf16 v[184:187], v[192:195], v[80:83], v[184:187]\n\t"
    "v_exp_f32_e32 v14, v14\n\t"
    "v_mfma_f32_16x16x32_bf16 v[16:19], a[128:131], v[112:115], v[164:167]\n\t"
    "buffer_load_dwordx4 v237, s[64:67], s76 offen offset:3072 lds\n\t"
    "s_add_u32 s76, s76, 65536\n\t"
    "s_and_b32 s76, s76, 0xfffff\n\t"
    "v_exp_f32_e32 v15, v15\n\t"
    "v_mfma_f32_16x16x32_bf16 v[16:19], a[132:135], v[116:119], v[16:19]\n\t"
    "v_cvt_pk_bf16_f32 v93, v58, v59\n\t"
    "v_mfma_f32_16x16x32_bf16 v[16:19], a[136:139], v[120:123], v[16:19]\n\t"
    "v_cvt_pk_bf16_f32 v94, v60, v61\n\t"
    "v_mfma_f32_16x16x32_bf16 v[16:19], a[140:143], v[124:127], v[16:19]\n\t"
    "s_add_u32 m0, s78, 114688\n\t"
    "s_nop 0\n\t"
    "buffer_load_dwordx4 v234, s[64:67], s76 offen lds\n\t"
    "v_cvt_pk_bf16_f32 v95, v62, v63\n\t"
    "v_mfma_f32_16x16x32_bf16 v[20:23], a[156:159], v[124:127], v[164:167]\n\t"
    "v_add_f32_e32 v216, v216, v45\n\t"
    "v_mfma_f32_16x16x32_bf16 v[20:23], a[152:155], v[120:123], v[20:23]\n\t"
    "v_add_f32_e32 v216, v216, v46\n\t"
    "v_add_f32_e32 v216, v216, v47\n\t"
    "v_mfma_f32_16x16x32_bf16 v[20:23], a[148:151], v[116:119], v[20:23]\n\t"
    "buffer_load_dwordx4 v235, s[64:67], s76 offen offset:1024 lds\n\t"
    "v_cvt_pk_bf16_f32 v64, v0, v1\n\t"
    "v_cvt_pk_bf16_f32 v65, v2, v3\n\t"
    "v_mfma_f32_16x16x32_bf16 v[20:23], a[144:147], v[112:115], v[20:23]\n\t"
    "v_exp_f32_e32 v16, v16\n\t"
    "v_mfma_f32_16x16x32_bf16 v[24:27], a[160:163], v[112:115], v[164:167]\n\t"
    "v_exp_f32_e32 v17, v17\n\t"
    "v_mfma_f32_16x16x32_bf16 v[24:27], a[164:167], v[116:119], v[24:27]\n\t"
    "buffer_load_dwordx4 v236, s[64:67], s76 offen offset:2048 lds\n\t"
    "v_exp_f32_e32 v18, v18\n\t"
    "v_mfma_f32_16x16x32_bf16 v[24:27], a[168:171], v[120:123], v[24:27]\n\t"
    "v_exp_f32_e32 v19, v19\n\t"
    "v_mfma_f32_16x16x32_bf16 v[24:27], a[172:175], v[124:127], v[24:27]\n\t"
    "v_exp_f32_e32 v20, v20\n\t"
    "v_mfma_f32_16x16x32_bf16 v[28:31], a[188:191], v[124:127], v[164:167]\n\t"
    "buffer_load_dwordx4 v237, s[64:67], s76 offen offset:3072 lds\n\t"
    "s_add_u32 s76, s76, 65536\n\t"
    "s_and_b32 s76, s76, 0xfffff\n\t"
    "v_exp_f32_e32 v21, v21\n\t"
    "v_mfma_f32_16x16x32_bf16 v[28:31], a[184:187], v[120:123], v[28:31]\n\t"
    "v_exp_f32_e32 v22, v22\n\t"
    "v_mfma_f32_16x16x32_bf16 v[28:31], a[180:183], v[116:119], v[28:31]\n\t"
    "v_exp_f32_e32 v23, v23\n\t"
    "v_mfma_f32_16x16x32_bf16 v[28:31], a[176:179], v[112:115], v[28:31]\n\t"
    "s_add_u32 m0, s78, 0\n\t"
    "s_nop 0\n\t"
    "buffer_load_dwordx4 v234, s[64:67], s76 offen lds\n\t"
    "v_exp_f32_e32 v24, v24\n\t"
    "v_mfma_f32_16x16x32_bf16 a[12:15], a[192:195], v[88:91], a[12:15]\n\t"
    "s_waitcnt vmcnt(17)\n\t"
    "s_barrier\n\t"
    "v_mfma_f32_16x16x32_bf16 a[12:15], a[196:199], v[92:95], a[12:15]\n\t"
    "ds_read_b64_tr_b16 a[192:193], v200 offset:49152\n\t"
    "v_cvt_pk_bf16_f32 v66, v4, v5\n\t"
    "v_mfma_f32_16x16x32_bf16 a[28:31], a[200:203], v[88:91], a[28:31]\n\t"
    "ds_read_b64_tr_b16 a[194:195], v200 offset:53248\n\t"
    "v_cvt_pk_bf16_f32 v67, v6, v7\n\t"
    "v_mfma_f32_16x16x32_bf16 a[28:31], a[204:207], v[92:95], a[28:31]\n\t"
    "ds_read_b64_tr_b16 a[196:197], v200 offset:57344\n\t"
    "v_cvt_pk_bf16_f32 v68, v8, v9\n\t"
    "v_mfma_f32_16x16x32_bf16 a[44:47], a[208:211], v[88:91], a[44:47]\n\t"
    "ds_read_b64_tr_b16 a[198:199], v200 offset:61440\n\t"
    "v_cvt_pk_bf16_f32 v69, v10, v11\n\t"
    "v_mfma_f32_16x16x32_bf16 a[44:47], a[212:215], v[92:95], a[44:47]\n\t"
    "ds_read_b64_tr_b16 a[200:201], v201 offset:49152\n\t"
    "v_cvt_pk_bf16_f32 v70, v12, v13\n\t"
    "v_mfma_f32_16x16x32_bf16 a[60:63], a[216:219], v[88:91], a[60:63]\n\t"
    "ds_read_b64_tr_b16 a[202:203], v201 offset:53248\n\t"
    "v_cvt_pk_bf16_f32 v71, v14, v15\n\t"
    "v_mfma_f32_16x16x32_bf16 a[60:63], a[220:223], v[92:95], a[60:63]\n\t"
    "ds_read_b64_tr_b16 a[204:205], v201 offset:57344\n\t"
    "v_add_f32_e32 v217, v217, v56\n\t"
    "v_mfma_f32_16x16x32_bf16 a[76:79], a[224:227], v[88:91], a[76:79]\n\t"
    "ds_read_b64_tr_b16 a[206:207], v201 offset:61440\n\t"
    "v_add_f32_e32 v217, v217, v57\n\t"
    "v_mfma_f32_16x16x32_bf16 a[76:79], a[228:231], v[92:95], a[76:79]\n\t"
    "ds_read_b64_tr_b16 a[208:209], v202 offset:49152\n\t"
    "v_add_f32_e32 v217, v217, v58\n\t"
    "v_mfma_f32_16x16x32_bf16 a[92:95], a[232:235], v[88:91], a[92:95]\n\t"
    "ds_read_b64_tr_b16 a[210:211], v202 offset:53248\n\t"
    "v_add_f32_e32 v217, v217, v59\n\t"
    "v_mfma_f32_16x16x32_bf16 a[92:95], a[236:239], v[92:95], a[92:95]\n\t"
    "ds_read_b64_tr_b16 a[212:213], v202 offset:57344\n\t"
    "v_add_f32_e32 v217, v217, v60\n\t"
    "v_mfma_f32_16x16x32_bf16 a[108:111], a[240:243], v[88:91], a[108:111]\n\t"
    "ds_read_b64_tr_b16 a[214:215], v202 offset:61440\n\t"
    "v_add_f32_e32 v217, v217, v61\n\t"
    "v_mfma_f32_16x16x32_bf16 a[108:111], a[244:247], v[92:95], a[108:111]\n\t"
    "ds_read_b64_tr_b16 a[216:217], v203 offset:49152\n\t"
    "v_add_f32_e32 v217, v217, v62\n\t"
    "v_mfma_f32_16x16x32_bf16 a[124:127], a[248:251], v[88:91], a[124:127]\n\t"
    "ds_read_b64_tr_b16 a[218:219], v203 offset:53248\n\t"
    "v_add_f32_e32 v217, v217, v63\n\t"
    "v_mfma_f32_16x16x32_bf16 a[124:127], a[252:255], v[92:95], a[124:127]\n\t"
    "ds_read_b64_tr_b16 a[220:221], v203 offset:57344\n\t"
    "v_cvt_pk_bf16_f32 v72, v16, v17\n\t"
    "v_mfma_f32_16x16x32_bf16 v[188:191], v[192:195], v[88:91], v[188:191]\n\t"
    "ds_read_b64_tr_b16 a[222:223], v203 offset:61440\n\t"
    "v_cvt_pk_bf16_f32 v73, v18, v19\n\t"
    "v_mfma_f32_16x16x32_bf16 v[32:35], a[128:131], v[128:131], v[168:171]\n\t"
    "ds_read_b64_tr_b16 a[224:225], v204 offset:49152\n\t"
    "v_cvt_pk_bf16_f32 v74, v20, v21\n\t"
    "v_mfma_f32_16x16x32_bf16 v[32:35], a[132:135], v[132:135], v[32:35]\n\t"
    "ds_read_b64_tr_b16 a[226:227], v204 offset:53248\n\t"
    "v_cvt_pk_bf16_f32 v75, v22, v23\n\t"
    "v_mfma_f32_16x16x32_bf16 v[32:35], a[136:139], v[136:139], v[32:35]\n\t"
    "ds_read_b64_tr_b16 a[228:229], v204 offset:57344\n\t"
    "v_add_f32_e32 v214, v214, v8\n\t"
    "v_mfma_f32_16x16x32_bf16 v[32:35], a[140:143], v[140:143], v[32:35]\n\t"
    "ds_read_b64_tr_b16 a[230:231], v204 offset:61440\n\t"
    "v_add_f32_e32 v214, v214, v9\n\t"
    "v_mfma_f32_16x16x32_bf16 v[36:39], a[156:159], v[140:143], v[168:171]\n\t"
    "ds_read_b64_tr_b16 a[232:233], v205 offset:49152\n\t"
    "v_add_f32_e32 v214, v214, v10\n\t"
    "v_mfma_f32_16x16x32_bf16 v[36:39], a[152:155], v[136:139], v[36:39]\n\t"
    "ds_read_b64_tr_b16 a[234:235], v205 offset:53248\n\t"
    "v_add_f32_e32 v214, v214, v11\n\t"
    "v_mfma_f32_16x16x32_bf16 v[36:39], a[148:151], v[132:135], v[36:39]\n\t"
    "ds_read_b64_tr_b16 a[236:237], v205 offset:57344\n\t"
    "v_add_f32_e32 v214, v214, v12\n\t"
    "v_mfma_f32_16x16x32_bf16 v[36:39], a[144:147], v[128:131], v[36:39]\n\t"
    "ds_read_b64_tr_b16 a[238:239], v205 offset:61440\n\t"
    "v_add_f32_e32 v214, v214, v13\n\t"
    "v_mfma_f32_16x16x32_bf16 v[40:43], a[160:163], v[128:131], v[168:171]\n\t"
    "ds_read_b64_tr_b16 a[240:241], v206 offset:49152\n\t"
    "v_add_f32_e32 v214, v214, v14\n\t"
    "v_mfma_f32_16x16x32_bf16 v[40:43], a[164:167], v[132:135], v[40:43]\n\t"
    "ds_read_b64_tr_b16 a[242:243], v206 offset:53248\n\t"
    "v_add_f32_e32 v214, v214, v15\n\t"
    "v_mfma_f32_16x16x32_bf16 v[40:43], a[168:171], v[136:139], v[40:43]\n\t"
    "ds_read_b64_tr_b16 a[244:245], v206 offset:57344\n\t"
    "v_add_f32_e32 v215, v215, v24\n\t"
    "v_mfma_f32_16x16x32_bf16 v[40:43], a[172:175], v[140:143], v[40:43]\n\t"
    "ds_read_b64_tr_b16 a[246:247], v206 offset:61440\n\t"
    "v_mfma_f32_16x16x32_bf16 v[44:47], a[188:191], v[140:143], v[168:171]\n\t"
    "ds_read_b64_tr_b16 a[248:249], v207 offset:49152\n\t"
    "v_mfma_f32_16x16x32_bf16 v[44:47], a[184:187], v[136:139], v[44:47]\n\t"
    "ds_read_b64_tr_b16 a[250:251], v207 offset:53248\n\t"
    "v_mfma_f32_16x16x32_bf16 v[44:47], a[180:183], v[132:135], v[44:47]\n\t"
    "ds_read_b64_tr_b16 a[252:253], v207 offset:57344\n\t"
    "v_mfma_f32_16x16x32_bf16 v[44:47], a[176:179], v[128:131], v[44:47]\n\t"
    "ds_read_b64_tr_b16 a[254:255], v207 offset:61440\n\t"
    "s_waitcnt lgkmcnt(15)\n\t"
    "v_mfma_f32_16x16x32_bf16 a[0:3], a[192:195], v[64:67], a[0:3]\n\t"
    "buffer_load_dwordx4 v235, s[64:67], s76 offen offset:1024 lds\n\t"
    "v_exp_f32_e32 v25, v25\n\t"
    "v_mfma_f32_16x16x32_bf16 a[0:3], a[196:199], v[68:71], a[0:3]\n\t"
    "v_exp_f32_e32 v26, v26\n\t"
    "v_mfma_f32_16x16x32_bf16 a[16:19], a[200:203], v[64:67], a[16:19]\n\t"
    "v_exp_f32_e32 v27, v27\n\t"
    "v_mfma_f32_16x16x32_bf16 a[16:19], a[204:207], v[68:71], a[16:19]\n\t"
    "buffer_load_dwordx4 v236, s[64:67], s76 offen offset:2048 lds\n\t"
    "v_exp_f32_e32 v28, v28\n\t"
    "v_mfma_f32_16x16x32_bf16 a[32:35], a[208:211], v[64:67], a[32:35]\n\t"
    "v_exp_f32_e32 v29, v29\n\t"
    "v_mfma_f32_16x16x32_bf16 a[32:35], a[212:215], v[68:71], a[32:35]\n\t"
    "v_exp_f32_e32 v30, v30\n\t"
    "v_mfma_f32_16x16x32_bf16 a[48:51], a[216:219], v[64:67], a[48:51]\n\t"
    "buffer_load_dwordx4 v237, s[64:67], s76 offen offset:3072 lds\n\t"
    "s_add_u32 s76, s76, 65536\n\t"
    "s_and_b32 s76, s76, 0xfffff\n\t"
    "v_exp_f32_e32 v31, v31\n\t"
    "v_mfma_f32_16x16x32_bf16 a[48:51], a[220:223], v[68:71], a[48:51]\n\t"
    "s_waitcnt lgkmcnt(0)\n\t"
    "v_mfma_f32_16x16x32_bf16 a[64:67], a[224:227], v[64:67], a[64:67]\n\t"
    "v_cvt_pk_bf16_f32 v76, v24, v25\n\t"
    "v_mfma_f32_16x16x32_bf16 a[64:67], a[228:231], v[68:71], a[64:67]\n\t"
    "s_add_u32 m0, s78, 16384\n\t"
    "s_nop 0\n\t"
    "buffer_load_dwordx4 v234, s[64:67], s76 offen lds\n\t"
    "v_exp_f32_e32 v32, v32\n\t"
    "v_mfma_f32_16x16x32_bf16 a[80:83], a[232:235], v[64:67], a[80:83]\n\t"
    "v_exp_f32_e32 v33, v33\n\t"
    "v_mfma_f32_16x16x32_bf16 a[80:83], a[236:239], v[68:71], a[80:83]\n\t"
    "v_exp_f32_e32 v34, v34\n\t"
    "v_mfma_f32_16x16x32_bf16 a[96:99], a[240:243], v[64:67], a[96:99]\n\t"
    "buffer_load_dwordx4 v235, s[64:67], s76 offen offset:1024 lds\n\t"
    "v_exp_f32_e32 v35, v35\n\t"
    "v_mfma_f32_16x16x32_bf16 a[96:99], a[244:247], v[68:71], a[96:99]\n\t"
    "v_exp_f32_e32 v36, v36\n\t"
    "v_mfma_f32_16x16x32_bf16 a[112:115], a[248:251], v[64:67], a[112:115]\n\t"
    "v_cvt_pk_bf16_f32 v77, v26, v27\n\t"
    "v_mfma_f32_16x16x32_bf16 a[112:115], a[252:255], v[68:71], a[112:115]\n\t"
    "buffer_load_dwordx4 v236, s[64:67], s76 offen offset:2048 lds\n\t"
    "v_exp_f32_e32 v37, v37\n\t"
    "v_mfma_f32_16x16x32_bf16 v[176:179], v[192:195], v[64:67], v[176:179]\n\t"
    "v_exp_f32_e32 v38, v38\n\t"
    "v_mfma_f32_16x16x32_bf16 v[48:51], a[128:131], v[144:147], v[172:175]\n\t"
    "v_exp_f32_e32 v39, v39\n\t"
    "v_mfma_f32_16x16x32_bf16 v[48:51], a[132:135], v[148:151], v[48:51]\n\t"
    "buffer_load_dwordx4 v237, s[64:67], s76 offen offset:3072 lds\n\t"
    "s_add_u32 s76, s76, 65536\n\t"
    "s_and_b32 s76, s76, 0xfffff\n\t"
    "v_exp_f32_e32 v40, v40\n\t"
    "v_mfma_f32_16x16x32_bf16 v[48:51], a[136:139], v[152:155], v[48:51]\n\t"
    "v_exp_f32_e32 v41, v41\n\t"
    "v_mfma_f32_16x16x32_bf16 v[48:51], a[140:143], v[156:159], v[48:51]\n\t"
    "v_exp_f32_e32 v42, v42\n\t"
    "v_mfma_f32_16x16x32_bf16 v[52:55], a[156:159], v[156:159], v[172:175]\n\t"
    "s_add_u32 m0, s78, 131072\n\t"
    "s_nop 0\n\t"
    "buffer_load_dwordx4 v234, s[64:67], s76 offen lds\n\t"
    "v_exp_f32_e32 v43, v43\n\t"
    "v_mfma_f32_16x16x32_bf16 v[52:55], a[152:155], v[152:155], v[52:55]\n\t"
    "v_exp_f32_e32 v44, v44\n\t"
    "v_mfma_f32_16x16x32_bf16 v[52:55], a[148:151], v[148:151], v[52:55]\n\t"
    "v_exp_f32_e32 v45, v45\n\t"
    "v_mfma_f32_16x16x32_bf16 v[52:55], a[144:147], v[144:147], v[52:55]\n\t"
    "buffer_load_dwordx4 v235, s[64:67], s76 offen offset:1024 lds\n\t"
    "v_exp_f32_e32 v46, v46\n\t"
    "v_mfma_f32_16x16x32_bf16 v[56:59], a[160:163], v[144:147], v[172:175]\n\t"
    "v_exp_f32_e32 v47, v47\n\t"
    "v_mfma_f32_16x16x32_bf16 v[56:59], a[164:167], v[148:151], v[56:59]\n\t"
    "v_cvt_pk_bf16_f32 v78, v28, v29\n\t"
    "v_cvt_pk_bf16_f32 v79, v30, v31\n\t"
    "v_mfma_f32_16x16x32_bf16 v[56:59], a[168:171], v[152:155], v[56:59]\n\t"
    "buffer_load_dwordx4 v236, s[64:67], s76 offen offset:2048 lds\n\t"
    "v_cvt_pk_bf16_f32 v80, v32, v33\n\t"
    "v_mfma_f32_16x16x32_bf16 v[56:59], a[172:175], v[156:159], v[56:59]\n\t"
    "v_cvt_pk_bf16_f32 v81, v34, v35\n\t"
    "v_mfma_f32_16x16x32_bf16 v[60:63], a[188:191], v[156:159], v[172:175]\n\t"
    "v_cvt_pk_bf16_f32 v82, v36, v37\n\t"
    "v_mfma_f32_16x16x32_bf16 v[60:63], a[184:187], v[152:155], v[60:63]\n\t"
    "buffer_load_dwordx4 v237, s[64:67], s76 offen offset:3072 lds\n\t"
    "s_add_u32 s76, s76, 65536\n\t"
    "s_and_b32 s76, s76, 0xfffff\n\t"
    "v_cvt_pk_bf16_f32 v83, v38, v39\n\t"
    "v_mfma_f32_16x16x32_bf16 v[60:63], a[180:183], v[148:151], v[60:63]\n\t"
    "v_cvt_pk_bf16_f32 v84, v40, v41\n\t"
    "v_mfma_f32_16x16x32_bf16 v[60:63], a[176:179], v[144:147], v[60:63]\n\t"
    "v_cvt_pk_bf16_f32 v85, v42, v43\n\t"
    "v_mfma_f32_16x16x32_bf16 a[4:7], a[192:195], v[72:75], a[4:7]\n\t"
    "s_add_u32 m0, s78, 147456\n\t"
    "s_nop 0\n\t"
    "buffer_load_dwordx4 v234, s[64:67], s76 offen lds\n\t"
    "v_cvt_pk_bf16_f32 v86, v44, v45\n\t"
    "v_mfma_f32_16x16x32_bf16 a[4:7], a[196:199], v[76:79], a[4:7]\n\t"
    "v_cvt_pk_bf16_f32 v87, v46, v47\n\t"
    "v_mfma_f32_16x16x32_bf16 a[20:23], a[200:203], v[72:75], a[20:23]\n\t"
    "v_add_f32_e32 v215, v215, v25\n\t"
    "v_mfma_f32_16x16x32_bf16 a[20:23], a[204:207], v[76:79], a[20:23]\n\t"
    "buffer_load_dwordx4 v235, s[64:67], s76 offen offset:1024 lds\n\t"
    "v_add_f32_e32 v215, v215, v26\n\t"
    "v_mfma_f32_16x16x32_bf16 a[36:39], a[208:211], v[72:75], a[36:39]\n\t"
    "v_add_f32_e32 v215, v215, v27\n\t"
    "v_mfma_f32_16x16x32_bf16 a[36:39], a[212:215], v[76:79], a[36:39]\n\t"
    "v_add_f32_e32 v215, v215, v28\n\t"
    "v_mfma_f32_16x16x32_bf16 a[52:55], a[216:219], v[72:75], a[52:55]\n\t"
    "buffer_load_dwordx4 v236, s[64:67], s76 offen offset:2048 lds\n\t"
    "v_add_f32_e32 v215, v215, v29\n\t"
    "v_mfma_f32_16x16x32_bf16 a[52:55], a[220:223], v[76:79], a[52:55]\n\t"
    "v_add_f32_e32 v215, v215, v30\n\t"
    "v_mfma_f32_16x16x32_bf16 a[68:71], a[224:227], v[72:75], a[68:71]\n\t"
    "v_add_f32_e32 v215, v215, v31\n\t"
    "v_mfma_f32_16x16x32_bf16 a[68:71], a[228:231], v[76:79], a[68:71]\n\t"
    "buffer_load_dwordx4 v237, s[64:67], s76 offen offset:3072 lds\n\t"
    "s_add_u32 s76, s76, 65536\n\t"
    "s_and_b32 s76, s76, 0xfffff\n\t"
    "v_add_f32_e32 v216, v216, v40\n\t"
    "v_mfma_f32_16x16x32_bf16 a[84:87], a[232:235], v[72:75], a[84:87]\n\t"
    "v_add_f32_e32 v216, v216, v41\n\t"
    "v_mfma_f32_16x16x32_bf16 a[84:87], a[236:239], v[76:79], a[84:87]\n\t"
    "v_add_f32_e32 v216, v216, v42\n\t"
    "v_mfma_f32_16x16x32_bf16 a[100:103], a[240:243], v[72:75], a[100:103]\n\t"
    "v_add_f32_e32 v216, v216, v43\n\t"
    "v_mfma_f32_16x16x32_bf16 a[100:103], a[244:247], v[76:79], a[100:103]\n\t"
    "v_add_f32_e32 v216, v216, v44\n\t"
    "v_mfma_f32_16x16x32_bf16 a[116:119], a[248:251], v[72:75], a[116:119]\n\t"
    "v_exp_f32_e32 v48, v48\n\t"
    "v_mfma_f32_16x16x32_bf16 a[116:119], a[252:255], v[76:79], a[116:119]\n\t"
    "v_exp_f32_e32 v49, v49\n\t"
    "v_mfma_f32_16x16x32_bf16 v[180:183], v[192:195], v[72:75], v[180:183]\n\t"
    "v_cvt_pk_bf16_f32 v88, v48, v49\n\t"
    "v_exp_f32_e32 v50, v50\n\t"
    "v_exp_f32_e32 v51, v51\n\t"
    "v_exp_f32_e32 v52, v52\n\t"
    "v_exp_f32_e32 v53, v53\n\t"
    "v_cvt_pk_bf16_f32 v89, v50, v51\n\t"
    "v_exp_f32_e32 v54, v54\n\t"
    "v_exp_f32_e32 v55, v55\n\t"
    "v_cvt_pk_bf16_f32 v90, v52, v53\n\t"
    "v_exp_f32_e32 v56, v56\n\t"
    "v_exp_f32_e32 v57, v57\n\t"
    "v_exp_f32_e32 v58, v58\n\t"
    "v_exp_f32_e32 v59, v59\n\t"
    "v_exp_f32_e32 v60, v60\n\t"
    "v_exp_f32_e32 v61, v61\n\t"
    "v_cvt_pk_bf16_f32 v91, v54, v55\n\t"
    "v_exp_f32_e32 v62, v62\n\t"
    "v_mfma_f32_16x16x32_bf16 a[8:11], a[192:195], v[80:83], a[8:11]\n\t"
    "v_exp_f32_e32 v63, v63\n\t"
    "v_mfma_f32_16x16x32_bf16 a[8:11], a[196:199], v[84:87], a[8:11]\n\t"
    "v_mfma_f32_16x16x32_bf16 a[24:27], a[200:203], v[80:83], a[24:27]\n\t"
    "v_mfma_f32_16x16x32_bf16 a[24:27], a[204:207], v[84:87], a[24:27]\n\t"
    "v_mfma_f32_16x16x32_bf16 a[40:43], a[208:211], v[80:83], a[40:43]\n\t"
    "v_mfma_f32_16x16x32_bf16 a[40:43], a[212:215], v[84:87], a[40:43]\n\t"
    "v_mfma_f32_16x16x32_bf16 a[56:59], a[216:219], v[80:83], a[56:59]\n\t"
    "v_mfma_f32_16x16x32_bf16 a[56:59], a[220:223], v[84:87], a[56:59]\n\t"
    "v_mfma_f32_16x16x32_bf16 a[72:75], a[224:227], v[80:83], a[72:75]\n\t"
    "v_cvt_pk_bf16_f32 v92, v56, v57\n\t"
    "v_mfma_f32_16x16x32_bf16 a[72:75], a[228:231], v[84:87], a[72:75]\n\t"
    "v_mfma_f32_16x16x32_bf16 a[88:91], a[232:235], v[80:83], a[88:91]\n\t"
    "v_mfma_f32_16x16x32_bf16 a[88:91], a[236:239], v[84:87], a[88:91]\n\t"
    "v_mfma_f32_16x16x32_bf16 a[104:107], a[240:243], v[80:83], a[104:107]\n\t"
    "v_mfma_f32_16x16x32_bf16 a[104:107], a[244:247], v[84:87], a[104:107]\n\t"
    "v_mfma_f32_16x16x32_bf16 a[120:123], a[248:251], v[80:83], a[120:123]\n\t"
    "v_mfma_f32_16x16x32_bf16 a[120:123], a[252:255], v[84:87], a[120:123]\n\t"
    "v_mfma_f32_16x16x32_bf16 v[184:187], v[192:195], v[80:83], v[184:187]\n\t"
    "v_cvt_pk_bf16_f32 v93, v58, v59\n\t"
    "v_cvt_pk_bf16_f32 v94, v60, v61\n\t"
    "v_cvt_pk_bf16_f32 v95, v62, v63\n\t"
    "v_add_f32_e32 v216, v216, v45\n\t"
    "v_add_f32_e32 v216, v216, v46\n\t"
    "v_add_f32_e32 v216, v216, v47\n\t"
    "v_mfma_f32_16x16x32_bf16 a[12:15], a[192:195], v[88:91], a[12:15]\n\t"
    "v_mfma_f32_16x16x32_bf16 a[12:15], a[196:199], v[92:95], a[12:15]\n\t"
    "v_mfma_f32_16x16x32_bf16 a[28:31], a[200:203], v[88:91], a[28:31]\n\t"
    "v_mfma_f32_16x16x32_bf16 a[28:31], a[204:207], v[92:95], a[28:31]\n\t"
    "v_mfma_f32_16x16x32_bf16 a[44:47], a[208:211], v[88:91], a[44:47]\n\t"
    "v_mfma_f32_16x16x32_bf16 a[44:47], a[212:215], v[92:95], a[44:47]\n\t"
    "v_mfma_f32_16x16x32_bf16 a[60:63], a[216:219], v[88:91], a[60:63]\n\t"
    "v_mfma_f32_16x16x32_bf16 a[60:63], a[220:223], v[92:95], a[60:63]\n\t"
    "v_add_f32_e32 v217, v217, v56\n\t"
    "v_mfma_f32_16x16x32_bf16 a[76:79], a[224:227], v[88:91], a[76:79]\n\t"
    "v_add_f32_e32 v217, v217, v57\n\t"
    "v_mfma_f32_16x16x32_bf16 a[76:79], a[228:231], v[92:95], a[76:79]\n\t"
    "v_add_f32_e32 v217, v217, v58\n\t"
    "v_mfma_f32_16x16x32_bf16 a[92:95], a[232:235], v[88:91], a[92:95]\n\t"
    "v_add_f32_e32 v217, v217, v59\n\t"
    "v_mfma_f32_16x16x32_bf16 a[92:95], a[236:239], v[92:95], a[92:95]\n\t"
    "v_add_f32_e32 v217, v217, v60\n\t"
    "v_mfma_f32_16x16x32_bf16 a[108:111], a[240:243], v[88:91], a[108:111]\n\t"
    "v_add_f32_e32 v217, v217, v61\n\t"
    "v_mfma_f32_16x16x32_bf16 a[108:111], a[244:247], v[92:95], a[108:111]\n\t"
    "v_add_f32_e32 v217, v217, v62\n\t"
    "v_mfma_f32_16x16x32_bf16 a[124:127], a[248:251], v[88:91], a[124:127]\n\t"
    "v_add_f32_e32 v217, v217, v63\n\t"
    "v_mfma_f32_16x16x32_bf16 a[124:127], a[252:255], v[92:95], a[124:127]\n\t"
    "v_mfma_f32_16x16x32_bf16 v[188:191], v[192:195], v[88:91], v[188:191]\n\t"
    "s_nop 7\n\t"
    "v_mov_b32_e32 v0, v214\n\t"
    "v_mov_b32_e32 v1, v215\n\t"
    "v_mov_b32_e32 v2, v216\n\t"
    "v_mov_b32_e32 v3, v217\n\t"
    "s_nop 1\n\t"
    "v_permlane32_swap_b32_e32 v214, v0\n\t"
    "v_permlane32_swap_b32_e32 v215, v1\n\t"
    "v_permlane32_swap_b32_e32 v216, v2\n\t"
    "v_permlane32_swap_b32_e32 v217, v3\n\t"
    "s_nop 1\n\t"
    "v_add_f32_e32 v214, v214, v0\n\t"
    "v_add_f32_e32 v215, v215, v1\n\t"
    "v_add_f32_e32 v216, v216, v2\n\t"
    "v_add_f32_e32 v217, v217, v3\n\t"
    "v_mov_b32_e32 v0, v214\n\t"
    "v_mov_b32_e32 v1, v215\n\t"
    "v_mov_b32_e32 v2, v216\n\t"
    "v_mov_b32_e32 v3, v217\n\t"
    "s_nop 1\n\t"
    "v_permlane16_swap_b32_e32 v214, v0\n\t"
    "v_permlane16_swap_b32_e32 v215, v1\n\t"
    "v_permlane16_swap_b32_e32 v216, v2\n\t"
    "v_permlane16_swap_b32_e32 v217, v3\n\t"
    "s_nop 1\n\t"
    "v_add_f32_e32 v214, v214, v0\n\t"
    "v_add_f32_e32 v215, v215, v1\n\t"
    "v_add_f32_e32 v216, v216, v2\n\t"
    "v_add_f32_e32 v217, v217, v3\n\t"
    "v_add_f32_e32 v214, v214, v176\n\t"
    "v_add_f32_e32 v215, v215, v180\n\t"
    "v_add_f32_e32 v216, v216, v184\n\t"
    "v_add_f32_e32 v217, v217, v188\n\t"
    "s_mov_b64 s[54:55], 0\n\t"
    "s_mov_b32 s53, 0x4f800000\n\t"
    "v_cmp_ngt_f32_e32 vcc, s53, v214\n\t"
    "s_nop 4\n\t"
    "s_or_b64 s[54:55], s[54:55], vcc\n\t"
    "v_cmp_ngt_f32_e32 vcc, s53, v215\n\t"
    "s_nop 4\n\t"
    "s_or_b64 s[54:55], s[54:55], vcc\n\t"
    "v_cmp_ngt_f32_e32 vcc, s53, v216\n\t"
    "s_nop 4\n\t"
    "s_or_b64 s[54:55], s[54:55], vcc\n\t"
    "v_cmp_ngt_f32_e32 vcc, s53, v217\n\t"
    "s_nop 4\n\t"
    "s_or_b64 s[54:55], s[54:55], vcc\n\t"
    "v_rcp_f32_e32 v8, v214\n\t"
    "v_rcp_f32_e32 v10, v215\n\t"
    "v_rcp_f32_e32 v12, v216\n\t"
    "v_rcp_f32_e32 v14, v217\n\t"
    "s_nop 1\n\t"
    "s_movk_i32 s53, 0\n\t"
    "v_accvgpr_read_b32 v16, a0\n\t"
    "v_accvgpr_read_b32 v17, a1\n\t"
    "v_accvgpr_read_b32 v18, a2\n\t"
    "v_accvgpr_read_b32 v19, a3\n\t"
    "v_accvgpr_read_b32 v20, a16\n\t"
    "v_accvgpr_read_b32 v21, a17\n\t"
    "v_accvgpr_read_b32 v22, a18\n\t"
    "v_accvgpr_read_b32 v23, a19\n\t"
    "v_pk_mul_f32 v[16:17], v[16:17], v[8:9] op_sel:[0,0] op_sel_hi:[1,0]\n\t"
    "v_pk_mul_f32 v[18:19], v[18:19], v[8:9] op_sel:[0,0] op_sel_hi:[1,0]\n\t"
    "v_pk_mul_f32 v[20:21], v[20:21], v[8:9] op_sel:[0,0] op_sel_hi:[1,0]\n\t"
    "v_pk_mul_f32 v[22:23], v[22:23], v[8:9] op_sel:[0,0] op_sel_hi:[1,0]\n\t"
    "v_cvt_pk_bf16_f32 v128, v16, v17\n\t"
    "v_cvt_pk_bf16_f32 v129, v18, v19\n\t"
    "v_cvt_pk_bf16_f32 v130, v20, v21\n\t"
    "v_cvt_pk_bf16_f32 v131, v22, v23\n\t"
    "buffer_store_dwordx4 v[128:131], v232, s[24:27], s53 offen sc1\n\t"
    "v_accvgpr_read_b32 v24, a32\n\t"
    "v_accvgpr_read_b32 v25, a33\n\t"
    "v_accvgpr_read_b32 v26, a34\n\t"
    "v_accvgpr_read_b32 v27, a35\n\t"
    "v_accvgpr_read_b32 v28, a48\n\t"
    "v_accvgpr_read_b32 v29, a49\n\t"
    "v_accvgpr_read_b32 v30, a50\n\t"
    "v_accvgpr_read_b32 v31, a51\n\t"
    "v_pk_mul_f32 v[24:25], v[24:25], v[8:9] op_sel:[0,0] op_sel_hi:[1,0]\n\t"
    "v_pk_mul_f32 v[26:27], v[26:27], v[8:9] op_sel:[0,0] op_sel_hi:[1,0]\n\t"
    "v_pk_mul_f32 v[28:29], v[28:29], v[8:9] op_sel:[0,0] op_sel_hi:[1,0]\n\t"
    "v_pk_mul_f32 v[30:31], v[30:31], v[8:9] op_sel:[0,0] op_sel_hi:[1,0]\n\t"
    "v_cvt_pk_bf16_f32 v132, v24, v25\n\t"
    "v_cvt_pk_bf16_f32 v133, v26, v27\n\t"
    "v_cvt_pk_bf16_f32 v134, v28, v29\n\t"
    "v_cvt_pk_bf16_f32 v135, v30, v31\n\t"
    "buffer_store_dwordx4 v[132:135], v232, s[24:27], s53 offen offset:1024 sc1\n\t"
    "s_movk_i32 s53, 8192\n\t"
    "v_accvgpr_read_b32 v32, a64\n\t"
    "v_accvgpr_read_b32 v33, a65\n\t"
    "v_accvgpr_read_b32 v34, a66\n\t"
    "v_accvgpr_read_b32 v35, a67\n\t"
    "v_accvgpr_read_b32 v36, a80\n\t"
    "v_accvgpr_read_b32 v37, a81\n\t"
    "v_accvgpr_read_b32 v38, a82\n\t"
    "v_accvgpr_read_b32 v39, a83\n\t"
    "v_pk_mul_f32 v[32:33], v[32:33], v[8:9] op_sel:[0,0] op_sel_hi:[1,0]\n\t"
    "v_pk_mul_f32 v[34:35], v[34:35], v[8:9] op_sel:[0,0] op_sel_hi:[1,0]\n\t"
    "v_pk_mul_f32 v[36:37], v[36:37], v[8:9] op_sel:[0,0] op_sel_hi:[1,0]\n\t"
    "v_pk_mul_f32 v[38:39], v[38:39], v[8:9] op_sel:[0,0] op_sel_hi:[1,0]\n\t"
    "v_cvt_pk_bf16_f32 v136, v32, v33\n\t"
    "v_cvt_pk_bf16_f32 v137, v34, v35\n\t"
    "v_cvt_pk_bf16_f32 v138, v36, v37\n\t"
    "v_cvt_pk_bf16_f32 v139, v38, v39\n\t"
    "buffer_store_dwordx4 v[136:139], v232, s[24:27], s53 offen sc1\n\t"
    "v_accvgpr_read_b32 v40, a96\n\t"
    "v_accvgpr_read_b32 v41, a97\n\t"
    "v_accvgpr_read_b32 v42, a98\n\t"
    "v_accvgpr_read_b32 v43, a99\n\t"
    "v_accvgpr_read_b32 v44, a112\n\t"
    "v_accvgpr_read_b32 v45, a113\n\t"
    "v_accvgpr_read_b32 v46, a114\n\t"
    "v_accvgpr_read_b32 v47, a115\n\t"
    "v_pk_mul_f32 v[40:41], v[40:41], v[8:9] op_sel:[0,0] op_sel_hi:[1,0]\n\t"
    "v_pk_mul_f32 v[42:43], v[42:43], v[8:9] op_sel:[0,0] op_sel_hi:[1,0]\n\t"
    "v_pk_mul_f32 v[44:45], v[44:45], v[8:9] op_sel:[0,0] op_sel_hi:[1,0]\n\t"
    "v_pk_mul_f32 v[46:47], v[46:47], v[8:9] op_sel:[0,0] op_sel_hi:[1,0]\n\t"
    "v_cvt_pk_bf16_f32 v140, v40, v41\n\t"
    "v_cvt_pk_bf16_f32 v141, v42, v43\n\t"
    "v_cvt_pk_bf16_f32 v142, v44, v45\n\t"
    "v_cvt_pk_bf16_f32 v143, v46, v47\n\t"
    "buffer_store_dwordx4 v[140:143], v232, s[24:27], s53 offen offset:1024 sc1\n\t"
    "s_movk_i32 s53, 2048\n\t"
    "v_accvgpr_read_b32 v48, a4\n\t"
    "v_accvgpr_read_b32 v49, a5\n\t"
    "v_accvgpr_read_b32 v50, a6\n\t"
    "v_accvgpr_read_b32 v51, a7\n\t"
    "v_accvgpr_read_b32 v52, a20\n\t"
    "v_accvgpr_read_b32 v53, a21\n\t"
    "v_accvgpr_read_b32 v54, a22\n\t"
    "v_accvgpr_read_b32 v55, a23\n\t"
    "v_pk_mul_f32 v[48:49], v[48:49], v[10:11] op_sel:[0,0] op_sel_hi:[1,0]\n\t"
    "v_pk_mul_f32 v[50:51], v[50:51], v[10:11] op_sel:[0,0] op_sel_hi:[1,0]\n\t"
    "v_pk_mul_f32 v[52:53], v[52:53], v[10:11] op_sel:[0,0] op_sel_hi:[1,0]\n\t"
    "v_pk_mul_f32 v[54:55], v[54:55], v[10:11] op_sel:[0,0] op_sel_hi:[1,0]\n\t"
    "v_cvt_pk_bf16_f32 v144, v48, v49\n\t"
    "v_cvt_pk_bf16_f32 v145, v50, v51\n\t"
    "v_cvt_pk_bf16_f32 v146, v52, v53\n\t"
    "v_cvt_pk_bf16_f32 v147, v54, v55\n\t"
    "buffer_store_dwordx4 v[144:147], v232, s[24:27], s53 offen sc1\n\t"
    "v_accvgpr_read_b32 v56, a36\n\t"
    "v_accvgpr_read_b32 v57, a37\n\t"
    "v_accvgpr_read_b32 v58, a38\n\t"
    "v_accvgpr_read_b32 v59, a39\n\t"
    "v_accvgpr_read_b32 v60, a52\n\t"
    "v_accvgpr_read_b32 v61, a53\n\t"
    "v_accvgpr_read_b32 v62, a54\n\t"
    "v_accvgpr_read_b32 v63, a55\n\t"
    "v_pk_mul_f32 v[56:57], v[56:57], v[10:11] op_sel:[0,0] op_sel_hi:[1,0]\n\t"
    "v_pk_mul_f32 v[58:59], v[58:59], v[10:11] op_sel:[0,0] op_sel_hi:[1,0]\n\t"
    "v_pk_mul_f32 v[60:61], v[60:61], v[10:11] op_sel:[0,0] op_sel_hi:[1,0]\n\t"
    "v_pk_mul_f32 v[62:63], v[62:63], v[10:11] op_sel:[0,0] op_sel_hi:[1,0]\n\t"
    "v_cvt_pk_bf16_f32 v148, v56, v57\n\t"
    "v_cvt_pk_bf16_f32 v149, v58, v59\n\t"
    "v_cvt_pk_bf16_f32 v150, v60, v61\n\t"
    "v_cvt_pk_bf16_f32 v151, v62, v63\n\t"
    "buffer_store_dwordx4 v[148:151], v232, s[24:27], s53 offen offset:1024 sc1\n\t"
    "s_movk_i32 s53, 10240\n\t"
    "v_accvgpr_read_b32 v16, a68\n\t"
    "v_accvgpr_read_b32 v17, a69\n\t"
    "v_accvgpr_read_b32 v18, a70\n\t"
    "v_accvgpr_read_b32 v19, a71\n\t"
    "v_accvgpr_read_b32 v20, a84\n\t"
    "v_accvgpr_read_b32 v21, a85\n\t"
    "v_accvgpr_read_b32 v22, a86\n\t"
    "v_accvgpr_read_b32 v23, a87\n\t"
    "v_pk_mul_f32 v[16:17], v[16:17], v[10:11] op_sel:[0,0] op_sel_hi:[1,0]\n\t"
    "v_pk_mul_f32 v[18:19], v[18:19], v[10:11] op_sel:[0,0] op_sel_hi:[1,0]\n\t"
    "v_pk_mul_f32 v[20:21], v[20:21], v[10:11] op_sel:[0,0] op_sel_hi:[1,0]\n\t"
    "v_pk_mul_f32 v[22:23], v[22:23], v[10:11] op_sel:[0,0] op_sel_hi:[1,0]\n\t"
    "v_cvt_pk_bf16_f32 v152, v16, v17\n\t"
    "v_cvt_pk_bf16_f32 v153, v18, v19\n\t"
    "v_cvt_pk_bf16_f32 v154, v20, v21\n\t"
    "v_cvt_pk_bf16_f32 v155, v22, v23\n\t"
    "buffer_store_dwordx4 v[152:155], v232, s[24:27], s53 offen sc1\n\t"
    "v_accvgpr_read_b32 v24, a100\n\t"
    "v_accvgpr_read_b32 v25, a101\n\t"
    "v_accvgpr_read_b32 v26, a102\n\t"
    "v_accvgpr_read_b32 v27, a103\n\t"
    "v_accvgpr_read_b32 v28, a116\n\t"
    "v_accvgpr_read_b32 v29, a117\n\t"
    "v_accvgpr_read_b32 v30, a118\n\t"
    "v_accvgpr_read_b32 v31, a119\n\t"
    "v_pk_mul_f32 v[24:25], v[24:25], v[10:11] op_sel:[0,0] op_sel_hi:[1,0]\n\t"
    "v_pk_mul_f32 v[26:27], v[26:27], v[10:11] op_sel:[0,0] op_sel_hi:[1,0]\n\t"
    "v_pk_mul_f32 v[28:29], v[28:29], v[10:11] op_sel:[0,0] op_sel_hi:[1,0]\n\t"
    "v_pk_mul_f32 v[30:31], v[30:31], v[10:11] op_sel:[0,0] op_sel_hi:[1,0]\n\t"
    "v_cvt_pk_bf16_f32 v156, v24, v25\n\t"
    "v_cvt_pk_bf16_f32 v157, v26, v27\n\t"
    "v_cvt_pk_bf16_f32 v158, v28, v29\n\t"
    "v_cvt_pk_bf16_f32 v159, v30, v31\n\t"
    "buffer_store_dwordx4 v[156:159], v232, s[24:27], s53 offen offset:1024 sc1\n\t"
    "s_movk_i32 s53, 4096\n\t"
    "v_accvgpr_read_b32 v32, a8\n\t"
    "v_accvgpr_read_b32 v33, a9\n\t"
    "v_accvgpr_read_b32 v34, a10\n\t"
    "v_accvgpr_read_b32 v35, a11\n\t"
    "v_accvgpr_read_b32 v36, a24\n\t"
    "v_accvgpr_read_b32 v37, a25\n\t"
    "v_accvgpr_read_b32 v38, a26\n\t"
    "v_accvgpr_read_b32 v39, a27\n\t"
    "v_pk_mul_f32 v[32:33], v[32:33], v[12:13] op_sel:[0,0] op_sel_hi:[1,0]\n\t"
    "v_pk_mul_f32 v[34:35], v[34:35], v[12:13] op_sel:[0,0] op_sel_hi:[1,0]\n\t"
    "v_pk_mul_f32 v[36:37], v[36:37], v[12:13] op_sel:[0,0] op_sel_hi:[1,0]\n\t"
    "v_pk_mul_f32 v[38:39], v[38:39], v[12:13] op_sel:[0,0] op_sel_hi:[1,0]\n\t"
    "v_cvt_pk_bf16_f32 v160, v32, v33\n\t"
    "v_cvt_pk_bf16_f32 v161, v34, v35\n\t"
    "v_cvt_pk_bf16_f32 v162, v36, v37\n\t"
    "v_cvt_pk_bf16_f32 v163, v38, v39\n\t"
    "buffer_store_dwordx4 v[160:163], v232, s[24:27], s53 offen sc1\n\t"
    "v_accvgpr_read_b32 v40, a40\n\t"
    "v_accvgpr_read_b32 v41, a41\n\t"
    "v_accvgpr_read_b32 v42, a42\n\t"
    "v_accvgpr_read_b32 v43, a43\n\t"
    "v_accvgpr_read_b32 v44, a56\n\t"
    "v_accvgpr_read_b32 v45, a57\n\t"
    "v_accvgpr_read_b32 v46, a58\n\t"
    "v_accvgpr_read_b32 v47, a59\n\t"
    "v_pk_mul_f32 v[40:41], v[40:41], v[12:13] op_sel:[0,0] op_sel_hi:[1,0]\n\t"
    "v_pk_mul_f32 v[42:43], v[42:43], v[12:13] op_sel:[0,0] op_sel_hi:[1,0]\n\t"
    "v_pk_mul_f32 v[44:45], v[44:45], v[12:13] op_sel:[0,0] op_sel_hi:[1,0]\n\t"
    "v_pk_mul_f32 v[46:47], v[46:47], v[12:13] op_sel:[0,0] op_sel_hi:[1,0]\n\t"
    "v_cvt_pk_bf16_f32 v164, v40, v41\n\t"
    "v_cvt_pk_bf16_f32 v165, v42, v43\n\t"
    "v_cvt_pk_bf16_f32 v166, v44, v45\n\t"
    "v_cvt_pk_bf16_f32 v167, v46, v47\n\t"
    "buffer_store_dwordx4 v[164:167], v232, s[24:27], s53 offen offset:1024 sc1\n\t"
    "s_movk_i32 s53, 12288\n\t"
    "v_accvgpr_read_b32 v48, a72\n\t"
    "v_accvgpr_read_b32 v49, a73\n\t"
    "v_accvgpr_read_b32 v50, a74\n\t"
    "v_accvgpr_read_b32 v51, a75\n\t"
    "v_accvgpr_read_b32 v52, a88\n\t"
    "v_accvgpr_read_b32 v53, a89\n\t"
    "v_accvgpr_read_b32 v54, a90\n\t"
    "v_accvgpr_read_b32 v55, a91\n\t"
    "v_pk_mul_f32 v[48:49], v[48:49], v[12:13] op_sel:[0,0] op_sel_hi:[1,0]\n\t"
    "v_pk_mul_f32 v[50:51], v[50:51], v[12:13] op_sel:[0,0] op_sel_hi:[1,0]\n\t"
    "v_pk_mul_f32 v[52:53], v[52:53], v[12:13] op_sel:[0,0] op_sel_hi:[1,0]\n\t"
    "v_pk_mul_f32 v[54:55], v[54:55], v[12:13] op_sel:[0,0] op_sel_hi:[1,0]\n\t"
    "v_cvt_pk_bf16_f32 v168, v48, v49\n\t"
    "v_cvt_pk_bf16_f32 v169, v50, v51\n\t"
    "v_cvt_pk_bf16_f32 v170, v52, v53\n\t"
    "v_cvt_pk_bf16_f32 v171, v54, v55\n\t"
    "buffer_store_dwordx4 v[168:171], v232, s[24:27], s53 offen sc1\n\t"
    "v_accvgpr_read_b32 v56, a104\n\t"
    "v_accvgpr_read_b32 v57, a105\n\t"
    "v_accvgpr_read_b32 v58, a106\n\t"
    "v_accvgpr_read_b32 v59, a107\n\t"
    "v_accvgpr_read_b32 v60, a120\n\t"
    "v_accvgpr_read_b32 v61, a121\n\t"
    "v_accvgpr_read_b32 v62, a122\n\t"
    "v_accvgpr_read_b32 v63, a123\n\t"
    "v_pk_mul_f32 v[56:57], v[56:57], v[12:13] op_sel:[0,0] op_sel_hi:[1,0]\n\t"
    "v_pk_mul_f32 v[58:59], v[58:59], v[12:13] op_sel:[0,0] op_sel_hi:[1,0]\n\t"
    "v_pk_mul_f32 v[60:61], v[60:61], v[12:13] op_sel:[0,0] op_sel_hi:[1,0]\n\t"
    "v_pk_mul_f32 v[62:63], v[62:63], v[12:13] op_sel:[0,0] op_sel_hi:[1,0]\n\t"
    "v_cvt_pk_bf16_f32 v172, v56, v57\n\t"
    "v_cvt_pk_bf16_f32 v173, v58, v59\n\t"
    "v_cvt_pk_bf16_f32 v174, v60, v61\n\t"
    "v_cvt_pk_bf16_f32 v175, v62, v63\n\t"
    "buffer_store_dwordx4 v[172:175], v232, s[24:27], s53 offen offset:1024 sc1\n\t"
    "s_movk_i32 s53, 6144\n\t"
    "v_accvgpr_read_b32 v16, a12\n\t"
    "v_accvgpr_read_b32 v17, a13\n\t"
    "v_accvgpr_read_b32 v18, a14\n\t"
    "v_accvgpr_read_b32 v19, a15\n\t"
    "v_accvgpr_read_b32 v20, a28\n\t"
    "v_accvgpr_read_b32 v21, a29\n\t"
    "v_accvgpr_read_b32 v22, a30\n\t"
    "v_accvgpr_read_b32 v23, a31\n\t"
    "v_pk_mul_f32 v[16:17], v[16:17], v[14:15] op_sel:[0,0] op_sel_hi:[1,0]\n\t"
    "v_pk_mul_f32 v[18:19], v[18:19], v[14:15] op_sel:[0,0] op_sel_hi:[1,0]\n\t"
    "v_pk_mul_f32 v[20:21], v[20:21], v[14:15] op_sel:[0,0] op_sel_hi:[1,0]\n\t"
    "v_pk_mul_f32 v[22:23], v[22:23], v[14:15] op_sel:[0,0] op_sel_hi:[1,0]\n\t"
    "v_cvt_pk_bf16_f32 v176, v16, v17\n\t"
    "v_cvt_pk_bf16_f32 v177, v18, v19\n\t"
    "v_cvt_pk_bf16_f32 v178, v20, v21\n\t"
    "v_cvt_pk_bf16_f32 v179, v22, v23\n\t"
    "buffer_store_dwordx4 v[176:179], v232, s[24:27], s53 offen sc1\n\t"
    "v_accvgpr_read_b32 v24, a44\n\t"
    "v_accvgpr_read_b32 v25, a45\n\t"
    "v_accvgpr_read_b32 v26, a46\n\t"
    "v_accvgpr_read_b32 v27, a47\n\t"
    "v_accvgpr_read_b32 v28, a60\n\t"
    "v_accvgpr_read_b32 v29, a61\n\t"
    "v_accvgpr_read_b32 v30, a62\n\t"
    "v_accvgpr_read_b32 v31, a63\n\t"
    "v_pk_mul_f32 v[24:25], v[24:25], v[14:15] op_sel:[0,0] op_sel_hi:[1,0]\n\t"
    "v_pk_mul_f32 v[26:27], v[26:27], v[14:15] op_sel:[0,0] op_sel_hi:[1,0]\n\t"
    "v_pk_mul_f32 v[28:29], v[28:29], v[14:15] op_sel:[0,0] op_sel_hi:[1,0]\n\t"
    "v_pk_mul_f32 v[30:31], v[30:31], v[14:15] op_sel:[0,0] op_sel_hi:[1,0]\n\t"
    "v_cvt_pk_bf16_f32 v180, v24, v25\n\t"
    "v_cvt_pk_bf16_f32 v181, v26, v27\n\t"
    "v_cvt_pk_bf16_f32 v182, v28, v29\n\t"
    "v_cvt_pk_bf16_f32 v183, v30, v31\n\t"
    "buffer_store_dwordx4 v[180:183], v232, s[24:27], s53 offen offset:1024 sc1\n\t"
    "s_movk_i32 s53, 14336\n\t"
    "v_accvgpr_read_b32 v32, a76\n\t"
    "v_accvgpr_read_b32 v33, a77\n\t"
    "v_accvgpr_read_b32 v34, a78\n\t"
    "v_accvgpr_read_b32 v35, a79\n\t"
    "v_accvgpr_read_b32 v36, a92\n\t"
    "v_accvgpr_read_b32 v37, a93\n\t"
    "v_accvgpr_read_b32 v38, a94\n\t"
    "v_accvgpr_read_b32 v39, a95\n\t"
    "v_pk_mul_f32 v[32:33], v[32:33], v[14:15] op_sel:[0,0] op_sel_hi:[1,0]\n\t"
    "v_pk_mul_f32 v[34:35], v[34:35], v[14:15] op_sel:[0,0] op_sel_hi:[1,0]\n\t"
    "v_pk_mul_f32 v[36:37], v[36:37], v[14:15] op_sel:[0,0] op_sel_hi:[1,0]\n\t"
    "v_pk_mul_f32 v[38:39], v[38:39], v[14:15] op_sel:[0,0] op_sel_hi:[1,0]\n\t"
    "v_cvt_pk_bf16_f32 v184, v32, v33\n\t"
    "v_cvt_pk_bf16_f32 v185, v34, v35\n\t"
    "v_cvt_pk_bf16_f32 v186, v36, v37\n\t"
    "v_cvt_pk_bf16_f32 v187, v38, v39\n\t"
    "buffer_store_dwordx4 v[184:187], v232, s[24:27], s53 offen sc1\n\t"
    "v_accvgpr_read_b32 v40, a108\n\t"
    "v_accvgpr_read_b32 v41, a109\n\t"
    "v_accvgpr_read_b32 v42, a110\n\t"
    "v_accvgpr_read_b32 v43, a111\n\t"
    "v_accvgpr_read_b32 v44, a124\n\t"
    "v_accvgpr_read_b32 v45, a125\n\t"
    "v_accvgpr_read_b32 v46, a126\n\t"
    "v_accvgpr_read_b32 v47, a127\n\t"
    "v_pk_mul_f32 v[40:41], v[40:41], v[14:15] op_sel:[0,0] op_sel_hi:[1,0]\n\t"
    "v_pk_mul_f32 v[42:43], v[42:43], v[14:15] op_sel:[0,0] op_sel_hi:[1,0]\n\t"
    "v_pk_mul_f32 v[44:45], v[44:45], v[14:15] op_sel:[0,0] op_sel_hi:[1,0]\n\t"
    "v_pk_mul_f32 v[46:47], v[46:47], v[14:15] op_sel:[0,0] op_sel_hi:[1,0]\n\t"
    "v_cvt_pk_bf16_f32 v188, v40, v41\n\t"
    "v_cvt_pk_bf16_f32 v189, v42, v43\n\t"
    "v_cvt_pk_bf16_f32 v190, v44, v45\n\t"
    "v_cvt_pk_bf16_f32 v191, v46, v47\n\t"
    "buffer_store_dwordx4 v[188:191], v232, s[24:27], s53 offen offset:1024 sc1\n\t"
    "s_or_b32 s60, s54, s55\n\t"
    "s_barrier\n\t"
    "s_cmp_eq_u32 s61, -1\n\t"
    "s_cbranch_scc1 Lnoown_%=\n\t"
    "v_add_u32_e32 v8, s61, v232\n\t"
    "ds_write_b128 v8, v[128:131] offset:0\n\t"
    "ds_write_b128 v8, v[132:135] offset:1024\n\t"
    "ds_write_b128 v8, v[136:139] offset:8192\n\t"
    "ds_write_b128 v8, v[140:143] offset:9216\n\t"
    "ds_write_b128 v8, v[144:147] offset:2048\n\t"
    "ds_write_b128 v8, v[148:151] offset:3072\n\t"
    "ds_write_b128 v8, v[152:155] offset:10240\n\t"
    "ds_write_b128 v8, v[156:159] offset:11264\n\t"
    "ds_write_b128 v8, v[160:163] offset:4096\n\t"
    "ds_write_b128 v8, v[164:167] offset:5120\n\t"
    "ds_write_b128 v8, v[168:171] offset:12288\n\t"
    "ds_write_b128 v8, v[172:175] offset:13312\n\t"
    "ds_write_b128 v8, v[176:179] offset:6144\n\t"
    "ds_write_b128 v8, v[180:183] offset:7168\n\t"
    "ds_write_b128 v8, v[184:187] offset:14336\n\t"
    "ds_write_b128 v8, v[188:191] offset:15360\n\t"
    "Lnoown_%=:\n\t"
    : "={s60}"(flag), "+{s76}"(swo)
    : "{v[196:199]}"(kb), "{v[200:207]}"(vb), "{v[208:211]}"(dk), "{v[212:213]}"(dvv), "{v[224:227]}"(wb), "{v[228:231]}"(wdv), "{v232}"(lane16), "{v[238:241]}"(wb2),
      "{s[8:11]}"(srdK), "{s[12:15]}"(srdV), "{s[16:19]}"(srdH), "{s[20:23]}"(srdW), "{s[24:27]}"(srdO),
      "{s29}"(w4k), "{s[32:35]}"(mk), "{s[36:39]}"(mv), "{s[40:43]}"(mw), "{s[44:47]}"(srdPF), "{s49}"(mpf),
      "{s[64:67]}"(srdWO), "{s78}"(mlw), "{v[234:237]}"(wdo),
      "{s[56:59]}"(srdB), "{s[68:71]}"(srdP), "{s[72:75]}"(srdF), "{s77}"(sfl), "{s61}"(sown), "{s62}"(sownr)
    : "memory", "vcc", "scc", "s28", "s48", "s50", "s51", "s52", "s53", "s54", "s55",
    "v0", "v1", "v2", "v3", "v4", "v5", "v6", "v7", "v8", "v9", "v10", "v11", "v12", "v13", "v14", "v15", "v16", "v17", "v18", "v19", "v20", "v21", "v22", "v23",
    "v24", "v25", "v26", "v27", "v28", "v29", "v30", "v31", "v32", "v33", "v34", "v35", "v36", "v37", "v38", "v39", "v40", "v41", "v42", "v43", "v44", "v45", "v46", "v47",
    "v48", "v49", "v50", "v51", "v52", "v53", "v54", "v55", "v56", "v57", "v58", "v59", "v60", "v61", "v62", "v63", "v64", "v65", "v66", "v67", "v68", "v69", "v70", "v71",
    "v72", "v73", "v74", "v75", "v76", "v77", "v78", "v79", "v80", "v81", "v82", "v83", "v84", "v85", "v86", "v87", "v88", "v89", "v90", "v91", "v92", "v93", "v94", "v95",
    "v96", "v97", "v98", "v99", "v100", "v101", "v102", "v103", "v104", "v105", "v106", "v107", "v108", "v109", "v110", "v111", "v112", "v113", "v114", "v115", "v116", "v117", "v118", "v119",
    "v120", "v121", "v122", "v123", "v124", "v125", "v126", "v127", "v128", "v129", "v130", "v131", "v132", "v133", "v134", "v135", "v136", "v137", "v138", "v139", "v140", "v141", "v142", "v143",
    "v144", "v145", "v146", "v147", "v148", "v149", "v150", "v151", "v152", "v153", "v154", "v155", "v156", "v157", "v158", "v159", "v160", "v161", "v162", "v163", "v164", "v165", "v166", "v167",
    "v168", "v169", "v170", "v171", "v172", "v173", "v174", "v175", "v176", "v177", "v178", "v179", "v180", "v181", "v182", "v183", "v184", "v185", "v186", "v187", "v188", "v189", "v190", "v191",
    "v192", "v193", "v194", "v195", "v214", "v215", "v216", "v217", "v218", "v219", "v220", "v221", "v222", "v223", "v242", "v243", "v244", "v245", "v246", "v247", "v248", "v249", "v250", "v251",
    "v252", "v253", "v254", "v255", "a0", "a1", "a2", "a3", "a4", "a5", "a6", "a7", "a8", "a9", "a10", "a11", "a12", "a13", "a14", "a15", "a16", "a17", "a18", "a19",
    "a20", "a21", "a22", "a23", "a24", "a25", "a26", "a27", "a28", "a29", "a30", "a31", "a32", "a33", "a34", "a35", "a36", "a37", "a38", "a39", "a40", "a41", "a42", "a43",
    "a44", "a45", "a46", "a47", "a48", "a49", "a50", "a51", "a52", "a53", "a54", "a55", "a56", "a57", "a58", "a59", "a60", "a61", "a62", "a63", "a64", "a65", "a66", "a67",
    "a68", "a69", "a70", "a71", "a72", "a73", "a74", "a75", "a76", "a77", "a78", "a79", "a80", "a81", "a82", "a83", "a84", "a85", "a86", "a87", "a88", "a89", "a90", "a91",
    "a92", "a93", "a94", "a95", "a96", "a97", "a98", "a99", "a100", "a101", "a102", "a103", "a104", "a105", "a106", "a107", "a108", "a109", "a110", "a111", "a112", "a113", "a114", "a115",
    "a116", "a117", "a118", "a119", "a120", "a121", "a122", "a123", "a124", "a125", "a126", "a127", "a128", "a129", "a130", "a131", "a132", "a133", "a134", "a135", "a136", "a137", "a138", "a139",
    "a140", "a141", "a142", "a143", "a144", "a145", "a146", "a147", "a148", "a149", "a150", "a151", "a152", "a153", "a154", "a155", "a156", "a157", "a158", "a159", "a160", "a161", "a162", "a163",
    "a164", "a165", "a166", "a167", "a168", "a169", "a170", "a171", "a172", "a173", "a174", "a175", "a176", "a177", "a178", "a179", "a180", "a181", "a182", "a183", "a184", "a185", "a186", "a187",
    "a188", "a189", "a190", "a191", "a192", "a193", "a194", "a195", "a196", "a197", "a198", "a199", "a200", "a201", "a202", "a203", "a204", "a205", "a206", "a207", "a208", "a209", "a210", "a211",
    "a212", "a213", "a214", "a215", "a216", "a217", "a218", "a219", "a220", "a221", "a222", "a223", "a224", "a225", "a226", "a227", "a228", "a229", "a230", "a231", "a232", "a233", "a234", "a235",
    "a236", "a237", "a238", "a239", "a240", "a241", "a242", "a243", "a244", "a245", "a246", "a247", "a248", "a249", "a250", "a251", "a252", "a253", "a254", "a255"
  );
  if (flag) {
    unsigned l2; asm volatile("v_mbcnt_lo_u32_b32 %0, -1, 0\n\tv_mbcnt_hi_u32_b32 %0, -1, %0" : "=v"(l2));
    asm volatile("s_waitcnt vmcnt(0)" ::: "memory");
    attn_slow(Ht, Wq, Kc, Vc, O, head, qblk * 4u + w, l2, (w >> 1) == rh ? lds + 32768u + (w & 1u) * 16384u : nullptr);
    asm volatile("s_waitcnt lgkmcnt(0)" ::: "memory");
  }
  asm volatile(
    "v_add_u32_e32 v136, 0x10000, v200\n\t"
    "v_add_u32_e32 v144, 0x20000, v200\n\t"
    "v_add_u32_e32 v137, 0x10000, v201\n\t"
    "v_add_u32_e32 v145, 0x20000, v201\n\t"
    "v_add_u32_e32 v138, 0x10000, v202\n\t"
    "v_add_u32_e32 v146, 0x20000, v202\n\t"
    "v_add_u32_e32 v139, 0x10000, v203\n\t"
    "v_add_u32_e32 v147, 0x20000, v203\n\t"
    "v_add_u32_e32 v140, 0x10000, v204\n\t"
    "v_add_u32_e32 v148, 0x20000, v204\n\t"
    "v_add_u32_e32 v141, 0x10000, v205\n\t"
    "v_add_u32_e32 v149, 0x20000, v205\n\t"
    "v_add_u32_e32 v142, 0x10000, v206\n\t"
    "v_add_u32_e32 v150, 0x20000, v206\n\t"
    "v_add_u32_e32 v143, 0x10000, v207\n\t"
    "v_add_u32_e32 v151, 0x20000, v207\n\t"
    "v_lshlrev_b32_e32 v152, 2, v232\n\t"
    "v_mov_b32_e32 v153, 1\n\t"
    "v_add_u32_e32 v154, s62, v232\n\t"
    "s_waitcnt lgkmcnt(0)\n\t"
    "s_waitcnt vmcnt(32)\n\t"
    "s_barrier\n\t"
    "ds_read_b128 a[0:3], v154 offset:0\n\t"
    "ds_read_b128 a[4:7], v154 offset:1024\n\t"
    "ds_read_b128 a[8:11], v154 offset:2048\n\t"
    "ds_read_b128 a[12:15], v154 offset:3072\n\t"
    "ds_read_b128 a[16:19], v154 offset:8192\n\t"
    "ds_read_b128 a[20:23], v154 offset:9216\n\t"
    "ds_read_b128 a[24:27], v154 offset:10240\n\t"
    "ds_read_b128 a[28:31], v154 offset:11264\n\t"
    "ds_read_b64_tr_b16 v[64:65], v136 offset:0\n\t"
    "ds_read_b64_tr_b16 v[66:67], v136 offset:4096\n\t"
    "ds_read_b64_tr_b16 v[68:69], v136 offset:8192\n\t"
    "ds_read_b64_tr_b16 v[70:71], v136 offset:12288\n\t"
    "ds_read_b64_tr_b16 v[72:73], v136 offset:16384\n\t"
    "ds_read_b64_tr_b16 v[74:75], v136 offset:20480\n\t"
    "ds_read_b64_tr_b16 v[76:77], v136 offset:24576\n\t"
    "ds_read_b64_tr_b16 v[78:79], v136 offset:28672\n\t"
    "ds_read_b64_tr_b16 v[80:81], v137 offset:0\n\t"
    "ds_read_b64_tr_b16 v[82:83], v137 offset:4096\n\t"
    "ds_read_b64_tr_b16 v[84:85], v137 offset:8192\n\t"
    "ds_read_b64_tr_b16 v[86:87], v137 offset:12288\n\t"
    "ds_read_b64_tr_b16 v[88:89], v137 offset:16384\n\t"
    "ds_read_b64_tr_b16 v[90:91], v137 offset:20480\n\t"
    "ds_read_b64_tr_b16 v[92:93], v137 offset:24576\n\t"
    "ds_read_b64_tr_b16 v[94:95], v137 offset:28672\n\t"
    "s_waitcnt lgkmcnt(15)\n\t"
    "s_waitcnt lgkmcnt(8)\n\t"
    "v_mfma_f32_16x16x32_bf16 v[0:3], v[64:67], a[0:3], 0\n\t"
    "ds_read_b64_tr_b16 v[96:97], v138 offset:0\n\t"
    "v_mfma_f32_16x16x32_bf16 v[0:3], v[68:71], a[4:7], v[0:3]\n\t"
    "ds_read_b64_tr_b16 v[98:99], v138 offset:4096\n\t"
    "v_mfma_f32_16x16x32_bf16 v[0:3], v[72:75], a[16:19], v[0:3]\n\t"
    "ds_read_b64_tr_b16 v[100:101], v138 offset:8192\n\t"
    "v_mfma_f32_16x16x32_bf16 v[0:3], v[76:79], a[20:23], v[0:3]\n\t"
    "ds_read_b64_tr_b16 v[102:103], v138 offset:12288\n\t"
    "v_mfma_f32_16x16x32_bf16 v[4:7], v[64:67], a[8:11], 0\n\t"
    "ds_read_b64_tr_b16 v[104:105], v138 offset:16384\n\t"
    "v_mfma_f32_16x16x32_bf16 v[4:7], v[68:71], a[12:15], v[4:7]\n\t"
    "ds_read_b64_tr_b16 v[106:107], v138 offset:20480\n\t"
    "v_mfma_f32_16x16x32_bf16 v[4:7], v[72:75], a[24:27], v[4:7]\n\t"
    "ds_read_b64_tr_b16 v[108:109], v138 offset:24576\n\t"
    "v_mfma_f32_16x16x32_bf16 v[4:7], v[76:79], a[28:31], v[4:7]\n\t"
    "ds_read_b64_tr_b16 v[110:111], v138 offset:28672\n\t"
    "s_waitcnt lgkmcnt(8)\n\t"
    "v_mfma_f32_16x16x32_bf16 v[8:11], v[80:83], a[0:3], 0\n\t"
    "ds_read_b64_tr_b16 v[112:113], v139 offset:0\n\t"
    "v_mfma_f32_16x16x32_bf16 v[8:11], v[84:87], a[4:7], v[8:11]\n\t"
    "ds_read_b64_tr_b16 v[114:115], v139 offset:4096\n\t"
    "v_mfma_f32_16x16x32_bf16 v[8:11], v[88:91], a[16:19], v[8:11]\n\t"
    "ds_read_b64_tr_b16 v[116:117], v139 offset:8192\n\t"
    "v_mfma_f32_16x16x32_bf16 v[8:11], v[92:95], a[20:23], v[8:11]\n\t"
    "ds_read_b64_tr_b16 v[118:119], v139 offset:12288\n\t"
    "v_mfma_f32_16x16x32_bf16 v[12:15], v[80:83], a[8:11], 0\n\t"
    "ds_read_b64_tr_b16 v[120:121], v139 offset:16384\n\t"
    "v_mfma_f32_16x16x32_bf16 v[12:15], v[84:87], a[12:15], v[12:15]\n\t"
    "ds_read_b64_tr_b16 v[122:123], v139 offset:20480\n\t"
    "v_mfma_f32_16x16x32_bf16 v[12:15], v[88:91], a[24:27], v[12:15]\n\t"
    "ds_read_b64_tr_b16 v[124:125], v139 offset:24576\n\t"
    "v_mfma_f32_16x16x32_bf16 v[12:15], v[92:95], a[28:31], v[12:15]\n\t"
    "ds_read_b64_tr_b16 v[126:127], v139 offset:28672\n\t"
    "s_waitcnt lgkmcnt(8)\n\t"
    "v_mfma_f32_16x16x32_bf16 v[16:19], v[96:99], a[0:3], 0\n\t"
    "ds_read_b64_tr_b16 v[64:65], v140 offset:0\n\t"
    "v_mfma_f32_16x16x32_bf16 v[16:19], v[100:103], a[4:7], v[16:19]\n\t"
    "ds_read_b64_tr_b16 v[66:67], v140 offset:4096\n\t"
    "v_mfma_f32_16x16x32_bf16 v[16:19], v[104:107], a[16:19], v[16:19]\n\t"
    "ds_read_b64_tr_b16 v[68:69], v140 offset:8192\n\t"
    "v_mfma_f32_16x16x32_bf16 v[16:19], v[108:111], a[20:23], v[16:19]\n\t"
    "ds_read_b64_tr_b16 v[70:71], v140 offset:12288\n\t"
    "v_mfma_f32_16x16x32_bf16 v[20:23], v[96:99], a[8:11], 0\n\t"
    "ds_read_b64_tr_b16 v[72:73], v140 offset:16384\n\t"
    "v_mfma_f32_16x16x32_bf16 v[20:23], v[100:103], a[12:15], v[20:23]\n\t"
    "ds_read_b64_tr_b16 v[74:75], v140 offset:20480\n\t"
    "v_mfma_f32_16x16x32_bf16 v[20:23], v[104:107], a[24:27], v[20:23]\n\t"
    "ds_read_b64_tr_b16 v[76:77], v140 offset:24576\n\t"
    "v_mfma_f32_16x16x32_bf16 v[20:23], v[108:111], a[28:31], v[20:23]\n\t"
    "ds_read_b64_tr_b16 v[78:79], v140 offset:28672\n\t"
    "s_waitcnt lgkmcnt(8)\n\t"
    "v_mfma_f32_16x16x32_bf16 v[24:27], v[112:115], a[0:3], 0\n\t"
    "ds_read_b64_tr_b16 v[80:81], v141 offset:0\n\t"
    "v_mfma_f32_16x16x32_bf16 v[24:27], v[116:119], a[4:7], v[24:27]\n\t"
    "ds_read_b64_tr_b16 v[82:83], v141 offset:4096\n\t"
    "v_mfma_f32_16x16x32_bf16 v[24:27], v[120:123], a[16:19], v[24:27]\n\t"
    "ds_read_b64_tr_b16 v[84:85], v141 offset:8192\n\t"
    "v_mfma_f32_16x16x32_bf16 v[24:27], v[124:127], a[20:23], v[24:27]\n\t"
    "ds_read_b64_tr_b16 v[86:87], v141 offset:12288\n\t"
    "v_mfma_f32_16x16x32_bf16 v[28:31], v[112:115], a[8:11], 0\n\t"
    "ds_read_b64_tr_b16 v[88:89], v141 offset:16384\n\t"
    "v_mfma_f32_16x16x32_bf16 v[28:31], v[116:119], a[12:15], v[28:31]\n\t"
    "ds_read_b64_tr_b16 v[90:91], v141 offset:20480\n\t"
    "v_mfma_f32_16x16x32_bf16 v[28:31], v[120:123], a[24:27], v[28:31]\n\t"
    "ds_read_b64_tr_b16 v[92:93], v141 offset:24576\n\t"
    "v_mfma_f32_16x16x32_bf16 v[28:31], v[124:127], a[28:31], v[28:31]\n\t"
    "ds_read_b64_tr_b16 v[94:95], v141 offset:28672\n\t"
    "s_waitcnt lgkmcnt(8)\n\t"
    "v_mfma_f32_16x16x32_bf16 v[32:35], v[64:67], a[0:3], 0\n\t"
    "ds_read_b64_tr_b16 v[96:97], v142 offset:0\n\t"
    "v_mfma_f32_16x16x32_bf16 v[32:35], v[68:71], a[4:7], v[32:35]\n\t"
    "ds_read_b64_tr_b16 v[98:99], v142 offset:4096\n\t"
    "v_mfma_f32_16x16x32_bf16 v[32:35], v[72:75], a[16:19], v[32:35]\n\t"
    "ds_read_b64_tr_b16 v[100:101], v142 offset:8192\n\t"
    "v_mfma_f32_16x16x32_bf16 v[32:35], v[76:79], a[20:23], v[32:35]\n\t"
    "ds_read_b64_tr_b16 v[102:103], v142 offset:12288\n\t"
    "v_mfma_f32_16x16x32_bf16 v[36:39], v[64:67], a[8:11], 0\n\t"
    "ds_read_b64_tr_b16 v[104:105], v142 offset:16384\n\t"
    "v_mfma_f32_16x16x32_bf16 v[36:39], v[68:71], a[12:15], v[36:39]\n\t"
    "ds_read_b64_tr_b16 v[106:107], v142 offset:20480\n\t"
    "v_mfma_f32_16x16x32_bf16 v[36:39], v[72:75], a[24:27], v[36:39]\n\t"
    "ds_read_b64_tr_b16 v[108:109], v142 offset:24576\n\t"
    "v_mfma_f32_16x16x32_bf16 v[36:39], v[76:79], a[28:31], v[36:39]\n\t"
    "ds_read_b64_tr_b16 v[110:111], v142 offset:28672\n\t"
    "s_waitcnt lgkmcnt(8)\n\t"
    "v_mfma_f32_16x16x32_bf16 v[40:43], v[80:83], a[0:3], 0\n\t"
    "ds_read_b64_tr_b16 v[112:113], v143 offset:0\n\t"
    "v_mfma_f32_16x16x32_bf16 v[40:43], v[84:87], a[4:7], v[40:43]\n\t"
    "ds_read_b64_tr_b16 v[114:115], v143 offset:4096\n\t"
    "v_mfma_f32_16x16x32_bf16 v[40:43], v[88:91], a[16:19], v[40:43]\n\t"
    "ds_read_b64_tr_b16 v[116:117], v143 offset:8192\n\t"
    "v_mfma_f32_16x16x32_bf16 v[40:43], v[92:95], a[20:23], v[40:43]\n\t"
    "ds_read_b64_tr_b16 v[118:119], v143 offset:12288\n\t"
    "v_mfma_f32_16x16x32_bf16 v[44:47], v[80:83], a[8:11], 0\n\t"
    "ds_read_b64_tr_b16 v[120:121], v143 offset:16384\n\t"
    "v_mfma_f32_16x16x32_bf16 v[44:47], v[84:87], a[12:15], v[44:47]\n\t"
    "ds_read_b64_tr_b16 v[122:123], v143 offset:20480\n\t"
    "v_mfma_f32_16x16x32_bf16 v[44:47], v[88:91], a[24:27], v[44:47]\n\t"
    "ds_read_b64_tr_b16 v[124:125], v143 offset:24576\n\t"
    "v_mfma_f32_16x16x32_bf16 v[44:47], v[92:95], a[28:31], v[44:47]\n\t"
    "ds_read_b64_tr_b16 v[126:127], v143 offset:28672\n\t"
    "s_waitcnt lgkmcnt(8)\n\t"
    "v_mfma_f32_16x16x32_bf16 v[48:51], v[96:99], a[0:3], 0\n\t"
    "ds_read_b64_tr_b16 v[64:65], v136 offset:32768\n\t"
    "v_mfma_f32_16x16x32_bf16 v[48:51], v[100:103], a[4:7], v[48:51]\n\t"
    "ds_read_b64_tr_b16 v[66:67], v136 offset:36864\n\t"
    "v_mfma_f32_16x16x32_bf16 v[48:51], v[104:107], a[16:19], v[48:51]\n\t"
    "ds_read_b64_tr_b16 v[68:69], v136 offset:40960\n\t"
    "v_mfma_f32_16x16x32_bf16 v[48:51], v[108:111], a[20:23], v[48:51]\n\t"
    "ds_read_b64_tr_b16 v[70:71], v136 offset:45056\n\t"
    "v_mfma_f32_16x16x32_bf16 v[52:55], v[96:99], a[8:11], 0\n\t"
    "ds_read_b64_tr_b16 v[72:73], v136 offset:49152\n\t"
    "v_mfma_f32_16x16x32_bf16 v[52:55], v[100:103], a[12:15], v[52:55]\n\t"
    "ds_read_b64_tr_b16 v[74:75], v136 offset:53248\n\t"
    "v_mfma_f32_16x16x32_bf16 v[52:55], v[104:107], a[24:27], v[52:55]\n\t"
    "ds_read_b64_tr_b16 v[76:77], v136 offset:57344\n\t"
    "v_mfma_f32_16x16x32_bf16 v[52:55], v[108:111], a[28:31], v[52:55]\n\t"
    "ds_read_b64_tr_b16 v[78:79], v136 offset:61440\n\t"
    "s_waitcnt lgkmcnt(8)\n\t"
    "v_mfma_f32_16x16x32_bf16 v[56:59], v[112:115], a[0:3], 0\n\t"
    "ds_read_b64_tr_b16 v[80:81], v137 offset:32768\n\t"
    "v_mfma_f32_16x16x32_bf16 v[56:59], v[116:119], a[4:7], v[56:59]\n\t"
    "ds_read_b64_tr_b16 v[82:83], v137 offset:36864\n\t"
    "v_mfma_f32_16x16x32_bf16 v[56:59], v[120:123], a[16:19], v[56:59]\n\t"
    "ds_read_b64_tr_b16 v[84:85], v137 offset:40960\n\t"
    "v_mfma_f32_16x16x32_bf16 v[56:59], v[124:127], a[20:23], v[56:59]\n\t"
    "ds_read_b64_tr_b16 v[86:87], v137 offset:45056\n\t"
    "v_mfma_f32_16x16x32_bf16 v[60:63], v[112:115], a[8:11], 0\n\t"
    "ds_read_b64_tr_b16 v[88:89], v137 offset:49152\n\t"
    "v_mfma_f32_16x16x32_bf16 v[60:63], v[116:119], a[12:15], v[60:63]\n\t"
    "ds_read_b64_tr_b16 v[90:91], v137 offset:53248\n\t"
    "v_mfma_f32_16x16x32_bf16 v[60:63], v[120:123], a[24:27], v[60:63]\n\t"
    "ds_read_b64_tr_b16 v[92:93], v137 offset:57344\n\t"
    "v_mfma_f32_16x16x32_bf16 v[60:63], v[124:127], a[28:31], v[60:63]\n\t"
    "ds_read_b64_tr_b16 v[94:95], v137 offset:61440\n\t"
    "s_waitcnt lgkmcnt(0)\n\t"
    "s_waitcnt vmcnt(0)\n\t"
    "s_barrier\n\t"
    "s_cmp_lg_u32 s29, 0\n\t"
    "s_cbranch_scc1 Lnoflag_%=\n\t"
    "s_mov_b64 exec, 1\n\t"
    "buffer_store_dword v153, off, s[72:75], s77 sc1\n\t"
    "s_mov_b64 exec, -1\n\t"
    "Lnoflag_%=:\n\t"
    "s_add_u32 m0, s78, 65536\n\t"
    "s_nop 0\n\t"
    "buffer_load_dwordx4 v234, s[64:67], s76 offen lds\n\t"
    "buffer_load_dwordx4 v235, s[64:67], s76 offen offset:1024 lds\n\t"
    "buffer_load_dwordx4 v236, s[64:67], s76 offen offset:2048 lds\n\t"
    "buffer_load_dwordx4 v237, s[64:67], s76 offen offset:3072 lds\n\t"
    "s_add_u32 s76, s76, 65536\n\t"
    "s_and_b32 s76, s76, 0xfffff\n\t"
    "s_add_u32 m0, s78, 81920\n\t"
    "s_nop 0\n\t"
    "buffer_load_dwordx4 v234, s[64:67], s76 offen lds\n\t"
    "buffer_load_dwordx4 v235, s[64:67], s76 offen offset:1024 lds\n\t"
    "buffer_load_dwordx4 v236, s[64:67], s76 offen offset:2048 lds\n\t"
    "buffer_load_dwordx4 v237, s[64:67], s76 offen offset:3072 lds\n\t"
    "s_add_u32 s76, s76, 65536\n\t"
    "s_and_b32 s76, s76, 0xfffff\n\t"
    "s_add_u32 m0, s78, 32768\n\t"
    "s_nop 0\n\t"
    "buffer_load_dwordx4 v234, s[64:67], s76 offen lds\n\t"
    "buffer_load_dwordx4 v235, s[64:67], s76 offen offset:1024 lds\n\t"
    "buffer_load_dwordx4 v236, s[64:67], s76 offen offset:2048 lds\n\t"
    "buffer_load_dwordx4 v237, s[64:67], s76 offen offset:3072 lds\n\t"
    "s_add_u32 s76, s76, 65536\n\t"
    "s_and_b32 s76, s76, 0xfffff\n\t"
    "s_add_u32 m0, s78, 49152\n\t"
    "s_nop 0\n\t"
    "buffer_load_dwordx4 v234, s[64:67], s76 offen lds\n\t"
    "buffer_load_dwordx4 v235, s[64:67], s76 offen offset:1024 lds\n\t"
    "buffer_load_dwordx4 v236, s[64:67], s76 offen offset:2048 lds\n\t"
    "buffer_load_dwordx4 v237, s[64:67], s76 offen offset:3072 lds\n\t"
    "s_add_u32 s76, s76, 65536\n\t"
    "s_and_b32 s76, s76, 0xfffff\n\t"
    "s_mov_b32 s52, 262144\n\t"
    "s_mov_b64 exec, 0xff\n\t"
    "Lpoll_%=:\n\t"
    "buffer_load_dword v153, v152, s[72:75], 0 offen sc1\n\t"
    "s_waitcnt vmcnt(0)\n\t"
    "v_cmp_eq_u32_e32 vcc, 0, v153\n\t"
    "s_nop 4\n\t"
    "s_cmp_eq_u64 vcc, 0\n\t"
    "s_cbranch_scc1 Lgo_%=\n\t"
    "s_sub_u32 s52, s52, 1\n\t"
    "s_cbranch_scc1 Lgo_%=\n\t"
    "s_sleep 1\n\t"
    "s_branch Lpoll_%=\n\t"
    "Lgo_%=:\n\t"
    "s_mov_b64 exec, -1\n\t"
    "s_nop 4\n\t"
    "buffer_load_dwordx4 a[32:35], v232, s[56:59], s51 offen sc1\n\t"
    "buffer_load_dwordx4 a[36:39], v232, s[56:59], s51 offen offset:1024 sc1\n\t"
    "buffer_load_dwordx4 a[40:43], v232, s[56:59], s51 offen offset:2048 sc1\n\t"
    "buffer_load_dwordx4 a[44:47], v232, s[56:59], s51 offen offset:3072 sc1\n\t"
    "s_add_u32 s51, s51, 8192\n\t"
    "buffer_load_dwordx4 a[48:51], v232, s[56:59], s51 offen sc1\n\t"
    "buffer_load_dwordx4 a[52:55], v232, s[56:59], s51 offen offset:1024 sc1\n\t"
    "buffer_load_dwordx4 a[56:59], v232, s[56:59], s51 offen offset:2048 sc1\n\t"
    "buffer_load_dwordx4 a[60:63], v232, s[56:59], s51 offen offset:3072 sc1\n\t"
    "s_add_u32 s51, s51, 516096\n\t"
    "s_and_b32 s51, s51, 0x3fffff\n\t"
    "buffer_load_dwordx4 a[64:67], v232, s[56:59], s51 offen sc1\n\t"
    "buffer_load_dwordx4 a[68:71], v232, s[56:59], s51 offen offset:1024 sc1\n\t"
    "buffer_load_dwordx4 a[72:75], v232, s[56:59], s51 offen offset:2048 sc1\n\t"
    "buffer_load_dwordx4 a[76:79], v232, s[56:59], s51 offen offset:3072 sc1\n\t"
    "s_add_u32 s51, s51, 8192\n\t"
    "buffer_load_dwordx4 a[80:83], v232, s[56:59], s51 offen sc1\n\t"
    "buffer_load_dwordx4 a[84:87], v232, s[56:59], s51 offen offset:1024 sc1\n\t"
    "buffer_load_dwordx4 a[88:91], v232, s[56:59], s51 offen offset:2048 sc1\n\t"
    "buffer_load_dwordx4 a[92:95], v232, s[56:59], s51 offen offset:3072 sc1\n\t"
    "s_add_u32 s51, s51, 516096\n\t"
    "s_and_b32 s51, s51, 0x3fffff\n\t"
    "buffer_load_dwordx4 a[96:99], v232, s[56:59], s51 offen sc1\n\t"
    "buffer_load_dwordx4 a[100:103], v232, s[56:59], s51 offen offset:1024 sc1\n\t"
    "buffer_load_dwordx4 a[104:107], v232, s[56:59], s51 offen offset:2048 sc1\n\t"
    "buffer_load_dwordx4 a[108:111], v232, s[56:59], s51 offen offset:3072 sc1\n\t"
    "s_add_u32 s51, s51, 8192\n\t"
    "buffer_load_dwordx4 a[112:115], v232, s[56:59], s51 offen sc1\n\t"
    "buffer_load_dwordx4 a[116:119], v232, s[56:59], s51 offen offset:1024 sc1\n\t"
    "buffer_load_dwordx4 a[120:123], v232, s[56:59], s51 offen offset:2048 sc1\n\t"
    "buffer_load_dwordx4 a[124:127], v232, s[56:59], s51 offen offset:3072 sc1\n\t"
    "s_add_u32 s51, s51, 516096\n\t"
    "s_and_b32 s51, s51, 0x3fffff\n\t"
    "s_waitcnt vmcnt(16)\n\t"
    "s_waitcnt lgkmcnt(0)\n\t"
    "v_mfma_f32_16x16x32_bf16 v[0:3], v[64:67], a[32:35], v[0:3]\n\t"
    "ds_read_b64_tr_b16 v[96:97], v138 offset:32768\n\t"
    "v_mfma_f32_16x16x32_bf16 v[0:3], v[68:71], a[36:39], v[0:3]\n\t"
    "ds_read_b64_tr_b16 v[98:99], v138 offset:36864\n\t"
    "buffer_load_dwordx4 a[128:131], v232, s[56:59], s51 offen sc1\n\t"
    "v_mfma_f32_16x16x32_bf16 v[0:3], v[72:75], a[48:51], v[0:3]\n\t"
    "ds_read_b64_tr_b16 v[100:101], v138 offset:40960\n\t"
    "v_mfma_f32_16x16x32_bf16 v[0:3], v[76:79], a[52:55], v[0:3]\n\t"
    "ds_read_b64_tr_b16 v[102:103], v138 offset:45056\n\t"
    "v_mfma_f32_16x16x32_bf16 v[4:7], v[64:67], a[40:43], v[4:7]\n\t"
    "ds_read_b64_tr_b16 v[104:105], v138 offset:49152\n\t"
    "buffer_load_dwordx4 a[132:135], v232, s[56:59], s51 offen offset:1024 sc1\n\t"
    "v_mfma_f32_16x16x32_bf16 v[4:7], v[68:71], a[44:47], v[4:7]\n\t"
    "ds_read_b64_tr_b16 v[106:107], v138 offset:53248\n\t"
    "v_mfma_f32_16x16x32_bf16 v[4:7], v[72:75], a[56:59], v[4:7]\n\t"
    "ds_read_b64_tr_b16 v[108:109], v138 offset:57344\n\t"
    "buffer_load_dwordx4 a[136:139], v232, s[56:59], s51 offen offset:2048 sc1\n\t"
    "v_mfma_f32_16x16x32_bf16 v[4:7], v[76:79], a[60:63], v[4:7]\n\t"
    "ds_read_b64_tr_b16 v[110:111], v138 offset:61440\n\t"
    "s_waitcnt lgkmcnt(8)\n\t"
    "v_mfma_f32_16x16x32_bf16 v[8:11], v[80:83], a[32:35], v[8:11]\n\t"
    "ds_read_b64_tr_b16 v[112:113], v139 offset:32768\n\t"
    "v_mfma_f32_16x16x32_bf16 v[8:11], v[84:87], a[36:39], v[8:11]\n\t"
    "ds_read_b64_tr_b16 v[114:115], v139 offset:36864\n\t"
    "buffer_load_dwordx4 a[140:143], v232, s[56:59], s51 offen offset:3072 sc1\n\t"
    "s_add_u32 s51, s51, 8192\n\t"
    "v_mfma_f32_16x16x32_bf16 v[8:11], v[88:91], a[48:51], v[8:11]\n\t"
    "ds_read_b64_tr_b16 v[116:117], v139 offset:40960\n\t"
    "v_mfma_f32_16x16x32_bf16 v[8:11], v[92:95], a[52:55], v[8:11]\n\t"
    "ds_read_b64_tr_b16 v[118:119], v139 offset:45056\n\t"
    "v_mfma_f32_16x16x32_bf16 v[12:15], v[80:83], a[40:43], v[12:15]\n\t"
    "ds_read_b64_tr_b16 v[120:121], v139 offset:49152\n\t"
    "buffer_load_dwordx4 a[144:147], v232, s[56:59], s51 offen sc1\n\t"
    "v_mfma_f32_16x16x32_bf16 v[12:15], v[84:87], a[44:47], v[12:15]\n\t"
    "ds_read_b64_tr_b16 v[122:123], v139 offset:53248\n\t"
    "v_mfma_f32_16x16x32_bf16 v[12:15], v[88:91], a[56:59], v[12:15]\n\t"
    "ds_read_b64_tr_b16 v[124:125], v139 offset:57344\n\t"
    "buffer_load_dwordx4 a[148:151], v232, s[56:59], s51 offen offset:1024 sc1\n\t"
    "v_mfma_f32_16x16x32_bf16 v[12:15], v[92:95], a[60:63], v[12:15]\n\t"
    "ds_read_b64_tr_b16 v[126:127], v139 offset:61440\n\t"
    "s_waitcnt lgkmcnt(8)\n\t"
    "v_mfma_f32_16x16x32_bf16 v[16:19], v[96:99], a[32:35], v[16:19]\n\t"
    "ds_read_b64_tr_b16 v[64:65], v140 offset:32768\n\t"
    "v_mfma_f32_16x16x32_bf16 v[16:19], v[100:103], a[36:39], v[16:19]\n\t"
    "ds_read_b64_tr_b16 v[66:67], v140 offset:36864\n\t"
    "buffer_load_dwordx4 a[152:155], v232, s[56:59], s51 offen offset:2048 sc1\n\t"
    "v_mfma_f32_16x16x32_bf16 v[16:19], v[104:107], a[48:51], v[16:19]\n\t"
    "ds_read_b64_tr_b16 v[68:69], v140 offset:40960\n\t"
    "v_mfma_f32_16x16x32_bf16 v[16:19], v[108:111], a[52:55], v[16:19]\n\t"
    "ds_read_b64_tr_b16 v[70:71], v140 offset:45056\n\t"
    "v_mfma_f32_16x16x32_bf16 v[20:23], v[96:99], a[40:43], v[20:23]\n\t"
    "ds_read_b64_tr_b16 v[72:73], v140 offset:49152\n\t"
    "buffer_load_dwordx4 a[156:159], v232, s[56:59], s51 offen offset:3072 sc1\n\t"
    "s_add_u32 s51, s51, 516096\n\t"
    "s_and_b32 s51, s51, 0x3fffff\n\t"
    "v_mfma_f32_16x16x32_bf16 v[20:23], v[100:103], a[44:47], v[20:23]\n\t"
    "ds_read_b64_tr_b16 v[74:75], v140 offset:53248\n\t"
    "v_mfma_f32_16x16x32_bf16 v[20:23], v[104:107], a[56:59], v[20:23]\n\t"
    "ds_read_b64_tr_b16 v[76:77], v140 offset:57344\n\t"
    "buffer_load_dwordx4 a[160:163], v232, s[56:59], s51 offen sc1\n\t"
    "v_mfma_f32_16x16x32_bf16 v[20:23], v[108:111], a[60:63], v[20:23]\n\t"
    "ds_read_b64_tr_b16 v[78:79], v140 offset:61440\n\t"
    "s_waitcnt lgkmcnt(8)\n\t"
    "v_mfma_f32_16x16x32_bf16 v[24:27], v[112:115], a[32:35], v[24:27]\n\t"
    "ds_read_b64_tr_b16 v[80:81], v141 offset:32768\n\t"
    "v_mfma_f32_16x16x32_bf16 v[24:27], v[116:119], a[36:39], v[24:27]\n\t"
    "ds_read_b64_tr_b16 v[82:83], v141 offset:36864\n\t"
    "buffer_load_dwordx4 a[164:167], v232, s[56:59], s51 offen offset:1024 sc1\n\t"
    "v_mfma_f32_16x16x32_bf16 v[24:27], v[120:123], a[48:51], v[24:27]\n\t"
    "ds_read_b64_tr_b16 v[84:85], v141 offset:40960\n\t"
    "v_mfma_f32_16x16x32_bf16 v[24:27], v[124:127], a[52:55], v[24:27]\n\t"
    "ds_read_b64_tr_b16 v[86:87], v141 offset:45056\n\t"
    "v_mfma_f32_16x16x32_bf16 v[28:31], v[112:115], a[40:43], v[28:31]\n\t"
    "ds_read_b64_tr_b16 v[88:89], v141 offset:49152\n\t"
    "buffer_load_dwordx4 a[168:171], v232, s[56:59], s51 offen offset:2048 sc1\n\t"
    "v_mfma_f32_16x16x32_bf16 v[28:31], v[116:119], a[44:47], v[28:31]\n\t"
    "ds_read_b64_tr_b16 v[90:91], v141 offset:53248\n\t"
    "v_mfma_f32_16x16x32_bf16 v[28:31], v[120:123], a[56:59], v[28:31]\n\t"
    "ds_read_b64_tr_b16 v[92:93], v141 offset:57344\n\t"
    "buffer_load_dwordx4 a[172:175], v232, s[56:59], s51 offen offset:3072 sc1\n\t"
    "s_add_u32 s51, s51, 8192\n\t"
    "v_mfma_f32_16x16x32_bf16 v[28:31], v[124:127], a[60:63], v[28:31]\n\t"
    "ds_read_b64_tr_b16 v[94:95], v141 offset:61440\n\t"
    "s_waitcnt lgkmcnt(8)\n\t"
    "v_mfma_f32_16x16x32_bf16 v[32:35], v[64:67], a[32:35], v[32:35]\n\t"
    "ds_read_b64_tr_b16 v[96:97], v142 offset:32768\n\t"
    "v_mfma_f32_16x16x32_bf16 v[32:35], v[68:71], a[36:39], v[32:35]\n\t"
    "ds_read_b64_tr_b16 v[98:99], v142 offset:36864\n\t"
    "buffer_load_dwordx4 a[176:179], v232, s[56:59], s51 offen sc1\n\t"
    "v_mfma_f32_16x16x32_bf16 v[32:35], v[72:75], a[48:51], v[32:35]\n\t"
    "ds_read_b64_tr_b16 v[100:101], v142 offset:40960\n\t"
    "v_mfma_f32_16x16x32_bf16 v[32:35], v[76:79], a[52:55], v[32:35]\n\t"
    "ds_read_b64_tr_b16 v[102:103], v142 offset:45056\n\t"
    "v_mfma_f32_16x16x32_bf16 v[36:39], v[64:67], a[40:43], v[36:39]\n\t"
    "ds_read_b64_tr_b16 v[104:105], v142 offset:49152\n\t"
    "buffer_load_dwordx4 a[180:183], v232, s[56:59], s51 offen offset:1024 sc1\n\t"
    "v_mfma_f32_16x16x32_bf16 v[36:39], v[68:71], a[44:47], v[36:39]\n\t"
    "ds_read_b64_tr_b16 v[106:107], v142 offset:53248\n\t"
    "v_mfma_f32_16x16x32_bf16 v[36:39], v[72:75], a[56:59], v[36:39]\n\t"
    "ds_read_b64_tr_b16 v[108:109], v142 offset:57344\n\t"
    "buffer_load_dwordx4 a[184:187], v232, s[56:59], s51 offen offset:2048 sc1\n\t"
    "v_mfma_f32_16x16x32_bf16 v[36:39], v[76:79], a[60:63], v[36:39]\n\t"
    "ds_read_b64_tr_b16 v[110:111], v142 offset:61440\n\t"
    "s_waitcnt lgkmcnt(8)\n\t"
    "v_mfma_f32_16x16x32_bf16 v[40:43], v[80:83], a[32:35], v[40:43]\n\t"
    "ds_read_b64_tr_b16 v[112:113], v143 offset:32768\n\t"
    "v_mfma_f32_16x16x32_bf16 v[40:43], v[84:87], a[36:39], v[40:43]\n\t"
    "ds_read_b64_tr_b16 v[114:115], v143 offset:36864\n\t"
    "buffer_load_dwordx4 a[188:191], v232, s[56:59], s51 offen offset:3072 sc1\n\t"
    "s_add_u32 s51, s51, 516096\n\t"
    "s_and_b32 s51, s51, 0x3fffff\n\t"
    "v_mfma_f32_16x16x32_bf16 v[40:43], v[88:91], a[48:51], v[40:43]\n\t"
    "ds_read_b64_tr_b16 v[116:117], v143 offset:40960\n\t"
    "v_mfma_f32_16x16x32_bf16 v[40:43], v[92:95], a[52:55], v[40:43]\n\t"
    "ds_read_b64_tr_b16 v[118:119], v143 offset:45056\n\t"
    "v_mfma_f32_16x16x32_bf16 v[44:47], v[80:83], a[40:43], v[44:47]\n\t"
    "ds_read_b64_tr_b16 v[120:121], v143 offset:49152\n\t"
    "v_mfma_f32_16x16x32_bf16 v[44:47], v[84:87], a[44:47], v[44:47]\n\t"
    "ds_read_b64_tr_b16 v[122:123], v143 offset:53248\n\t"
    "v_mfma_f32_16x16x32_bf16 v[44:47], v[88:91], a[56:59], v[44:47]\n\t"
    "ds_read_b64_tr_b16 v[124:125], v143 offset:57344\n\t"
    "v_mfma_f32_16x16x32_bf16 v[44:47], v[92:95], a[60:63], v[44:47]\n\t"
    "ds_read_b64_tr_b16 v[126:127], v143 offset:61440\n\t"
    "s_waitcnt lgkmcnt(0)\n\t"
    "s_waitcnt vmcnt(24)\n\t"
    "s_barrier\n\t"
    "v_mfma_f32_16x16x32_bf16 v[48:51], v[96:99], a[32:35], v[48:51]\n\t"
    "ds_read_b64_tr_b16 v[64:65], v200 offset:0\n\t"
    "v_mfma_f32_16x16x32_bf16 v[48:51], v[100:103], a[36:39], v[48:51]\n\t"
    "ds_read_b64_tr_b16 v[66:67], v200 offset:4096\n\t"
    "s_add_u32 m0, s78, 98304\n\t"
    "s_nop 0\n\t"
    "buffer_load_dwordx4 v234, s[64:67], s76 offen lds\n\t"
    "v_mfma_f32_16x16x32_bf16 v[48:51], v[104:107], a[48:51], v[48:51]\n\t"
    "ds_read_b64_tr_b16 v[68:69], v200 offset:8192\n\t"
    "v_mfma_f32_16x16x32_bf16 v[48:51], v[108:111], a[52:55], v[48:51]\n\t"
    "ds_read_b64_tr_b16 v[70:71], v200 offset:12288\n\t"
    "v_mfma_f32_16x16x32_bf16 v[52:55], v[96:99], a[40:43], v[52:55]\n\t"
    "ds_read_b64_tr_b16 v[72:73], v200 offset:16384\n\t"
    "buffer_load_dwordx4 v235, s[64:67], s76 offen offset:1024 lds\n\t"
    "v_mfma_f32_16x16x32_bf16 v[52:55], v[100:103], a[44:47], v[52:55]\n\t"
    "ds_read_b64_tr_b16 v[74:75], v200 offset:20480\n\t"
    "v_mfma_f32_16x16x32_bf16 v[52:55], v[104:107], a[56:59], v[52:55]\n\t"
    "ds_read_b64_tr_b16 v[76:77], v200 offset:24576\n\t"
    "buffer_load_dwordx4 v236, s[64:67], s76 offen offset:2048 lds\n\t"
    "v_mfma_f32_16x16x32_bf16 v[52:55], v[108:111], a[60:63], v[52:55]\n\t"
    "ds_read_b64_tr_b16 v[78:79], v200 offset:28672\n\t"
    "s_waitcnt lgkmcnt(8)\n\t"
    "v_mfma_f32_16x16x32_bf16 v[56:59], v[112:115], a[32:35], v[56:59]\n\t"
    "ds_read_b64_tr_b16 v[80:81], v201 offset:0\n\t"
    "v_mfma_f32_16x16x32_bf16 v[56:59], v[116:119], a[36:39], v[56:59]\n\t"
    "ds_read_b64_tr_b16 v[82:83], v201 offset:4096\n\t"
    "buffer_load_dwordx4 v237, s[64:67], s76 offen offset:3072 lds\n\t"
    "s_add_u32 s76, s76, 65536\n\t"
    "s_and_b32 s76, s76, 0xfffff\n\t"
    "v_mfma_f32_16x16x32_bf16 v[56:59], v[120:123], a[48:51], v[56:59]\n\t"
    "ds_read_b64_tr_b16 v[84:85], v201 offset:8192\n\t"
    "v_mfma_f32_16x16x32_bf16 v[56:59], v[124:127], a[52:55], v[56:59]\n\t"
    "ds_read_b64_tr_b16 v[86:87], v201 offset:12288\n\t"
    "v_mfma_f32_16x16x32_bf16 v[60:63], v[112:115], a[40:43], v[60:63]\n\t"
    "ds_read_b64_tr_b16 v[88:89], v201 offset:16384\n\t"
    "s_add_u32 m0, s78, 114688\n\t"
    "s_nop 0\n\t"
    "buffer_load_dwordx4 v234, s[64:67], s76 offen lds\n\t"
    "v_mfma_f32_16x16x32_bf16 v[60:63], v[116:119], a[44:47], v[60:63]\n\t"
    "ds_read_b64_tr_b16 v[90:91], v201 offset:20480\n\t"
    "v_mfma_f32_16x16x32_bf16 v[60:63], v[120:123], a[56:59], v[60:63]\n\t"
    "ds_read_b64_tr_b16 v[92:93], v201 offset:24576\n\t"
    "buffer_load_dwordx4 v235, s[64:67], s76 offen offset:1024 lds\n\t"
    "v_mfma_f32_16x16x32_bf16 v[60:63], v[124:127], a[60:63], v[60:63]\n\t"
    "ds_read_b64_tr_b16 v[94:95], v201 offset:28672\n\t"
    "s_waitcnt lgkmcnt(8)\n\t"
    "v_mfma_f32_16x16x32_bf16 v[0:3], v[64:67], a[64:67], v[0:3]\n\t"
    "ds_read_b64_tr_b16 v[96:97], v202 offset:0\n\t"
    "v_mfma_f32_16x16x32_bf16 v[0:3], v[68:71], a[68:71], v[0:3]\n\t"
    "ds_read_b64_tr_b16 v[98:99], v202 offset:4096\n\t"
    "buffer_load_dwordx4 v236, s[64:67], s76 offen offset:2048 lds\n\t"
    "v_mfma_f32_16x16x32_bf16 v[0:3], v[72:75], a[80:83], v[0:3]\n\t"
    "ds_read_b64_tr_b16 v[100:101], v202 offset:8192\n\t"
    "v_mfma_f32_16x16x32_bf16 v[0:3], v[76:79], a[84:87], v[0:3]\n\t"
    "ds_read_b64_tr_b16 v[102:103], v202 offset:12288\n\t"
    "v_mfma_f32_16x16x32_bf16 v[4:7], v[64:67], a[72:75], v[4:7]\n\t"
    "ds_read_b64_tr_b16 v[104:105], v202 offset:16384\n\t"
    "buffer_load_dwordx4 v237, s[64:67], s76 offen offset:3072 lds\n\t"
    "s_add_u32 s76, s76, 65536\n\t"
    "s_and_b32 s76, s76, 0xfffff\n\t"
    "v_mfma_f32_16x16x32_bf16 v[4:7], v[68:71], a[76:79], v[4:7]\n\t"
    "ds_read_b64_tr_b16 v[106:107], v202 offset:20480\n\t"
    "v_mfma_f32_16x16x32_bf16 v[4:7], v[72:75], a[88:91], v[4:7]\n\t"
    "ds_read_b64_tr_b16 v[108:109], v202 offset:24576\n\t"
    "buffer_load_dwordx4 a[192:195], v232, s[56:59], s51 offen sc1\n\t"
    "v_mfma_f32_16x16x32_bf16 v[4:7], v[76:79], a[92:95], v[4:7]\n\t"
    "ds_read_b64_tr_b16 v[110:111], v202 offset:28672\n\t"
    "s_waitcnt lgkmcnt(8)\n\t"
    "v_mfma_f32_16x16x32_bf16 v[8:11], v[80:83], a[64:67], v[8:11]\n\t"
    "ds_read_b64_tr_b16 v[112:113], v203 offset:0\n\t"
    "v_mfma_f32_16x16x32_bf16 v[8:11], v[84:87], a[68:71], v[8:11]\n\t"
    "ds_read_b64_tr_b16 v[114:115], v203 offset:4096\n\t"
    "buffer_load_dwordx4 a[196:199], v232, s[56:59], s51 offen offset:1024 sc1\n\t"
    "v_mfma_f32_16x16x32_bf16 v[8:11], v[88:91], a[80:83], v[8:11]\n\t"
    "ds_read_b64_tr_b16 v[116:117], v203 offset:8192\n\t"
    "v_mfma_f32_16x16x32_bf16 v[8:11], v[92:95], a[84:87], v[8:11]\n\t"
    "ds_read_b64_tr_b16 v[118:119], v203 offset:12288\n\t"
    "v_mfma_f32_16x16x32_bf16 v[12:15], v[80:83], a[72:75], v[12:15]\n\t"
    "ds_read_b64_tr_b16 v[120:121], v203 offset:16384\n\t"
    "buffer_load_dwordx4 a[200:203], v232, s[56:59], s51 offen offset:2048 sc1\n\t"
    "v_mfma_f32_16x16x32_bf16 v[12:15], v[84:87], a[76:79], v[12:15]\n\t"
    "ds_read_b64_tr_b16 v[122:123], v203 offset:20480\n\t"
    "v_mfma_f32_16x16x32_bf16 v[12:15], v[88:91], a[88:91], v[12:15]\n\t"
    "ds_read_b64_tr_b16 v[124:125], v203 offset:24576\n\t"
    "buffer_load_dwordx4 a[204:207], v232, s[56:59], s51 offen offset:3072 sc1\n\t"
    "s_add_u32 s51, s51, 8192\n\t"
    "v_mfma_f32_16x16x32_bf16 v[12:15], v[92:95], a[92:95], v[12:15]\n\t"
    "ds_read_b64_tr_b16 v[126:127], v203 offset:28672\n\t"
    "s_waitcnt lgkmcnt(8)\n\t"
    "v_mfma_f32_16x16x32_bf16 v[16:19], v[96:99], a[64:67], v[16:19]\n\t"
    "ds_read_b64_tr_b16 v[64:65], v204 offset:0\n\t"
    "v_mfma_f32_16x16x32_bf16 v[16:19], v[100:103], a[68:71], v[16:19]\n\t"
    "ds_read_b64_tr_b16 v[66:67], v204 offset:4096\n\t"
    "buffer_load_dwordx4 a[208:211], v232, s[56:59], s51 offen sc1\n\t"
    "v_mfma_f32_16x16x32_bf16 v[16:19], v[104:107], a[80:83], v[16:19]\n\t"
    "ds_read_b64_tr_b16 v[68:69], v204 offset:8192\n\t"
    "v_mfma_f32_16x16x32_bf16 v[16:19], v[108:111], a[84:87], v[16:19]\n\t"
    "ds_read_b64_tr_b16 v[70:71], v204 offset:12288\n\t"
    "v_mfma_f32_16x16x32_bf16 v[20:23], v[96:99], a[72:75], v[20:23]\n\t"
    "ds_read_b64_tr_b16 v[72:73], v204 offset:16384\n\t"
    "buffer_load_dwordx4 a[212:215], v232, s[56:59], s51 offen offset:1024 sc1\n\t"
    "v_mfma_f32_16x16x32_bf16 v[20:23], v[100:103], a[76:79], v[20:23]\n\t"
    "ds_read_b64_tr_b16 v[74:75], v204 offset:20480\n\t"
    "v_mfma_f32_16x16x32_bf16 v[20:23], v[104:107], a[88:91], v[20:23]\n\t"
    "ds_read_b64_tr_b16 v[76:77], v204 offset:24576\n\t"
    "buffer_load_dwordx4 a[216:219], v232, s[56:59], s51 offen offset:2048 sc1\n\t"
    "v_mfma_f32_16x16x32_bf16 v[20:23], v[108:111], a[92:95], v[20:23]\n\t"
    "ds_read_b64_tr_b16 v[78:79], v204 offset:28672\n\t"
    "s_waitcnt lgkmcnt(8)\n\t"
    "v_mfma_f32_16x16x32_bf16 v[24:27], v[112:115], a[64:67], v[24:27]\n\t"
    "ds_read_b64_tr_b16 v[80:81], v205 offset:0\n\t"
    "v_mfma_f32_16x16x32_bf16 v[24:27], v[116:119], a[68:71], v[24:27]\n\t"
    "ds_read_b64_tr_b16 v[82:83], v205 offset:4096\n\t"
    "buffer_load_dwordx4 a[220:223], v232, s[56:59], s51 offen offset:3072 sc1\n\t"
    "s_add_u32 s51, s51, 516096\n\t"
    "s_and_b32 s51, s51, 0x3fffff\n\t"
    "v_mfma_f32_16x16x32_bf16 v[24:27], v[120:123], a[80:83], v[24:27]\n\t"
    "ds_read_b64_tr_b16 v[84:85], v205 offset:8192\n\t"
    "v_mfma_f32_16x16x32_bf16 v[24:27], v[124:127], a[84:87], v[24:27]\n\t"
    "ds_read_b64_tr_b16 v[86:87], v205 offset:12288\n\t"
    "v_mfma_f32_16x16x32_bf16 v[28:31], v[112:115], a[72:75], v[28:31]\n\t"
    "ds_read_b64_tr_b16 v[88:89], v205 offset:16384\n\t"
    "v_mfma_f32_16x16x32_bf16 v[28:31], v[116:119], a[76:79], v[28:31]\n\t"
    "ds_read_b64_tr_b16 v[90:91], v205 offset:20480\n\t"
    "v_mfma_f32_16x16x32_bf16 v[28:31], v[120:123], a[88:91], v[28:31]\n\t"
    "ds_read_b64_tr_b16 v[92:93], v205 offset:24576\n\t"
    "v_mfma_f32_16x16x32_bf16 v[28:31], v[124:127], a[92:95], v[28:31]\n\t"
    "ds_read_b64_tr_b16 v[94:95], v205 offset:28672\n\t"
    "s_waitcnt lgkmcnt(8)\n\t"
    "v_mfma_f32_16x16x32_bf16 v[32:35], v[64:67], a[64:67], v[32:35]\n\t"
    "ds_read_b64_tr_b16 v[96:97], v206 offset:0\n\t"
    "v_mfma_f32_16x16x32_bf16 v[32:35], v[68:71], a[68:71], v[32:35]\n\t"
    "ds_read_b64_tr_b16 v[98:99], v206 offset:4096\n\t"
    "v_mfma_f32_16x16x32_bf16 v[32:35], v[72:75], a[80:83], v[32:35]\n\t"
    "ds_read_b64_tr_b16 v[100:101], v206 offset:8192\n\t"
    "v_mfma_f32_16x16x32_bf16 v[32:35], v[76:79], a[84:87], v[32:35]\n\t"
    "ds_read_b64_tr_b16 v[102:103], v206 offset:12288\n\t"
    "v_mfma_f32_16x16x32_bf16 v[36:39], v[64:67], a[72:75], v[36:39]\n\t"
    "ds_read_b64_tr_b16 v[104:105], v206 offset:16384\n\t"
    "v_mfma_f32_16x16x32_bf16 v[36:39], v[68:71], a[76:79], v[36:39]\n\t"
    "ds_read_b64_tr_b16 v[106:107], v206 offset:20480\n\t"
    "v_mfma_f32_16x16x32_bf16 v[36:39], v[72:75], a[88:91], v[36:39]\n\t"
    "ds_read_b64_tr_b16 v[108:109], v206 offset:24576\n\t"
    "v_mfma_f32_16x16x32_bf16 v[36:39], v[76:79], a[92:95], v[36:39]\n\t"
    "ds_read_b64_tr_b16 v[110:111], v206 offset:28672\n\t"
    "s_waitcnt lgkmcnt(8)\n\t"
    "v_mfma_f32_16x16x32_bf16 v[40:43], v[80:83], a[64:67], v[40:43]\n\t"
    "ds_read_b64_tr_b16 v[112:113], v207 offset:0\n\t"
    "v_mfma_f32_16x16x32_bf16 v[40:43], v[84:87], a[68:71], v[40:43]\n\t"
    "ds_read_b64_tr_b16 v[114:115], v207 offset:4096\n\t"
    "v_mfma_f32_16x16x32_bf16 v[40:43], v[88:91], a[80:83], v[40:43]\n\t"
    "ds_read_b64_tr_b16 v[116:117], v207 offset:8192\n\t"
    "v_mfma_f32_16x16x32_bf16 v[40:43], v[92:95], a[84:87], v[40:43]\n\t"
    "ds_read_b64_tr_b16 v[118:119], v207 offset:12288\n\t"
    "v_mfma_f32_16x16x32_bf16 v[44:47], v[80:83], a[72:75], v[44:47]\n\t"
    "ds_read_b64_tr_b16 v[120:121], v207 offset:16384\n\t"
    "v_mfma_f32_16x16x32_bf16 v[44:47], v[84:87], a[76:79], v[44:47]\n\t"
    "ds_read_b64_tr_b16 v[122:123], v207 offset:20480\n\t"
    "v_mfma_f32_16x16x32_bf16 v[44:47], v[88:91], a[88:91], v[44:47]\n\t"
    "ds_read_b64_tr_b16 v[124:125], v207 offset:24576\n\t"
    "v_mfma_f32_16x16x32_bf16 v[44:47], v[92:95], a[92:95], v[44:47]\n\t"
    "ds_read_b64_tr_b16 v[126:127], v207 offset:28672\n\t"
    "s_waitcnt lgkmcnt(0)\n\t"
    "s_waitcnt vmcnt(32)\n\t"
    "s_barrier\n\t"
    "v_mfma_f32_16x16x32_bf16 v[48:51], v[96:99], a[64:67], v[48:51]\n\t"
    "ds_read_b64_tr_b16 v[64:65], v144 offset:0\n\t"
    "v_mfma_f32_16x16x32_bf16 v[48:51], v[100:103], a[68:71], v[48:51]\n\t"
    "ds_read_b64_tr_b16 v[66:67], v144 offset:4096\n\t"
    "s_add_u32 m0, s78, 0\n\t"
    "s_nop 0\n\t"
    "buffer_load_dwordx4 v234, s[64:67], s76 offen lds\n\t"
    "v_mfma_f32_16x16x32_bf16 v[48:51], v[104:107], a[80:83], v[48:51]\n\t"
    "ds_read_b64_tr_b16 v[68:69], v144 offset:8192\n\t"
    "v_mfma_f32_16x16x32_bf16 v[48:51], v[108:111], a[84:87], v[48:51]\n\t"
    "ds_read_b64_tr_b16 v[70:71], v144 offset:12288\n\t"
    "v_mfma_f32_16x16x32_bf16 v[52:55], v[96:99], a[72:75], v[52:55]\n\t"
    "ds_read_b64_tr_b16 v[72:73], v144 offset:16384\n\t"
    "buffer_load_dwordx4 v235, s[64:67], s76 offen offset:1024 lds\n\t"
    "v_mfma_f32_16x16x32_bf16 v[52:55], v[100:103], a[76:79], v[52:55]\n\t"
    "ds_read_b64_tr_b16 v[74:75], v144 offset:20480\n\t"
    "v_mfma_f32_16x16x32_bf16 v[52:55], v[104:107], a[88:91], v[52:55]\n\t"
    "ds_read_b64_tr_b16 v[76:77], v144 offset:24576\n\t"
    "buffer_load_dwordx4 v236, s[64:67], s76 offen offset:2048 lds\n\t"
    "v_mfma_f32_16x16x32_bf16 v[52:55], v[108:111], a[92:95], v[52:55]\n\t"
    "ds_read_b64_tr_b16 v[78:79], v144 offset:28672\n\t"
    "s_waitcnt lgkmcnt(8)\n\t"
    "v_mfma_f32_16x16x32_bf16 v[56:59], v[112:115], a[64:67], v[56:59]\n\t"
    "ds_read_b64_tr_b16 v[80:81], v145 offset:0\n\t"
    "v_mfma_f32_16x16x32_bf16 v[56:59], v[116:119], a[68:71], v[56:59]\n\t"
    "ds_read_b64_tr_b16 v[82:83], v145 offset:4096\n\t"
    "buffer_load_dwordx4 v237, s[64:67], s76 offen offset:3072 lds\n\t"
    "s_add_u32 s76, s76, 65536\n\t"
    "s_and_b32 s76, s76, 0xfffff\n\t"
    "v_mfma_f32_16x16x32_bf16 v[56:59], v[120:123], a[80:83], v[56:59]\n\t"
    "ds_read_b64_tr_b16 v[84:85], v145 offset:8192\n\t"
    "v_mfma_f32_16x16x32_bf16 v[56:59], v[124:127], a[84:87], v[56:59]\n\t"
    "ds_read_b64_tr_b16 v[86:87], v145 offset:12288\n\t"
    "v_mfma_f32_16x16x32_bf16 v[60:63], v[112:115], a[72:75], v[60:63]\n\t"
    "ds_read_b64_tr_b16 v[88:89], v145 offset:16384\n\t"
    "s_add_u32 m0, s78, 16384\n\t"
    "s_nop 0\n\t"
    "buffer_load_dwordx4 v234, s[64:67], s76 offen lds\n\t"
    "v_mfma_f32_16x16x32_bf16 v[60:63], v[116:119], a[76:79], v[60:63]\n\t"
    "ds_read_b64_tr_b16 v[90:91], v145 offset:20480\n\t"
    "v_mfma_f32_16x16x32_bf16 v[60:63], v[120:123], a[88:91], v[60:63]\n\t"
    "ds_read_b64_tr_b16 v[92:93], v145 offset:24576\n\t"
    "buffer_load_dwordx4 v235, s[64:67], s76 offen offset:1024 lds\n\t"
    "v_mfma_f32_16x16x32_bf16 v[60:63], v[124:127], a[92:95], v[60:63]\n\t"
    "ds_read_b64_tr_b16 v[94:95], v145 offset:28672\n\t"
    "s_waitcnt lgkmcnt(8)\n\t"
    "v_mfma_f32_16x16x32_bf16 v[0:3], v[64:67], a[96:99], v[0:3]\n\t"
    "ds_read_b64_tr_b16 v[96:97], v146 offset:0\n\t"
    "v_mfma_f32_16x16x32_bf16 v[0:3], v[68:71], a[100:103], v[0:3]\n\t"
    "ds_read_b64_tr_b16 v[98:99], v146 offset:4096\n\t"
    "buffer_load_dwordx4 v236, s[64:67], s76 offen offset:2048 lds\n\t"
    "v_mfma_f32_16x16x32_bf16 v[0:3], v[72:75], a[112:115], v[0:3]\n\t"
    "ds_read_b64_tr_b16 v[100:101], v146 offset:8192\n\t"
    "v_mfma_f32_16x16x32_bf16 v[0:3], v[76:79], a[116:119], v[0:3]\n\t"
    "ds_read_b64_tr_b16 v[102:103], v146 offset:12288\n\t"
    "v_mfma_f32_16x16x32_bf16 v[4:7], v[64:67], a[104:107], v[4:7]\n\t"
    "ds_read_b64_tr_b16 v[104:105], v146 offset:16384\n\t"
    "buffer_load_dwordx4 v237, s[64:67], s76 offen offset:3072 lds\n\t"
    "s_add_u32 s76, s76, 65536\n\t"
    "s_and_b32 s76, s76, 0xfffff\n\t"
    "v_mfma_f32_16x16x32_bf16 v[4:7], v[68:71], a[108:111], v[4:7]\n\t"
    "ds_read_b64_tr_b16 v[106:107], v146 offset:20480\n\t"
    "v_mfma_f32_16x16x32_bf16 v[4:7], v[72:75], a[120:123], v[4:7]\n\t"
    "ds_read_b64_tr_b16 v[108:109], v146 offset:24576\n\t"
    "buffer_load_dwordx4 a[224:227], v232, s[56:59], s51 offen sc1\n\t"
    "v_mfma_f32_16x16x32_bf16 v[4:7], v[76:79], a[124:127], v[4:7]\n\t"
    "ds_read_b64_tr_b16 v[110:111], v146 offset:28672\n\t"
    "s_waitcnt lgkmcnt(8)\n\t"
    "v_mfma_f32_16x16x32_bf16 v[8:11], v[80:83], a[96:99], v[8:11]\n\t"
    "ds_read_b64_tr_b16 v[112:113], v147 offset:0\n\t"
    "v_mfma_f32_16x16x32_bf16 v[8:11], v[84:87], a[100:103], v[8:11]\n\t"
    "ds_read_b64_tr_b16 v[114:115], v147 offset:4096\n\t"
    "buffer_load_dwordx4 a[228:231], v232, s[56:59], s51 offen offset:1024 sc1\n\t"
    "v_mfma_f32_16x16x32_bf16 v[8:11], v[88:91], a[112:115], v[8:11]\n\t"
    "ds_read_b64_tr_b16 v[116:117], v147 offset:8192\n\t"
    "v_mfma_f32_16x16x32_bf16 v[8:11], v[92:95], a[116:119], v[8:11]\n\t"
    "ds_read_b64_tr_b16 v[118:119], v147 offset:12288\n\t"
    "v_mfma_f32_16x16x32_bf16 v[12:15], v[80:83], a[104:107], v[12:15]\n\t"
    "ds_read_b64_tr_b16 v[120:121], v147 offset:16384\n\t"
    "buffer_load_dwordx4 a[232:235], v232, s[56:59], s51 offen offset:2048 sc1\n\t"
    "v_mfma_f32_16x16x32_bf16 v[12:15], v[84:87], a[108:111], v[12:15]\n\t"
    "ds_read_b64_tr_b16 v[122:123], v147 offset:20480\n\t"
    "v_mfma_f32_16x16x32_bf16 v[12:15], v[88:91], a[120:123], v[12:15]\n\t"
    "ds_read_b64_tr_b16 v[124:125], v147 offset:24576\n\t"
    "buffer_load_dwordx4 a[236:239], v232, s[56:59], s51 offen offset:3072 sc1\n\t"
    "s_add_u32 s51, s51, 8192\n\t"
    "v_mfma_f32_16x16x32_bf16 v[12:15], v[92:95], a[124:127], v[12:15]\n\t"
    "ds_read_b64_tr_b16 v[126:127], v147 offset:28672\n\t"
    "s_waitcnt lgkmcnt(8)\n\t"
    "v_mfma_f32_16x16x32_bf16 v[16:19], v[96:99], a[96:99], v[16:19]\n\t"
    "ds_read_b64_tr_b16 v[64:65], v148 offset:0\n\t"
    "v_mfma_f32_16x16x32_bf16 v[16:19], v[100:103], a[100:103], v[16:19]\n\t"
    "ds_read_b64_tr_b16 v[66:67], v148 offset:4096\n\t"
    "buffer_load_dwordx4 a[240:243], v232, s[56:59], s51 offen sc1\n\t"
    "v_mfma_f32_16x16x32_bf16 v[16:19], v[104:107], a[112:115], v[16:19]\n\t"
    "ds_read_b64_tr_b16 v[68:69], v148 offset:8192\n\t"
    "v_mfma_f32_16x16x32_bf16 v[16:19], v[108:111], a[116:119], v[16:19]\n\t"
    "ds_read_b64_tr_b16 v[70:71], v148 offset:12288\n\t"
    "v_mfma_f32_16x16x32_bf16 v[20:23], v[96:99], a[104:107], v[20:23]\n\t"
    "ds_read_b64_tr_b16 v[72:73], v148 offset:16384\n\t"
    "buffer_load_dwordx4 a[244:247], v232, s[56:59], s51 offen offset:1024 sc1\n\t"
    "v_mfma_f32_16x16x32_bf16 v[20:23], v[100:103], a[108:111], v[20:23]\n\t"
    "ds_read_b64_tr_b16 v[74:75], v148 offset:20480\n\t"
    "v_mfma_f32_16x16x32_bf16 v[20:23], v[104:107], a[120:123], v[20:23]\n\t"
    "ds_read_b64_tr_b16 v[76:77], v148 offset:24576\n\t"
    "buffer_load_dwordx4 a[248:251], v232, s[56:59], s51 offen offset:2048 sc1\n\t"
    "v_mfma_f32_16x16x32_bf16 v[20:23], v[108:111], a[124:127], v[20:23]\n\t"
    "ds_read_b64_tr_b16 v[78:79], v148 offset:28672\n\t"
    "s_waitcnt lgkmcnt(8)\n\t"
    "v_mfma_f32_16x16x32_bf16 v[24:27], v[112:115], a[96:99], v[24:27]\n\t"
    "ds_read_b64_tr_b16 v[80:81], v149 offset:0\n\t"
    "v_mfma_f32_16x16x32_bf16 v[24:27], v[116:119], a[100:103], v[24:27]\n\t"
    "ds_read_b64_tr_b16 v[82:83], v149 offset:4096\n\t"
    "buffer_load_dwordx4 a[252:255], v232, s[56:59], s51 offen offset:3072 sc1\n\t"
    "s_add_u32 s51, s51, 516096\n\t"
    "s_and_b32 s51, s51, 0x3fffff\n\t"
    "v_mfma_f32_16x16x32_bf16 v[24:27], v[120:123], a[112:115], v[24:27]\n\t"
    "ds_read_b64_tr_b16 v[84:85], v149 offset:8192\n\t"
    "v_mfma_f32_16x16x32_bf16 v[24:27], v[124:127], a[116:119], v[24:27]\n\t"
    "ds_read_b64_tr_b16 v[86:87], v149 offset:12288\n\t"
    "v_mfma_f32_16x16x32_bf16 v[28:31], v[112:115], a[104:107], v[28:31]\n\t"
    "ds_read_b64_tr_b16 v[88:89], v149 offset:16384\n\t"
    "v_mfma_f32_16x16x32_bf16 v[28:31], v[116:119], a[108:111], v[28:31]\n\t"
    "ds_read_b64_tr_b16 v[90:91], v149 offset:20480\n\t"
    "v_mfma_f32_16x16x32_bf16 v[28:31], v[120:123], a[120:123], v[28:31]\n\t"
    "ds_read_b64_tr_b16 v[92:93], v149 offset:24576\n\t"
    "v_mfma_f32_16x16x32_bf16 v[28:31], v[124:127], a[124:127], v[28:31]\n\t"
    "ds_read_b64_tr_b16 v[94:95], v149 offset:28672\n\t"
    "s_waitcnt lgkmcnt(8)\n\t"
    "v_mfma_f32_16x16x32_bf16 v[32:35], v[64:67], a[96:99], v[32:35]\n\t"
    "ds_read_b64_tr_b16 v[96:97], v150 offset:0\n\t"
    "v_mfma_f32_16x16x32_bf16 v[32:35], v[68:71], a[100:103], v[32:35]\n\t"
    "ds_read_b64_tr_b16 v[98:99], v150 offset:4096\n\t"
    "v_mfma_f32_16x16x32_bf16 v[32:35], v[72:75], a[112:115], v[32:35]\n\t"
    "ds_read_b64_tr_b16 v[100:101], v150 offset:8192\n\t"
    "v_mfma_f32_16x16x32_bf16 v[32:35], v[76:79], a[116:119], v[32:35]\n\t"
    "ds_read_b64_tr_b16 v[102:103], v150 offset:12288\n\t"
    "v_mfma_f32_16x16x32_bf16 v[36:39], v[64:67], a[104:107], v[36:39]\n\t"
    "ds_read_b64_tr_b16 v[104:105], v150 offset:16384\n\t"
    "v_mfma_f32_16x16x32_bf16 v[36:39], v[68:71], a[108:111], v[36:39]\n\t"
    "ds_read_b64_tr_b16 v[106:107], v150 offset:20480\n\t"
    "v_mfma_f32_16x16x32_bf16 v[36:39], v[72:75], a[120:123], v[36:39]\n\t"
    "ds_read_b64_tr_b16 v[108:109], v150 offset:24576\n\t"
    "v_mfma_f32_16x16x32_bf16 v[36:39], v[76:79], a[124:127], v[36:39]\n\t"
    "ds_read_b64_tr_b16 v[110:111], v150 offset:28672\n\t"
    "s_waitcnt lgkmcnt(8)\n\t"
    "v_mfma_f32_16x16x32_bf16 v[40:43], v[80:83], a[96:99], v[40:43]\n\t"
    "ds_read_b64_tr_b16 v[112:113], v151 offset:0\n\t"
    "v_mfma_f32_16x16x32_bf16 v[40:43], v[84:87], a[100:103], v[40:43]\n\t"
    "ds_read_b64_tr_b16 v[114:115], v151 offset:4096\n\t"
    "v_mfma_f32_16x16x32_bf16 v[40:43], v[88:91], a[112:115], v[40:43]\n\t"
    "ds_read_b64_tr_b16 v[116:117], v151 offset:8192\n\t"
    "v_mfma_f32_16x16x32_bf16 v[40:43], v[92:95], a[116:119], v[40:43]\n\t"
    "ds_read_b64_tr_b16 v[118:119], v151 offset:12288\n\t"
    "v_mfma_f32_16x16x32_bf16 v[44:47], v[80:83], a[104:107], v[44:47]\n\t"
    "ds_read_b64_tr_b16 v[120:121], v151 offset:16384\n\t"
    "v_mfma_f32_16x16x32_bf16 v[44:47], v[84:87], a[108:111], v[44:47]\n\t"
    "ds_read_b64_tr_b16 v[122:123], v151 offset:20480\n\t"
    "v_mfma_f32_16x16x32_bf16 v[44:47], v[88:91], a[120:123], v[44:47]\n\t"
    "ds_read_b64_tr_b16 v[124:125], v151 offset:24576\n\t"
    "v_mfma_f32_16x16x32_bf16 v[44:47], v[92:95], a[124:127], v[44:47]\n\t"
    "ds_read_b64_tr_b16 v[126:127], v151 offset:28672\n\t"
    "s_waitcnt lgkmcnt(0)\n\t"
    "s_waitcnt vmcnt(40)\n\t"
    "s_barrier\n\t"
    "v_mfma_f32_16x16x32_bf16 v[48:51], v[96:99], a[96:99], v[48:51]\n\t"
    "ds_read_b64_tr_b16 v[64:65], v136 offset:0\n\t"
    "v_mfma_f32_16x16x32_bf16 v[48:51], v[100:103], a[100:103], v[48:51]\n\t"
    "ds_read_b64_tr_b16 v[66:67], v136 offset:4096\n\t"
    "v_mfma_f32_16x16x32_bf16 v[48:51], v[104:107], a[112:115], v[48:51]\n\t"
    "ds_read_b64_tr_b16 v[68:69], v136 offset:8192\n\t"
    "v_mfma_f32_16x16x32_bf16 v[48:51], v[108:111], a[116:119], v[48:51]\n\t"
    "ds_read_b64_tr_b16 v[70:71], v136 offset:12288\n\t"
    "v_mfma_f32_16x16x32_bf16 v[52:55], v[96:99], a[104:107], v[52:55]\n\t"
    "ds_read_b64_tr_b16 v[72:73], v136 offset:16384\n\t"
    "v_mfma_f32_16x16x32_bf16 v[52:55], v[100:103], a[108:111], v[52:55]\n\t"
    "ds_read_b64_tr_b16 v[74:75], v136 offset:20480\n\t"
    "v_mfma_f32_16x16x32_bf16 v[52:55], v[104:107], a[120:123], v[52:55]\n\t"
    "ds_read_b64_tr_b16 v[76:77], v136 offset:24576\n\t"
    "v_mfma_f32_16x16x32_bf16 v[52:55], v[108:111], a[124:127], v[52:55]\n\t"
    "ds_read_b64_tr_b16 v[78:79], v136 offset:28672\n\t"
    "s_waitcnt lgkmcnt(8)\n\t"
    "v_mfma_f32_16x16x32_bf16 v[56:59], v[112:115], a[96:99], v[56:59]\n\t"
    "ds_read_b64_tr_b16 v[80:81], v137 offset:0\n\t"
    "v_mfma_f32_16x16x32_bf16 v[56:59], v[116:119], a[100:103], v[56:59]\n\t"
    "ds_read_b64_tr_b16 v[82:83], v137 offset:4096\n\t"
    "v_mfma_f32_16x16x32_bf16 v[56:59], v[120:123], a[112:115], v[56:59]\n\t"
    "ds_read_b64_tr_b16 v[84:85], v137 offset:8192\n\t"
    "v_mfma_f32_16x16x32_bf16 v[56:59], v[124:127], a[116:119], v[56:59]\n\t"
    "ds_read_b64_tr_b16 v[86:87], v137 offset:12288\n\t"
    "v_mfma_f32_16x16x32_bf16 v[60:63], v[112:115], a[104:107], v[60:63]\n\t"
    "ds_read_b64_tr_b16 v[88:89], v137 offset:16384\n\t"
    "v_mfma_f32_16x16x32_bf16 v[60:63], v[116:119], a[108:111], v[60:63]\n\t"
    "ds_read_b64_tr_b16 v[90:91], v137 offset:20480\n\t"
    "v_mfma_f32_16x16x32_bf16 v[60:63], v[120:123], a[120:123], v[60:63]\n\t"
    "ds_read_b64_tr_b16 v[92:93], v137 offset:24576\n\t"
    "v_mfma_f32_16x16x32_bf16 v[60:63], v[124:127], a[124:127], v[60:63]\n\t"
    "ds_read_b64_tr_b16 v[94:95], v137 offset:28672\n\t"
    "s_waitcnt lgkmcnt(8)\n\t"
    "v_mfma_f32_16x16x32_bf16 v[0:3], v[64:67], a[128:131], v[0:3]\n\t"
    "ds_read_b64_tr_b16 v[96:97], v138 offset:0\n\t"
    "v_mfma_f32_16x16x32_bf16 v[0:3], v[68:71], a[132:135], v[0:3]\n\t"
    "ds_read_b64_tr_b16 v[98:99], v138 offset:4096\n\t"
    "v_mfma_f32_16x16x32_bf16 v[0:3], v[72:75], a[144:147], v[0:3]\n\t"
    "ds_read_b64_tr_b16 v[100:101], v138 offset:8192\n\t"
    "v_mfma_f32_16x16x32_bf16 v[0:3], v[76:79], a[148:151], v[0:3]\n\t"
    "ds_read_b64_tr_b16 v[102:103], v138 offset:12288\n\t"
    "v_mfma_f32_16x16x32_bf16 v[4:7], v[64:67], a[136:139], v[4:7]\n\t"
    "ds_read_b64_tr_b16 v[104:105], v138 offset:16384\n\t"
    "v_mfma_f32_16x16x32_bf16 v[4:7], v[68:71], a[140:143], v[4:7]\n\t"
    "ds_read_b64_tr_b16 v[106:107], v138 offset:20480\n\t"
    "v_mfma_f32_16x16x32_bf16 v[4:7], v[72:75], a[152:155], v[4:7]\n\t"
    "ds_read_b64_tr_b16 v[108:109], v138 offset:24576\n\t"
    "v_mfma_f32_16x16x32_bf16 v[4:7], v[76:79], a[156:159], v[4:7]\n\t"
    "ds_read_b64_tr_b16 v[110:111], v138 offset:28672\n\t"
    "s_waitcnt lgkmcnt(8)\n\t"
    "v_mfma_f32_16x16x32_bf16 v[8:11], v[80:83], a[128:131], v[8:11]\n\t"
    "ds_read_b64_tr_b16 v[112:113], v139 offset:0\n\t"
    "v_mfma_f32_16x16x32_bf16 v[8:11], v[84:87], a[132:135], v[8:11]\n\t"
    "ds_read_b64_tr_b16 v[114:115], v139 offset:4096\n\t"
    "v_mfma_f32_16x16x32_bf16 v[8:11], v[88:91], a[144:147], v[8:11]\n\t"
    "ds_read_b64_tr_b16 v[116:117], v139 offset:8192\n\t"
    "v_mfma_f32_16x16x32_bf16 v[8:11], v[92:95], a[148:151], v[8:11]\n\t"
    "ds_read_b64_tr_b16 v[118:119], v139 offset:12288\n\t"
    "v_mfma_f32_16x16x32_bf16 v[12:15], v[80:83], a[136:139], v[12:15]\n\t"
    "ds_read_b64_tr_b16 v[120:121], v139 offset:16384\n\t"
    "v_mfma_f32_16x16x32_bf16 v[12:15], v[84:87], a[140:143], v[12:15]\n\t"
    "ds_read_b64_tr_b16 v[122:123], v139 offset:20480\n\t"
    "v_mfma_f32_16x16x32_bf16 v[12:15], v[88:91], a[152:155], v[12:15]\n\t"
    "ds_read_b64_tr_b16 v[124:125], v139 offset:24576\n\t"
    "v_mfma_f32_16x16x32_bf16 v[12:15], v[92:95], a[156:159], v[12:15]\n\t"
    "ds_read_b64_tr_b16 v[126:127], v139 offset:28672\n\t"
    "s_waitcnt lgkmcnt(8)\n\t"
    "v_mfma_f32_16x16x32_bf16 v[16:19], v[96:99], a[128:131], v[16:19]\n\t"
    "ds_read_b64_tr_b16 v[64:65], v140 offset:0\n\t"
    "v_mfma_f32_16x16x32_bf16 v[16:19], v[100:103], a[132:135], v[16:19]\n\t"
    "ds_read_b64_tr_b16 v[66:67], v140 offset:4096\n\t"
    "v_mfma_f32_16x16x32_bf16 v[16:19], v[104:107], a[144:147], v[16:19]\n\t"
    "ds_read_b64_tr_b16 v[68:69], v140 offset:8192\n\t"
    "v_mfma_f32_16x16x32_bf16 v[16:19], v[108:111], a[148:151], v[16:19]\n\t"
    "ds_read_b64_tr_b16 v[70:71], v140 offset:12288\n\t"
    "v_mfma_f32_16x16x32_bf16 v[20:23], v[96:99], a[136:139], v[20:23]\n\t"
    "ds_read_b64_tr_b16 v[72:73], v140 offset:16384\n\t"
    "v_mfma_f32_16x16x32_bf16 v[20:23], v[100:103], a[140:143], v[20:23]\n\t"
    "ds_read_b64_tr_b16 v[74:75], v140 offset:20480\n\t"
    "v_mfma_f32_16x16x32_bf16 v[20:23], v[104:107], a[152:155], v[20:23]\n\t"
    "ds_read_b64_tr_b16 v[76:77], v140 offset:24576\n\t"
    "v_mfma_f32_16x16x32_bf16 v[20:23], v[108:111], a[156:159], v[20:23]\n\t"
    "ds_read_b64_tr_b16 v[78:79], v140 offset:28672\n\t"
    "s_waitcnt lgkmcnt(8)\n\t"
    "v_mfma_f32_16x16x32_bf16 v[24:27], v[112:115], a[128:131], v[24:27]\n\t"
    "ds_read_b64_tr_b16 v[80:81], v141 offset:0\n\t"
    "v_mfma_f32_16x16x32_bf16 v[24:27], v[116:119], a[132:135], v[24:27]\n\t"
    "ds_read_b64_tr_b16 v[82:83], v141 offset:4096\n\t"
    "v_mfma_f32_16x16x32_bf16 v[24:27], v[120:123], a[144:147], v[24:27]\n\t"
    "ds_read_b64_tr_b16 v[84:85], v141 offset:8192\n\t"
    "v_mfma_f32_16x16x32_bf16 v[24:27], v[124:127], a[148:151], v[24:27]\n\t"
    "ds_read_b64_tr_b16 v[86:87], v141 offset:12288\n\t"
    "v_mfma_f32_16x16x32_bf16 v[28:31], v[112:115], a[136:139], v[28:31]\n\t"
    "ds_read_b64_tr_b16 v[88:89], v141 offset:16384\n\t"
    "v_mfma_f32_16x16x32_bf16 v[28:31], v[116:119], a[140:143], v[28:31]\n\t"
    "ds_read_b64_tr_b16 v[90:91], v141 offset:20480\n\t"
    "v_mfma_f32_16x16x32_bf16 v[28:31], v[120:123], a[152:155], v[28:31]\n\t"
    "ds_read_b64_tr_b16 v[92:93], v141 offset:24576\n\t"
    "v_mfma_f32_16x16x32_bf16 v[28:31], v[124:127], a[156:159], v[28:31]\n\t"
    "ds_read_b64_tr_b16 v[94:95], v141 offset:28672\n\t"
    "s_waitcnt lgkmcnt(8)\n\t"
    "v_mfma_f32_16x16x32_bf16 v[32:35], v[64:67], a[128:131], v[32:35]\n\t"
    "ds_read_b64_tr_b16 v[96:97], v142 offset:0\n\t"
    "v_mfma_f32_16x16x32_bf16 v[32:35], v[68:71], a[132:135], v[32:35]\n\t"
    "ds_read_b64_tr_b16 v[98:99], v142 offset:4096\n\t"
    "v_mfma_f32_16x16x32_bf16 v[32:35], v[72:75], a[144:147], v[32:35]\n\t"
    "ds_read_b64_tr_b16 v[100:101], v142 offset:8192\n\t"
    "v_mfma_f32_16x16x32_bf16 v[32:35], v[76:79], a[148:151], v[32:35]\n\t"
    "ds_read_b64_tr_b16 v[102:103], v142 offset:12288\n\t"
    "v_mfma_f32_16x16x32_bf16 v[36:39], v[64:67], a[136:139], v[36:39]\n\t"
    "ds_read_b64_tr_b16 v[104:105], v142 offset:16384\n\t"
    "v_mfma_f32_16x16x32_bf16 v[36:39], v[68:71], a[140:143], v[36:39]\n\t"
    "ds_read_b64_tr_b16 v[106:107], v142 offset:20480\n\t"
    "v_mfma_f32_16x16x32_bf16 v[36:39], v[72:75], a[152:155], v[36:39]\n\t"
    "ds_read_b64_tr_b16 v[108:109], v142 offset:24576\n\t"
    "v_mfma_f32_16x16x32_bf16 v[36:39], v[76:79], a[156:159], v[36:39]\n\t"
    "ds_read_b64_tr_b16 v[110:111], v142 offset:28672\n\t"
    "s_waitcnt lgkmcnt(8)\n\t"
    "v_mfma_f32_16x16x32_bf16 v[40:43], v[80:83], a[128:131], v[40:43]\n\t"
    "ds_read_b64_tr_b16 v[112:113], v143 offset:0\n\t"
    "v_mfma_f32_16x16x32_bf16 v[40:43], v[84:87], a[132:135], v[40:43]\n\t"
    "ds_read_b64_tr_b16 v[114:115], v143 offset:4096\n\t"
    "v_mfma_f32_16x16x32_bf16 v[40:43], v[88:91], a[144:147], v[40:43]\n\t"
    "ds_read_b64_tr_b16 v[116:117], v143 offset:8192\n\t"
    "v_mfma_f32_16x16x32_bf16 v[40:43], v[92:95], a[148:151], v[40:43]\n\t"
    "ds_read_b64_tr_b16 v[118:119], v143 offset:12288\n\t"
    "v_mfma_f32_16x16x32_bf16 v[44:47], v[80:83], a[136:139], v[44:47]\n\t"
    "ds_read_b64_tr_b16 v[120:121], v143 offset:16384\n\t"
    "v_mfma_f32_16x16x32_bf16 v[44:47], v[84:87], a[140:143], v[44:47]\n\t"
    "ds_read_b64_tr_b16 v[122:123], v143 offset:20480\n\t"
    "v_mfma_f32_16x16x32_bf16 v[44:47], v[88:91], a[152:155], v[44:47]\n\t"
    "ds_read_b64_tr_b16 v[124:125], v143 offset:24576\n\t"
    "v_mfma_f32_16x16x32_bf16 v[44:47], v[92:95], a[156:159], v[44:47]\n\t"
    "ds_read_b64_tr_b16 v[126:127], v143 offset:28672\n\t"
    "s_waitcnt lgkmcnt(0)\n\t"
    "s_waitcnt vmcnt(32)\n\t"
    "s_barrier\n\t"
    "v_mfma_f32_16x16x32_bf16 v[48:51], v[96:99], a[128:131], v[48:51]\n\t"
    "ds_read_b64_tr_b16 v[64:65], v200 offset:32768\n\t"
    "v_mfma_f32_16x16x32_bf16 v[48:51], v[100:103], a[132:135], v[48:51]\n\t"
    "ds_read_b64_tr_b16 v[66:67], v200 offset:36864\n\t"
    "v_mfma_f32_16x16x32_bf16 v[48:51], v[104:107], a[144:147], v[48:51]\n\t"
    "ds_read_b64_tr_b16 v[68:69], v200 offset:40960\n\t"
    "v_mfma_f32_16x16x32_bf16 v[48:51], v[108:111], a[148:151], v[48:51]\n\t"
    "ds_read_b64_tr_b16 v[70:71], v200 offset:45056\n\t"
    "v_mfma_f32_16x16x32_bf16 v[52:55], v[96:99], a[136:139], v[52:55]\n\t"
    "ds_read_b64_tr_b16 v[72:73], v200 offset:49152\n\t"
    "v_mfma_f32_16x16x32_bf16 v[52:55], v[100:103], a[140:143], v[52:55]\n\t"
    "ds_read_b64_tr_b16 v[74:75], v200 offset:53248\n\t"
    "v_mfma_f32_16x16x32_bf16 v[52:55], v[104:107], a[152:155], v[52:55]\n\t"
    "ds_read_b64_tr_b16 v[76:77], v200 offset:57344\n\t"
    "v_mfma_f32_16x16x32_bf16 v[52:55], v[108:111], a[156:159], v[52:55]\n\t"
    "ds_read_b64_tr_b16 v[78:79], v200 offset:61440\n\t"
    "s_waitcnt lgkmcnt(8)\n\t"
    "v_mfma_f32_16x16x32_bf16 v[56:59], v[112:115], a[128:131], v[56:59]\n\t"
    "ds_read_b64_tr_b16 v[80:81], v201 offset:32768\n\t"
    "v_mfma_f32_16x16x32_bf16 v[56:59], v[116:119], a[132:135], v[56:59]\n\t"
    "ds_read_b64_tr_b16 v[82:83], v201 offset:36864\n\t"
    "v_mfma_f32_16x16x32_bf16 v[56:59], v[120:123], a[144:147], v[56:59]\n\t"
    "ds_read_b64_tr_b16 v[84:85], v201 offset:40960\n\t"
    "v_mfma_f32_16x16x32_bf16 v[56:59], v[124:127], a[148:151], v[56:59]\n\t"
    "ds_read_b64_tr_b16 v[86:87], v201 offset:45056\n\t"
    "v_mfma_f32_16x16x32_bf16 v[60:63], v[112:115], a[136:139], v[60:63]\n\t"
    "ds_read_b64_tr_b16 v[88:89], v201 offset:49152\n\t"
    "v_mfma_f32_16x16x32_bf16 v[60:63], v[116:119], a[140:143], v[60:63]\n\t"
    "ds_read_b64_tr_b16 v[90:91], v201 offset:53248\n\t"
    "v_mfma_f32_16x16x32_bf16 v[60:63], v[120:123], a[152:155], v[60:63]\n\t"
    "ds_read_b64_tr_b16 v[92:93], v201 offset:57344\n\t"
    "v_mfma_f32_16x16x32_bf16 v[60:63], v[124:127], a[156:159], v[60:63]\n\t"
    "ds_read_b64_tr_b16 v[94:95], v201 offset:61440\n\t"
    "s_waitcnt lgkmcnt(8)\n\t"
    "v_mfma_f32_16x16x32_bf16 v[0:3], v[64:67], a[160:163], v[0:3]\n\t"
    "ds_read_b64_tr_b16 v[96:97], v202 offset:32768\n\t"
    "v_mfma_f32_16x16x32_bf16 v[0:3], v[68:71], a[164:167], v[0:3]\n\t"
    "ds_read_b64_tr_b16 v[98:99], v202 offset:36864\n\t"
    "v_mfma_f32_16x16x32_bf16 v[0:3], v[72:75], a[176:179], v[0:3]\n\t"
    "ds_read_b64_tr_b16 v[100:101], v202 offset:40960\n\t"
    "v_mfma_f32_16x16x32_bf16 v[0:3], v[76:79], a[180:183], v[0:3]\n\t"
    "ds_read_b64_tr_b16 v[102:103], v202 offset:45056\n\t"
    "v_mfma_f32_16x16x32_bf16 v[4:7], v[64:67], a[168:171], v[4:7]\n\t"
    "ds_read_b64_tr_b16 v[104:105], v202 offset:49152\n\t"
    "v_mfma_f32_16x16x32_bf16 v[4:7], v[68:71], a[172:175], v[4:7]\n\t"
    "ds_read_b64_tr_b16 v[106:107], v202 offset:53248\n\t"
    "v_mfma_f32_16x16x32_bf16 v[4:7], v[72:75], a[184:187], v[4:7]\n\t"
    "ds_read_b64_tr_b16 v[108:109], v202 offset:57344\n\t"
    "v_mfma_f32_16x16x32_bf16 v[4:7], v[76:79], a[188:191], v[4:7]\n\t"
    "ds_read_b64_tr_b16 v[110:111], v202 offset:61440\n\t"
    "s_waitcnt lgkmcnt(8)\n\t"
    "v_mfma_f32_16x16x32_bf16 v[8:11], v[80:83], a[160:163], v[8:11]\n\t"
    "ds_read_b64_tr_b16 v[112:113], v203 offset:32768\n\t"
    "v_mfma_f32_16x16x32_bf16 v[8:11], v[84:87], a[164:167], v[8:11]\n\t"
    "ds_read_b64_tr_b16 v[114:115], v203 offset:36864\n\t"
    "v_mfma_f32_16x16x32_bf16 v[8:11], v[88:91], a[176:179], v[8:11]\n\t"
    "ds_read_b64_tr_b16 v[116:117], v203 offset:40960\n\t"
    "v_mfma_f32_16x16x32_bf16 v[8:11], v[92:95], a[180:183], v[8:11]\n\t"
    "ds_read_b64_tr_b16 v[118:119], v203 offset:45056\n\t"
    "v_mfma_f32_16x16x32_bf16 v[12:15], v[80:83], a[168:171], v[12:15]\n\t"
    "ds_read_b64_tr_b16 v[120:121], v203 offset:49152\n\t"
    "v_mfma_f32_16x16x32_bf16 v[12:15], v[84:87], a[172:175], v[12:15]\n\t"
    "ds_read_b64_tr_b16 v[122:123], v203 offset:53248\n\t"
    "v_mfma_f32_16x16x32_bf16 v[12:15], v[88:91], a[184:187], v[12:15]\n\t"
    "ds_read_b64_tr_b16 v[124:125], v203 offset:57344\n\t"
    "v_mfma_f32_16x16x32_bf16 v[12:15], v[92:95], a[188:191], v[12:15]\n\t"
    "ds_read_b64_tr_b16 v[126:127], v203 offset:61440\n\t"
    "s_waitcnt lgkmcnt(8)\n\t"
    "v_mfma_f32_16x16x32_bf16 v[16:19], v[96:99], a[160:163], v[16:19]\n\t"
    "ds_read_b64_tr_b16 v[64:65], v204 offset:32768\n\t"
    "v_mfma_f32_16x16x32_bf16 v[16:19], v[100:103], a[164:167], v[16:19]\n\t"
    "ds_read_b64_tr_b16 v[66:67], v204 offset:36864\n\t"
    "v_mfma_f32_16x16x32_bf16 v[16:19], v[104:107], a[176:179], v[16:19]\n\t"
    "ds_read_b64_tr_b16 v[68:69], v204 offset:40960\n\t"
    "v_mfma_f32_16x16x32_bf16 v[16:19], v[108:111], a[180:183], v[16:19]\n\t"
    "ds_read_b64_tr_b16 v[70:71], v204 offset:45056\n\t"
    "v_mfma_f32_16x16x32_bf16 v[20:23], v[96:99], a[168:171], v[20:23]\n\t"
    "ds_read_b64_tr_b16 v[72:73], v204 offset:49152\n\t"
    "v_mfma_f32_16x16x32_bf16 v[20:23], v[100:103], a[172:175], v[20:23]\n\t"
    "ds_read_b64_tr_b16 v[74:75], v204 offset:53248\n\t"
    "v_mfma_f32_16x16x32_bf16 v[20:23], v[104:107], a[184:187], v[20:23]\n\t"
    "ds_read_b64_tr_b16 v[76:77], v204 offset:57344\n\t"
    "v_mfma_f32_16x16x32_bf16 v[20:23], v[108:111], a[188:191], v[20:23]\n\t"
    "ds_read_b64_tr_b16 v[78:79], v204 offset:61440\n\t"
    "s_waitcnt lgkmcnt(8)\n\t"
    "v_mfma_f32_16x16x32_bf16 v[24:27], v[112:115], a[160:163], v[24:27]\n\t"
    "ds_read_b64_tr_b16 v[80:81], v205 offset:32768\n\t"
    "v_mfma_f32_16x16x32_bf16 v[24:27], v[116:119], a[164:167], v[24:27]\n\t"
    "ds_read_b64_tr_b16 v[82:83], v205 offset:36864\n\t"
    "v_mfma_f32_16x16x32_bf16 v[24:27], v[120:123], a[176:179], v[24:27]\n\t"
    "ds_read_b64_tr_b16 v[84:85], v205 offset:40960\n\t"
    "v_mfma_f32_16x16x32_bf16 v[24:27], v[124:127], a[180:183], v[24:27]\n\t"
    "ds_read_b64_tr_b16 v[86:87], v205 offset:45056\n\t"
    "v_mfma_f32_16x16x32_bf16 v[28:31], v[112:115], a[168:171], v[28:31]\n\t"
    "ds_read_b64_tr_b16 v[88:89], v205 offset:49152\n\t"
    "v_mfma_f32_16x16x32_bf16 v[28:31], v[116:119], a[172:175], v[28:31]\n\t"
    "ds_read_b64_tr_b16 v[90:91], v205 offset:53248\n\t"
    "v_mfma_f32_16x16x32_bf16 v[28:31], v[120:123], a[184:187], v[28:31]\n\t"
    "ds_read_b64_tr_b16 v[92:93], v205 offset:57344\n\t"
    "v_mfma_f32_16x16x32_bf16 v[28:31], v[124:127], a[188:191], v[28:31]\n\t"
    "ds_read_b64_tr_b16 v[94:95], v205 offset:61440\n\t"
    "s_waitcnt lgkmcnt(8)\n\t"
    "v_mfma_f32_16x16x32_bf16 v[32:35], v[64:67], a[160:163], v[32:35]\n\t"
    "ds_read_b64_tr_b16 v[96:97], v206 offset:32768\n\t"
    "v_mfma_f32_16x16x32_bf16 v[32:35], v[68:71], a[164:167], v[32:35]\n\t"
    "ds_read_b64_tr_b16 v[98:99], v206 offset:36864\n\t"
    "v_mfma_f32_16x16x32_bf16 v[32:35], v[72:75], a[176:179], v[32:35]\n\t"
    "ds_read_b64_tr_b16 v[100:101], v206 offset:40960\n\t"
    "v_mfma_f32_16x16x32_bf16 v[32:35], v[76:79], a[180:183], v[32:35]\n\t"
    "ds_read_b64_tr_b16 v[102:103], v206 offset:45056\n\t"
    "v_mfma_f32_16x16x32_bf16 v[36:39], v[64:67], a[168:171], v[36:39]\n\t"
    "ds_read_b64_tr_b16 v[104:105], v206 offset:49152\n\t"
    "v_mfma_f32_16x16x32_bf16 v[36:39], v[68:71], a[172:175], v[36:39]\n\t"
    "ds_read_b64_tr_b16 v[106:107], v206 offset:53248\n\t"
    "v_mfma_f32_16x16x32_bf16 v[36:39], v[72:75], a[184:187], v[36:39]\n\t"
    "ds_read_b64_tr_b16 v[108:109], v206 offset:57344\n\t"
    "v_mfma_f32_16x16x32_bf16 v[36:39], v[76:79], a[188:191], v[36:39]\n\t"
    "ds_read_b64_tr_b16 v[110:111], v206 offset:61440\n\t"
    "s_waitcnt lgkmcnt(8)\n\t"
    "v_mfma_f32_16x16x32_bf16 v[40:43], v[80:83], a[160:163], v[40:43]\n\t"
    "ds_read_b64_tr_b16 v[112:113], v207 offset:32768\n\t"
    "v_mfma_f32_16x16x32_bf16 v[40:43], v[84:87], a[164:167], v[40:43]\n\t"
    "ds_read_b64_tr_b16 v[114:115], v207 offset:36864\n\t"
    "v_mfma_f32_16x16x32_bf16 v[40:43], v[88:91], a[176:179], v[40:43]\n\t"
    "ds_read_b64_tr_b16 v[116:117], v207 offset:40960\n\t"
    "v_mfma_f32_16x16x32_bf16 v[40:43], v[92:95], a[180:183], v[40:43]\n\t"
    "ds_read_b64_tr_b16 v[118:119], v207 offset:45056\n\t"
    "v_mfma_f32_16x16x32_bf16 v[44:47], v[80:83], a[168:171], v[44:47]\n\t"
    "ds_read_b64_tr_b16 v[120:121], v207 offset:49152\n\t"
    "v_mfma_f32_16x16x32_bf16 v[44:47], v[84:87], a[172:175], v[44:47]\n\t"
    "ds_read_b64_tr_b16 v[122:123], v207 offset:53248\n\t"
    "v_mfma_f32_16x16x32_bf16 v[44:47], v[88:91], a[184:187], v[44:47]\n\t"
    "ds_read_b64_tr_b16 v[124:125], v207 offset:57344\n\t"
    "v_mfma_f32_16x16x32_bf16 v[44:47], v[92:95], a[188:191], v[44:47]\n\t"
    "ds_read_b64_tr_b16 v[126:127], v207 offset:61440\n\t"
    "s_waitcnt lgkmcnt(0)\n\t"
    "s_waitcnt vmcnt(16)\n\t"
    "s_barrier\n\t"
    "v_mfma_f32_16x16x32_bf16 v[48:51], v[96:99], a[160:163], v[48:51]\n\t"
    "ds_read_b64_tr_b16 v[64:65], v136 offset:32768\n\t"
    "v_mfma_f32_16x16x32_bf16 v[48:51], v[100:103], a[164:167], v[48:51]\n\t"
    "ds_read_b64_tr_b16 v[66:67], v136 offset:36864\n\t"
    "v_mfma_f32_16x16x32_bf16 v[48:51], v[104:107], a[176:179], v[48:51]\n\t"
    "ds_read_b64_tr_b16 v[68:69], v136 offset:40960\n\t"
    "v_mfma_f32_16x16x32_bf16 v[48:51], v[108:111], a[180:183], v[48:51]\n\t"
    "ds_read_b64_tr_b16 v[70:71], v136 offset:45056\n\t"
    "v_mfma_f32_16x16x32_bf16 v[52:55], v[96:99], a[168:171], v[52:55]\n\t"
    "ds_read_b64_tr_b16 v[72:73], v136 offset:49152\n\t"
    "v_mfma_f32_16x16x32_bf16 v[52:55], v[100:103], a[172:175], v[52:55]\n\t"
    "ds_read_b64_tr_b16 v[74:75], v136 offset:53248\n\t"
    "v_mfma_f32_16x16x32_bf16 v[52:55], v[104:107], a[184:187], v[52:55]\n\t"
    "ds_read_b64_tr_b16 v[76:77], v136 offset:57344\n\t"
    "v_mfma_f32_16x16x32_bf16 v[52:55], v[108:111], a[188:191], v[52:55]\n\t"
    "ds_read_b64_tr_b16 v[78:79], v136 offset:61440\n\t"
    "s_waitcnt lgkmcnt(8)\n\t"
    "v_mfma_f32_16x16x32_bf16 v[56:59], v[112:115], a[160:163], v[56:59]\n\t"
    "ds_read_b64_tr_b16 v[80:81], v137 offset:32768\n\t"
    "v_mfma_f32_16x16x32_bf16 v[56:59], v[116:119], a[164:167], v[56:59]\n\t"
    "ds_read_b64_tr_b16 v[82:83], v137 offset:36864\n\t"
    "v_mfma_f32_16x16x32_bf16 v[56:59], v[120:123], a[176:179], v[56:59]\n\t"
    "ds_read_b64_tr_b16 v[84:85], v137 offset:40960\n\t"
    "v_mfma_f32_16x16x32_bf16 v[56:59], v[124:127], a[180:183], v[56:59]\n\t"
    "ds_read_b64_tr_b16 v[86:87], v137 offset:45056\n\t"
    "v_mfma_f32_16x16x32_bf16 v[60:63], v[112:115], a[168:171], v[60:63]\n\t"
    "ds_read_b64_tr_b16 v[88:89], v137 offset:49152\n\t"
    "v_mfma_f32_16x16x32_bf16 v[60:63], v[116:119], a[172:175], v[60:63]\n\t"
    "ds_read_b64_tr_b16 v[90:91], v137 offset:53248\n\t"
    "v_mfma_f32_16x16x32_bf16 v[60:63], v[120:123], a[184:187], v[60:63]\n\t"
    "ds_read_b64_tr_b16 v[92:93], v137 offset:57344\n\t"
    "v_mfma_f32_16x16x32_bf16 v[60:63], v[124:127], a[188:191], v[60:63]\n\t"
    "ds_read_b64_tr_b16 v[94:95], v137 offset:61440\n\t"
    "s_waitcnt lgkmcnt(8)\n\t"
    "v_mfma_f32_16x16x32_bf16 v[0:3], v[64:67], a[192:195], v[0:3]\n\t"
    "ds_read_b64_tr_b16 v[96:97], v138 offset:32768\n\t"
    "v_mfma_f32_16x16x32_bf16 v[0:3], v[68:71], a[196:199], v[0:3]\n\t"
    "ds_read_b64_tr_b16 v[98:99], v138 offset:36864\n\t"
    "v_mfma_f32_16x16x32_bf16 v[0:3], v[72:75], a[208:211], v[0:3]\n\t"
    "ds_read_b64_tr_b16 v[100:101], v138 offset:40960\n\t"
    "v_mfma_f32_16x16x32_bf16 v[0:3], v[76:79], a[212:215], v[0:3]\n\t"
    "ds_read_b64_tr_b16 v[102:103], v138 offset:45056\n\t"
    "v_mfma_f32_16x16x32_bf16 v[4:7], v[64:67], a[200:203], v[4:7]\n\t"
    "ds_read_b64_tr_b16 v[104:105], v138 offset:49152\n\t"
    "v_mfma_f32_16x16x32_bf16 v[4:7], v[68:71], a[204:207], v[4:7]\n\t"
    "ds_read_b64_tr_b16 v[106:107], v138 offset:53248\n\t"
    "v_mfma_f32_16x16x32_bf16 v[4:7], v[72:75], a[216:219], v[4:7]\n\t"
    "ds_read_b64_tr_b16 v[108:109], v138 offset:57344\n\t"
    "v_mfma_f32_16x16x32_bf16 v[4:7], v[76:79], a[220:223], v[4:7]\n\t"
    "ds_read_b64_tr_b16 v[110:111], v138 offset:61440\n\t"
    "s_waitcnt lgkmcnt(8)\n\t"
    "v_mfma_f32_16x16x32_bf16 v[8:11], v[80:83], a[192:195], v[8:11]\n\t"
    "ds_read_b64_tr_b16 v[112:113], v139 offset:32768\n\t"
    "v_mfma_f32_16x16x32_bf16 v[8:11], v[84:87], a[196:199], v[8:11]\n\t"
    "ds_read_b64_tr_b16 v[114:115], v139 offset:36864\n\t"
    "v_mfma_f32_16x16x32_bf16 v[8:11], v[88:91], a[208:211], v[8:11]\n\t"
    "ds_read_b64_tr_b16 v[116:117], v139 offset:40960\n\t"
    "v_mfma_f32_16x16x32_bf16 v[8:11], v[92:95], a[212:215], v[8:11]\n\t"
    "ds_read_b64_tr_b16 v[118:119], v139 offset:45056\n\t"
    "v_mfma_f32_16x16x32_bf16 v[12:15], v[80:83], a[200:203], v[12:15]\n\t"
    "ds_read_b64_tr_b16 v[120:121], v139 offset:49152\n\t"
    "v_mfma_f32_16x16x32_bf16 v[12:15], v[84:87], a[204:207], v[12:15]\n\t"
    "ds_read_b64_tr_b16 v[122:123], v139 offset:53248\n\t"
    "v_mfma_f32_16x16x32_bf16 v[12:15], v[88:91], a[216:219], v[12:15]\n\t"
    "ds_read_b64_tr_b16 v[124:125], v139 offset:57344\n\t"
    "v_mfma_f32_16x16x32_bf16 v[12:15], v[92:95], a[220:223], v[12:15]\n\t"
    "ds_read_b64_tr_b16 v[126:127], v139 offset:61440\n\t"
    "s_waitcnt lgkmcnt(8)\n\t"
    "v_mfma_f32_16x16x32_bf16 v[16:19], v[96:99], a[192:195], v[16:19]\n\t"
    "ds_read_b64_tr_b16 v[64:65], v140 offset:32768\n\t"
    "v_mfma_f32_16x16x32_bf16 v[16:19], v[100:103], a[196:199], v[16:19]\n\t"
    "ds_read_b64_tr_b16 v[66:67], v140 offset:36864\n\t"
    "v_mfma_f32_16x16x32_bf16 v[16:19], v[104:107], a[208:211], v[16:19]\n\t"
    "ds_read_b64_tr_b16 v[68:69], v140 offset:40960\n\t"
    "v_mfma_f32_16x16x32_bf16 v[16:19], v[108:111], a[212:215], v[16:19]\n\t"
    "ds_read_b64_tr_b16 v[70:71], v140 offset:45056\n\t"
    "v_mfma_f32_16x16x32_bf16 v[20:23], v[96:99], a[200:203], v[20:23]\n\t"
    "ds_read_b64_tr_b16 v[72:73], v140 offset:49152\n\t"
    "v_mfma_f32_16x16x32_bf16 v[20:23], v[100:103], a[204:207], v[20:23]\n\t"
    "ds_read_b64_tr_b16 v[74:75], v140 offset:53248\n\t"
    "v_mfma_f32_16x16x32_bf16 v[20:23], v[104:107], a[216:219], v[20:23]\n\t"
    "ds_read_b64_tr_b16 v[76:77], v140 offset:57344\n\t"
    "v_mfma_f32_16x16x32_bf16 v[20:23], v[108:111], a[220:223], v[20:23]\n\t"
    "ds_read_b64_tr_b16 v[78:79], v140 offset:61440\n\t"
    "s_waitcnt lgkmcnt(8)\n\t"
    "v_mfma_f32_16x16x32_bf16 v[24:27], v[112:115], a[192:195], v[24:27]\n\t"
    "ds_read_b64_tr_b16 v[80:81], v141 offset:32768\n\t"
    "v_mfma_f32_16x16x32_bf16 v[24:27], v[116:119], a[196:199], v[24:27]\n\t"
    "ds_read_b64_tr_b16 v[82:83], v141 offset:36864\n\t"
    "v_mfma_f32_16x16x32_bf16 v[24:27], v[120:123], a[208:211], v[24:27]\n\t"
    "ds_read_b64_tr_b16 v[84:85], v141 offset:40960\n\t"
    "v_mfma_f32_16x16x32_bf16 v[24:27], v[124:127], a[212:215], v[24:27]\n\t"
    "ds_read_b64_tr_b16 v[86:87], v141 offset:45056\n\t"
    "v_mfma_f32_16x16x32_bf16 v[28:31], v[112:115], a[200:203], v[28:31]\n\t"
    "ds_read_b64_tr_b16 v[88:89], v141 offset:49152\n\t"
    "v_mfma_f32_16x16x32_bf16 v[28:31], v[116:119], a[204:207], v[28:31]\n\t"
    "ds_read_b64_tr_b16 v[90:91], v141 offset:53248\n\t"
    "v_mfma_f32_16x16x32_bf16 v[28:31], v[120:123], a[216:219], v[28:31]\n\t"
    "ds_read_b64_tr_b16 v[92:93], v141 offset:57344\n\t"
    "v_mfma_f32_16x16x32_bf16 v[28:31], v[124:127], a[220:223], v[28:31]\n\t"
    "ds_read_b64_tr_b16 v[94:95], v141 offset:61440\n\t"
    "s_waitcnt lgkmcnt(8)\n\t"
    "v_mfma_f32_16x16x32_bf16 v[32:35], v[64:67], a[192:195], v[32:35]\n\t"
    "ds_read_b64_tr_b16 v[96:97], v142 offset:32768\n\t"
    "v_mfma_f32_16x16x32_bf16 v[32:35], v[68:71], a[196:199], v[32:35]\n\t"
    "ds_read_b64_tr_b16 v[98:99], v142 offset:36864\n\t"
    "v_mfma_f32_16x16x32_bf16 v[32:35], v[72:75], a[208:211], v[32:35]\n\t"
    "ds_read_b64_tr_b16 v[100:101], v142 offset:40960\n\t"
    "v_mfma_f32_16x16x32_bf16 v[32:35], v[76:79], a[212:215], v[32:35]\n\t"
    "ds_read_b64_tr_b16 v[102:103], v142 offset:45056\n\t"
    "v_mfma_f32_16x16x32_bf16 v[36:39], v[64:67], a[200:203], v[36:39]\n\t"
    "ds_read_b64_tr_b16 v[104:105], v142 offset:49152\n\t"
    "v_mfma_f32_16x16x32_bf16 v[36:39], v[68:71], a[204:207], v[36:39]\n\t"
    "ds_read_b64_tr_b16 v[106:107], v142 offset:53248\n\t"
    "v_mfma_f32_16x16x32_bf16 v[36:39], v[72:75], a[216:219], v[36:39]\n\t"
    "ds_read_b64_tr_b16 v[108:109], v142 offset:57344\n\t"
    "v_mfma_f32_16x16x32_bf16 v[36:39], v[76:79], a[220:223], v[36:39]\n\t"
    "ds_read_b64_tr_b16 v[110:111], v142 offset:61440\n\t"
    "s_waitcnt lgkmcnt(8)\n\t"
    "v_mfma_f32_16x16x32_bf16 v[40:43], v[80:83], a[192:195], v[40:43]\n\t"
    "ds_read_b64_tr_b16 v[112:113], v143 offset:32768\n\t"
    "v_mfma_f32_16x16x32_bf16 v[40:43], v[84:87], a[196:199], v[40:43]\n\t"
    "ds_read_b64_tr_b16 v[114:115], v143 offset:36864\n\t"
    "v_mfma_f32_16x16x32_bf16 v[40:43], v[88:91], a[208:211], v[40:43]\n\t"
    "ds_read_b64_tr_b16 v[116:117], v143 offset:40960\n\t"
    "v_mfma_f32_16x16x32_bf16 v[40:43], v[92:95], a[212:215], v[40:43]\n\t"
    "ds_read_b64_tr_b16 v[118:119], v143 offset:45056\n\t"
    "v_mfma_f32_16x16x32_bf16 v[44:47], v[80:83], a[200:203], v[44:47]\n\t"
    "ds_read_b64_tr_b16 v[120:121], v143 offset:49152\n\t"
    "v_mfma_f32_16x16x32_bf16 v[44:47], v[84:87], a[204:207], v[44:47]\n\t"
    "ds_read_b64_tr_b16 v[122:123], v143 offset:53248\n\t"
    "v_mfma_f32_16x16x32_bf16 v[44:47], v[88:91], a[216:219], v[44:47]\n\t"
    "ds_read_b64_tr_b16 v[124:125], v143 offset:57344\n\t"
    "v_mfma_f32_16x16x32_bf16 v[44:47], v[92:95], a[220:223], v[44:47]\n\t"
    "ds_read_b64_tr_b16 v[126:127], v143 offset:61440\n\t"
    "s_waitcnt lgkmcnt(0)\n\t"
    "s_waitcnt vmcnt(0)\n\t"
    "s_barrier\n\t"
    "v_mfma_f32_16x16x32_bf16 v[48:51], v[96:99], a[192:195], v[48:51]\n\t"
    "ds_read_b64_tr_b16 v[64:65], v200 offset:0\n\t"
    "v_mfma_f32_16x16x32_bf16 v[48:51], v[100:103], a[196:199], v[48:51]\n\t"
    "ds_read_b64_tr_b16 v[66:67], v200 offset:4096\n\t"
    "v_mfma_f32_16x16x32_bf16 v[48:51], v[104:107], a[208:211], v[48:51]\n\t"
    "ds_read_b64_tr_b16 v[68:69], v200 offset:8192\n\t"
    "v_mfma_f32_16x16x32_bf16 v[48:51], v[108:111], a[212:215], v[48:51]\n\t"
    "ds_read_b64_tr_b16 v[70:71], v200 offset:12288\n\t"
    "v_mfma_f32_16x16x32_bf16 v[52:55], v[96:99], a[200:203], v[52:55]\n\t"
    "ds_read_b64_tr_b16 v[72:73], v200 offset:16384\n\t"
    "v_mfma_f32_16x16x32_bf16 v[52:55], v[100:103], a[204:207], v[52:55]\n\t"
    "ds_read_b64_tr_b16 v[74:75], v200 offset:20480\n\t"
    "v_mfma_f32_16x16x32_bf16 v[52:55], v[104:107], a[216:219], v[52:55]\n\t"
    "ds_read_b64_tr_b16 v[76:77], v200 offset:24576\n\t"
    "v_mfma_f32_16x16x32_bf16 v[52:55], v[108:111], a[220:223], v[52:55]\n\t"
    "ds_read_b64_tr_b16 v[78:79], v200 offset:28672\n\t"
    "s_waitcnt lgkmcnt(8)\n\t"
    "v_mfma_f32_16x16x32_bf16 v[56:59], v[112:115], a[192:195], v[56:59]\n\t"
    "ds_read_b64_tr_b16 v[80:81], v201 offset:0\n\t"
    "v_mfma_f32_16x16x32_bf16 v[56:59], v[116:119], a[196:199], v[56:59]\n\t"
    "ds_read_b64_tr_b16 v[82:83], v201 offset:4096\n\t"
    "v_mfma_f32_16x16x32_bf16 v[56:59], v[120:123], a[208:211], v[56:59]\n\t"
    "ds_read_b64_tr_b16 v[84:85], v201 offset:8192\n\t"
    "v_mfma_f32_16x16x32_bf16 v[56:59], v[124:127], a[212:215], v[56:59]\n\t"
    "ds_read_b64_tr_b16 v[86:87], v201 offset:12288\n\t"
    "v_mfma_f32_16x16x32_bf16 v[60:63], v[112:115], a[200:203], v[60:63]\n\t"
    "ds_read_b64_tr_b16 v[88:89], v201 offset:16384\n\t"
    "v_mfma_f32_16x16x32_bf16 v[60:63], v[116:119], a[204:207], v[60:63]\n\t"
    "ds_read_b64_tr_b16 v[90:91], v201 offset:20480\n\t"
    "v_mfma_f32_16x16x32_bf16 v[60:63], v[120:123], a[216:219], v[60:63]\n\t"
    "ds_read_b64_tr_b16 v[92:93], v201 offset:24576\n\t"
    "v_mfma_f32_16x16x32_bf16 v[60:63], v[124:127], a[220:223], v[60:63]\n\t"
    "ds_read_b64_tr_b16 v[94:95], v201 offset:28672\n\t"
    "s_waitcnt lgkmcnt(8)\n\t"
    "v_mfma_f32_16x16x32_bf16 v[0:3], v[64:67], a[224:227], v[0:3]\n\t"
    "ds_read_b64_tr_b16 v[96:97], v202 offset:0\n\t"
    "v_mfma_f32_16x16x32_bf16 v[0:3], v[68:71], a[228:231], v[0:3]\n\t"
    "ds_read_b64_tr_b16 v[98:99], v202 offset:4096\n\t"
    "v_mfma_f32_16x16x32_bf16 v[0:3], v[72:75], a[240:243], v[0:3]\n\t"
    "ds_read_b64_tr_b16 v[100:101], v202 offset:8192\n\t"
    "v_mfma_f32_16x16x32_bf16 v[0:3], v[76:79], a[244:247], v[0:3]\n\t"
    "ds_read_b64_tr_b16 v[102:103], v202 offset:12288\n\t"
    "v_mfma_f32_16x16x32_bf16 v[4:7], v[64:67], a[232:235], v[4:7]\n\t"
    "ds_read_b64_tr_b16 v[104:105], v202 offset:16384\n\t"
    "v_mfma_f32_16x16x32_bf16 v[4:7], v[68:71], a[236:239], v[4:7]\n\t"
    "ds_read_b64_tr_b16 v[106:107], v202 offset:20480\n\t"
    "v_mfma_f32_16x16x32_bf16 v[4:7], v[72:75], a[248:251], v[4:7]\n\t"
    "ds_read_b64_tr_b16 v[108:109], v202 offset:24576\n\t"
    "v_mfma_f32_16x16x32_bf16 v[4:7], v[76:79], a[252:255], v[4:7]\n\t"
    "ds_read_b64_tr_b16 v[110:111], v202 offset:28672\n\t"
    "s_waitcnt lgkmcnt(8)\n\t"
    "v_mfma_f32_16x16x32_bf16 v[8:11], v[80:83], a[224:227], v[8:11]\n\t"
    "ds_read_b64_tr_b16 v[112:113], v203 offset:0\n\t"
    "v_mfma_f32_16x16x32_bf16 v[8:11], v[84:87], a[228:231], v[8:11]\n\t"
    "ds_read_b64_tr_b16 v[114:115], v203 offset:4096\n\t"
    "v_mfma_f32_16x16x32_bf16 v[8:11], v[88:91], a[240:243], v[8:11]\n\t"
    "ds_read_b64_tr_b16 v[116:117], v203 offset:8192\n\t"
    "v_mfma_f32_16x16x32_bf16 v[8:11], v[92:95], a[244:247], v[8:11]\n\t"
    "ds_read_b64_tr_b16 v[118:119], v203 offset:12288\n\t"
    "v_mfma_f32_16x16x32_bf16 v[12:15], v[80:83], a[232:235], v[12:15]\n\t"
    "ds_read_b64_tr_b16 v[120:121], v203 offset:16384\n\t"
    "v_mfma_f32_16x16x32_bf16 v[12:15], v[84:87], a[236:239], v[12:15]\n\t"
    "ds_read_b64_tr_b16 v[122:123], v203 offset:20480\n\t"
    "v_mfma_f32_16x16x32_bf16 v[12:15], v[88:91], a[248:251], v[12:15]\n\t"
    "ds_read_b64_tr_b16 v[124:125], v203 offset:24576\n\t"
    "v_mfma_f32_16x16x32_bf16 v[12:15], v[92:95], a[252:255], v[12:15]\n\t"
    "ds_read_b64_tr_b16 v[126:127], v203 offset:28672\n\t"
    "s_waitcnt lgkmcnt(8)\n\t"
    "v_mfma_f32_16x16x32_bf16 v[16:19], v[96:99], a[224:227], v[16:19]\n\t"
    "ds_read_b64_tr_b16 v[64:65], v204 offset:0\n\t"
    "v_mfma_f32_16x16x32_bf16 v[16:19], v[100:103], a[228:231], v[16:19]\n\t"
    "ds_read_b64_tr_b16 v[66:67], v204 offset:4096\n\t"
    "v_mfma_f32_16x16x32_bf16 v[16:19], v[104:107], a[240:243], v[16:19]\n\t"
    "ds_read_b64_tr_b16 v[68:69], v204 offset:8192\n\t"
    "v_cvt_pk_bf16_f32 v160, v0, v1\n\t"
    "v_cvt_pk_bf16_f32 v161, v2, v3\n\t"
    "v_mfma_f32_16x16x32_bf16 v[16:19], v[108:111], a[244:247], v[16:19]\n\t"
    "ds_read_b64_tr_b16 v[70:71], v204 offset:12288\n\t"
    "v_mfma_f32_16x16x32_bf16 v[20:23], v[96:99], a[232:235], v[20:23]\n\t"
    "ds_read_b64_tr_b16 v[72:73], v204 offset:16384\n\t"
    "v_mfma_f32_16x16x32_bf16 v[20:23], v[100:103], a[236:239], v[20:23]\n\t"
    "ds_read_b64_tr_b16 v[74:75], v204 offset:20480\n\t"
    "v_cvt_pk_bf16_f32 v162, v8, v9\n\t"
    "v_cvt_pk_bf16_f32 v163, v10, v11\n\t"
    "v_mfma_f32_16x16x32_bf16 v[20:23], v[104:107], a[248:251], v[20:23]\n\t"
    "ds_read_b64_tr_b16 v[76:77], v204 offset:24576\n\t"
    "v_mfma_f32_16x16x32_bf16 v[20:23], v[108:111], a[252:255], v[20:23]\n\t"
    "ds_read_b64_tr_b16 v[78:79], v204 offset:28672\n\t"
    "s_waitcnt lgkmcnt(8)\n\t"
    "v_mfma_f32_16x16x32_bf16 v[24:27], v[112:115], a[224:227], v[24:27]\n\t"
    "ds_read_b64_tr_b16 v[80:81], v205 offset:0\n\t"
    "v_mfma_f32_16x16x32_bf16 v[24:27], v[116:119], a[228:231], v[24:27]\n\t"
    "ds_read_b64_tr_b16 v[82:83], v205 offset:4096\n\t"
    "v_mfma_f32_16x16x32_bf16 v[24:27], v[120:123], a[240:243], v[24:27]\n\t"
    "ds_read_b64_tr_b16 v[84:85], v205 offset:8192\n\t"
    "s_mov_b32 s53, 0\n\t"
    "buffer_store_dwordx4 v[160:163], v232, s[68:71], s53 offen sc1\n\t"
    "v_mfma_f32_16x16x32_bf16 v[24:27], v[124:127], a[244:247], v[24:27]\n\t"
    "ds_read_b64_tr_b16 v[86:87], v205 offset:12288\n\t"
    "v_mfma_f32_16x16x32_bf16 v[28:31], v[112:115], a[232:235], v[28:31]\n\t"
    "ds_read_b64_tr_b16 v[88:89], v205 offset:16384\n\t"
    "v_mfma_f32_16x16x32_bf16 v[28:31], v[116:119], a[236:239], v[28:31]\n\t"
    "ds_read_b64_tr_b16 v[90:91], v205 offset:20480\n\t"
    "v_cvt_pk_bf16_f32 v164, v4, v5\n\t"
    "v_cvt_pk_bf16_f32 v165, v6, v7\n\t"
    "v_mfma_f32_16x16x32_bf16 v[28:31], v[120:123], a[248:251], v[28:31]\n\t"
    "ds_read_b64_tr_b16 v[92:93], v205 offset:24576\n\t"
    "v_mfma_f32_16x16x32_bf16 v[28:31], v[124:127], a[252:255], v[28:31]\n\t"
    "ds_read_b64_tr_b16 v[94:95], v205 offset:28672\n\t"
    "s_waitcnt lgkmcnt(8)\n\t"
    "v_mfma_f32_16x16x32_bf16 v[32:35], v[64:67], a[224:227], v[32:35]\n\t"
    "ds_read_b64_tr_b16 v[96:97], v206 offset:0\n\t"
    "v_mfma_f32_16x16x32_bf16 v[32:35], v[68:71], a[228:231], v[32:35]\n\t"
    "ds_read_b64_tr_b16 v[98:99], v206 offset:4096\n\t"
    "v_mfma_f32_16x16x32_bf16 v[32:35], v[72:75], a[240:243], v[32:35]\n\t"
    "ds_read_b64_tr_b16 v[100:101], v206 offset:8192\n\t"
    "v_cvt_pk_bf16_f32 v166, v12, v13\n\t"
    "v_cvt_pk_bf16_f32 v167, v14, v15\n\t"
    "v_mfma_f32_16x16x32_bf16 v[32:35], v[76:79], a[244:247], v[32:35]\n\t"
    "ds_read_b64_tr_b16 v[102:103], v206 offset:12288\n\t"
    "v_mfma_f32_16x16x32_bf16 v[36:39], v[64:67], a[232:235], v[36:39]\n\t"
    "ds_read_b64_tr_b16 v[104:105], v206 offset:16384\n\t"
    "v_mfma_f32_16x16x32_bf16 v[36:39], v[68:71], a[236:239], v[36:39]\n\t"
    "ds_read_b64_tr_b16 v[106:107], v206 offset:20480\n\t"
    "s_mov_b32 s53, 65536\n\t"
    "buffer_store_dwordx4 v[164:167], v232, s[68:71], s53 offen sc1\n\t"
    "v_mfma_f32_16x16x32_bf16 v[36:39], v[72:75], a[248:251], v[36:39]\n\t"
    "ds_read_b64_tr_b16 v[108:109], v206 offset:24576\n\t"
    "v_mfma_f32_16x16x32_bf16 v[36:39], v[76:79], a[252:255], v[36:39]\n\t"
    "ds_read_b64_tr_b16 v[110:111], v206 offset:28672\n\t"
    "s_waitcnt lgkmcnt(8)\n\t"
    "v_mfma_f32_16x16x32_bf16 v[40:43], v[80:83], a[224:227], v[40:43]\n\t"
    "ds_read_b64_tr_b16 v[112:113], v207 offset:0\n\t"
    "v_mfma_f32_16x16x32_bf16 v[40:43], v[84:87], a[228:231], v[40:43]\n\t"
    "ds_read_b64_tr_b16 v[114:115], v207 offset:4096\n\t"
    "v_mfma_f32_16x16x32_bf16 v[40:43], v[88:91], a[240:243], v[40:43]\n\t"
    "ds_read_b64_tr_b16 v[116:117], v207 offset:8192\n\t"
    "v_cvt_pk_bf16_f32 v168, v16, v17\n\t"
    "v_cvt_pk_bf16_f32 v169, v18, v19\n\t"
    "v_mfma_f32_16x16x32_bf16 v[40:43], v[92:95], a[244:247], v[40:43]\n\t"
    "ds_read_b64_tr_b16 v[118:119], v207 offset:12288\n\t"
    "v_mfma_f32_16x16x32_bf16 v[44:47], v[80:83], a[232:235], v[44:47]\n\t"
    "ds_read_b64_tr_b16 v[120:121], v207 offset:16384\n\t"
    "v_mfma_f32_16x16x32_bf16 v[44:47], v[84:87], a[236:239], v[44:47]\n\t"
    "ds_read_b64_tr_b16 v[122:123], v207 offset:20480\n\t"
    "v_cvt_pk_bf16_f32 v170, v24, v25\n\t"
    "v_cvt_pk_bf16_f32 v171, v26, v27\n\t"
    "v_mfma_f32_16x16x32_bf16 v[44:47], v[88:91], a[248:251], v[44:47]\n\t"
    "ds_read_b64_tr_b16 v[124:125], v207 offset:24576\n\t"
    "v_mfma_f32_16x16x32_bf16 v[44:47], v[92:95], a[252:255], v[44:47]\n\t"
    "ds_read_b64_tr_b16 v[126:127], v207 offset:28672\n\t"
    "s_waitcnt lgkmcnt(8)\n\t"
    "v_mfma_f32_16x16x32_bf16 v[48:51], v[96:99], a[224:227], v[48:51]\n\t"
    "v_mfma_f32_16x16x32_bf16 v[48:51], v[100:103], a[228:231], v[48:51]\n\t"
    "v_mfma_f32_16x16x32_bf16 v[48:51], v[104:107], a[240:243], v[48:51]\n\t"
    "s_mov_b32 s53, 0\n\t"
    "buffer_store_dwordx4 v[168:171], v232, s[68:71], s53 offen offset:1024 sc1\n\t"
    "v_mfma_f32_16x16x32_bf16 v[48:51], v[108:111], a[244:247], v[48:51]\n\t"
    "v_mfma_f32_16x16x32_bf16 v[52:55], v[96:99], a[232:235], v[52:55]\n\t"
    "v_mfma_f32_16x16x32_bf16 v[52:55], v[100:103], a[236:239], v[52:55]\n\t"
    "v_cvt_pk_bf16_f32 v172, v20, v21\n\t"
    "v_cvt_pk_bf16_f32 v173, v22, v23\n\t"
    "v_mfma_f32_16x16x32_bf16 v[52:55], v[104:107], a[248:251], v[52:55]\n\t"
    "v_mfma_f32_16x16x32_bf16 v[52:55], v[108:111], a[252:255], v[52:55]\n\t"
    "s_waitcnt lgkmcnt(0)\n\t"
    "v_mfma_f32_16x16x32_bf16 v[56:59], v[112:115], a[224:227], v[56:59]\n\t"
    "v_mfma_f32_16x16x32_bf16 v[56:59], v[116:119], a[228:231], v[56:59]\n\t"
    "v_mfma_f32_16x16x32_bf16 v[56:59], v[120:123], a[240:243], v[56:59]\n\t"
    "v_cvt_pk_bf16_f32 v174, v28, v29\n\t"
    "v_cvt_pk_bf16_f32 v175, v30, v31\n\t"
    "v_mfma_f32_16x16x32_bf16 v[56:59], v[124:127], a[244:247], v[56:59]\n\t"
    "v_mfma_f32_16x16x32_bf16 v[60:63], v[112:115], a[232:235], v[60:63]\n\t"
    "v_mfma_f32_16x16x32_bf16 v[60:63], v[116:119], a[236:239], v[60:63]\n\t"
    "s_mov_b32 s53, 65536\n\t"
    "buffer_store_dwordx4 v[172:175], v232, s[68:71], s53 offen offset:1024 sc1\n\t"
    "v_mfma_f32_16x16x32_bf16 v[60:63], v[120:123], a[248:251], v[60:63]\n\t"
    "v_mfma_f32_16x16x32_bf16 v[60:63], v[124:127], a[252:255], v[60:63]\n\t"
    "s_nop 7\n\t"
    "v_cvt_pk_bf16_f32 v160, v32, v33\n\t"
    "v_cvt_pk_bf16_f32 v161, v34, v35\n\t"
    "v_cvt_pk_bf16_f32 v162, v40, v41\n\t"
    "v_cvt_pk_bf16_f32 v163, v42, v43\n\t"
    "s_mov_b32 s53, 0\n\t"
    "buffer_store_dwordx4 v[160:163], v232, s[68:71], s53 offen offset:2048 sc1\n\t"
    "v_cvt_pk_bf16_f32 v164, v36, v37\n\t"
    "v_cvt_pk_bf16_f32 v165, v38, v39\n\t"
    "v_cvt_pk_bf16_f32 v166, v44, v45\n\t"
    "v_cvt_pk_bf16_f32 v167, v46, v47\n\t"
    "s_mov_b32 s53, 65536\n\t"
    "buffer_store_dwordx4 v[164:167], v232, s[68:71], s53 offen offset:2048 sc1\n\t"
    "v_cvt_pk_bf16_f32 v168, v48, v49\n\t"
    "v_cvt_pk_bf16_f32 v169, v50, v51\n\t"
    "v_cvt_pk_bf16_f32 v170, v56, v57\n\t"
    "v_cvt_pk_bf16_f32 v171, v58, v59\n\t"
    "s_mov_b32 s53, 0\n\t"
    "buffer_store_dwordx4 v[168:171], v232, s[68:71], s53 offen offset:3072 sc1\n\t"
    "v_cvt_pk_bf16_f32 v172, v52, v53\n\t"
    "v_cvt_pk_bf16_f32 v173, v54, v55\n\t"
    "v_cvt_pk_bf16_f32 v174, v60, v61\n\t"
    "v_cvt_pk_bf16_f32 v175, v62, v63\n\t"
    "s_mov_b32 s53, 65536\n\t"
    "buffer_store_dwordx4 v[172:175], v232, s[68:71], s53 offen offset:3072 sc1\n\t"
    "s_waitcnt vmcnt(0)\n\t"
    : "+{s76}"(swo), "+{s51}"(shb)
    : "{v[200:207]}"(vb), "{v232}"(lane16), "{v[234:237]}"(wdo),
      "{s[56:59]}"(srdB), "{s[64:67]}"(srdWO), "{s[68:71]}"(srdP), "{s[72:75]}"(srdF), "{s77}"(sfl), "{s78}"(mlw), "{s29}"(w4k), "{s62}"(sownr)
    : "memory", "vcc", "scc", "s52", "s53",
    "v0", "v1", "v2", "v3", "v4", "v5", "v6", "v7", "v8", "v9", "v10", "v11", "v12", "v13", "v14", "v15", "v16", "v17", "v18", "v19", "v20", "v21", "v22", "v23",
    "v24", "v25", "v26", "v27", "v28", "v29", "v30", "v31", "v32", "v33", "v34", "v35", "v36", "v37", "v38", "v39", "v40", "v41", "v42", "v43", "v44", "v45", "v46", "v47",
    "v48", "v49", "v50", "v51", "v52", "v53", "v54", "v55", "v56", "v57", "v58", "v59", "v60", "v61", "v62", "v63", "v64", "v65", "v66", "v67", "v68", "v69", "v70", "v71",
    "v72", "v73", "v74", "v75", "v76", "v77", "v78", "v79", "v80", "v81", "v82", "v83", "v84", "v85", "v86", "v87", "v88", "v89", "v90", "v91", "v92", "v93", "v94", "v95",
    "v96", "v97", "v98", "v99", "v100", "v101", "v102", "v103", "v104", "v105", "v106", "v107", "v108", "v109", "v110", "v111", "v112", "v113", "v114", "v115", "v116", "v117", "v118", "v119",
    "v120", "v121", "v122", "v123", "v124", "v125", "v126", "v127", "v128", "v129", "v130", "v131", "v132", "v133", "v134", "v135", "v136", "v137", "v138", "v139", "v140", "v141", "v142", "v143",
    "v144", "v145", "v146", "v147", "v148", "v149", "v150", "v151", "v152", "v153", "v154", "v155", "v156", "v157", "v158", "v159", "v160", "v161", "v162", "v163", "v164", "v165", "v166", "v167",
    "v168", "v169", "v170", "v171", "v172", "v173", "v174", "v175", "v176", "v177", "v178", "v179", "v180", "v181", "v182", "v183", "v184", "v185", "v186", "v187", "v188", "v189", "v190", "v191",
    "v192", "v193", "v194", "v195", "v196", "v197", "v198", "v199", "a0", "a1", "a2", "a3", "a4", "a5", "a6", "a7", "a8", "a9", "a10", "a11", "a12", "a13", "a14", "a15",
    "a16", "a17", "a18", "a19", "a20", "a21", "a22", "a23", "a24", "a25", "a26", "a27", "a28", "a29", "a30", "a31", "a32", "a33", "a34", "a35", "a36", "a37", "a38", "a39",
    "a40", "a41", "a42", "a43", "a44", "a45", "a46", "a47", "a48", "a49", "a50", "a51", "a52", "a53", "a54", "a55", "a56", "a57", "a58", "a59", "a60", "a61", "a62", "a63",
    "a64", "a65", "a66", "a67", "a68", "a69", "a70", "a71", "a72", "a73", "a74", "a75", "a76", "a77", "a78", "a79", "a80", "a81", "a82", "a83", "a84", "a85", "a86", "a87",
    "a88", "a89", "a90", "a91", "a92", "a93", "a94", "a95", "a96", "a97", "a98", "a99", "a100", "a101", "a102", "a103", "a104", "a105", "a106", "a107", "a108", "a109", "a110", "a111",
    "a112", "a113", "a114", "a115", "a116", "a117", "a118", "a119", "a120", "a121", "a122", "a123", "a124", "a125", "a126", "a127", "a128", "a129", "a130", "a131", "a132", "a133", "a134", "a135",
    "a136", "a137", "a138", "a139", "a140", "a141", "a142", "a143", "a144", "a145", "a146", "a147", "a148", "a149", "a150", "a151", "a152", "a153", "a154", "a155", "a156", "a157", "a158", "a159",
    "a160", "a161", "a162", "a163", "a164", "a165", "a166", "a167", "a168", "a169", "a170", "a171", "a172", "a173", "a174", "a175", "a176", "a177", "a178", "a179", "a180", "a181", "a182", "a183",
    "a184", "a185", "a186", "a187", "a188", "a189", "a190", "a191", "a192", "a193", "a194", "a195", "a196", "a197", "a198", "a199", "a200", "a201", "a202", "a203", "a204", "a205", "a206", "a207",
    "a208", "a209", "a210", "a211", "a212", "a213", "a214", "a215", "a216", "a217", "a218", "a219", "a220", "a221", "a222", "a223", "a224", "a225", "a226", "a227", "a228", "a229", "a230", "a231",
    "a232", "a233", "a234", "a235", "a236", "a237", "a238", "a239", "a240", "a241", "a242", "a243", "a244", "a245", "a246", "a247", "a248", "a249", "a250", "a251", "a252", "a253", "a254", "a255"
  );
}

__global__ __launch_bounds__(512, 1)
void k_tail(const u16* __restrict__ P, const u16* __restrict__ x, const u16* __restrict__ modb, const u16* __restrict__ wr,
            const u16* __restrict__ wg, const u16* __restrict__ wu, const u16* __restrict__ wd,
            u16* __restrict__ out, unsigned* __restrict__ sync) {
  __shared__ __attribute__((aligned(16))) char smem[8192 + 4096];
  asm volatile("" :: "s"(P), "s"(x), "s"(modb), "s"(wr), "s"(wg), "s"(wu), "s"(wd), "s"(out), "s"(sync));
  const int t = threadIdx.x, l = t & 63, wv = t >> 6;
  unsigned klo = 0x00003f80u, khi = 0x3f800000u;
  asm volatile("" : "+v"(klo), "+v"(khi));
  const __amdgpu_buffer_rsrc_t rsSy = __builtin_amdgcn_make_buffer_rsrc(sync, 0, SY_WORDS * 4, 0x00020000);
  const __amdgpu_buffer_rsrc_t rsOut = __builtin_amdgcn_make_buffer_rsrc(out, 0, 2048 * 512 * 2, 0x00020000);
  const int j = blockIdx.x, n = wv & 3, c8 = l & 7, g = (l >> 3) & 3, nbp = (l >> 5) + 2 * (wv >> 2);
  const bool is_gu = j < NGU, is_dn = j >= NGU && j < NGU + NDN;
  const int row = 8 * j + c8, col0 = 128 * n + 32 * nbp + 4 * g;
  u32x2 xv[2], ga[2], sb[2], cb[2], gb[2], wv_[2];
  #pragma unroll
  for (int k = 0; k < 2; ++k) {
    const int col = col0 + 16 * k;
    cb[k] = *(const u32x2*)(modb + 2048 + col); wv_[k] = *(const u32x2*)(wr + col); sb[k] = *(const u32x2*)(modb + 1536 + col);
    xv[k] = *(const u32x2*)(x + row * 512 + col); ga[k] = *(const u32x2*)(modb + 1024 + col); gb[k] = *(const u32x2*)(modb + 2560 + col);
  }
  const char* pb = (const char*)P + (size_t)(j >> 1) * 65536 + n * 4096 + nbp * 1024 + (16 * g + 8 * (j & 1) + c8) * 16;
  u32x4 pv[4];
  #pragma unroll
  for (int s = 0; s < 4; ++s) pv[s] = *(const u32x4*)(pb + s * 16384);
  const int eb = (j & 7) * 7 + (j >> 3), f0 = eb * 32, cq = t & 3, r = t >> 2;
  const int db = j - NGU, cg = (db & 7) * 8 + (db >> 3), tt = t < 448 ? t : 447;
  u32x4 w4[8]; u32x2 mw[4];
  __builtin_amdgcn_sched_barrier(0);
  float u[8], uw[8], bb = 0, cc_ = 0;
  #pragma unroll
  for (int e = 0; e < 8; ++e) {
    const unsigned cw = cb[e >> 2][(e >> 1) & 1], ww = wv_[e >> 2][(e >> 1) & 1], sw = sb[e >> 2][(e >> 1) & 1];
    const float w_ = (e & 1) ? bf_hi(ww) : bf_lo(ww), s_ = (e & 1) ? bf_hi(sw) : bf_lo(sw);
    u[e] = rbf(1.f + ((e & 1) ? bf_hi(cw) : bf_lo(cw)));
    uw[e] = u[e] * w_; bb += uw[e]; cc_ = fmaf(s_, w_, cc_);
  }
  bb = sum_hi3(bb); cc_ = sum_hi3(cc_);
  __builtin_amdgcn_sched_barrier(0);
  float v[8];
  #pragma unroll
  for (int e = 0; e < 4; ++e) {
    float a0 = bf_lo(pv[0][e]), a1 = bf_hi(pv[0][e]);
    #pragma unroll
    for (int s = 1; s < 4; ++s) { a0 = addsel(pv[s][e], klo, a0); a1 = addsel(pv[s][e], khi, a1); }
    v[2 * e] = fmaf(bf_lo(ga[e >> 1][e & 1]), a0, bf_lo(xv[e >> 1][e & 1]));
    v[2 * e + 1] = fmaf(bf_hi(ga[e >> 1][e & 1]), a1, bf_hi(xv[e >> 1][e & 1]));
  }
  float* red = (float*)(smem + 8192);
  const float mw_ = sum_hi3(((v[0] + v[1]) + (v[2] + v[3])) + ((v[4] + v[5]) + (v[6] + v[7]))) * (1.f / 64.f);
  float m2 = 0, aa = 0;
  #pragma unroll
  for (int e = 0; e < 8; ++e) { const float d = v[e] - mw_; m2 = fmaf(d, d, m2); aa = fmaf(d, uw[e], aa); }
  m2 = sum_hi3(m2); aa = sum_hi3(aa);
  if (l < 8) { *(f32x4*)(red + (wv * 8 + l) * 8) = f32x4{mw_, m2, aa, bb}; red[(wv * 8 + l) * 8 + 4] = cc_; }
  __syncthreads();
  float mean, rs, rr;
  {
    f32x4 q[8]; float cs = 0, ms = 0;
    #pragma unroll
    for (int w = 0; w < 8; ++w) { q[w] = *(const f32x4*)(red + (w * 8 + c8) * 8); cs += red[(w * 8 + c8) * 8 + 4]; ms += q[w][0]; }
    mean = ms * 0.125f;
    float M2 = 0, A = 0;
    #pragma unroll
    for (int w = 0; w < 8; ++w) { const float dm = q[w][0] - mean; M2 += fmaf(64.f * dm, dm, q[w][1]); A += fmaf(dm, q[w][3], q[w][2]); }
    rs = __builtin_amdgcn_rsqf(M2 * (1.f / 512.f) + 1e-5f);
    rr = fmaf(rs, A, cs);
  }
  if (t < 64) {
    float bv = rr; unsigned bi = c8;
    #pragma unroll
    for (int o = 1; o < 8; o <<= 1) {
      const int ctl = o == 1 ? 0xb1 : o == 2 ? 0x4e : 0x141;
      const float ov = __builtin_bit_cast(float, o == 1 ? __builtin_amdgcn_update_dpp(0, __builtin_bit_cast(int, bv), 0xb1, 0xf, 0xf, true)
                                               : o == 2 ? __builtin_amdgcn_update_dpp(0, __builtin_bit_cast(int, bv), 0x4e, 0xf, 0xf, true)
                                                        : __builtin_amdgcn_update_dpp(0, __builtin_bit_cast(int, bv), 0x141, 0xf, 0xf, true));
      const unsigned oi = (unsigned)(o == 1 ? __builtin_amdgcn_update_dpp(0, (int)bi, 0xb1, 0xf, 0xf, true)
                                   : o == 2 ? __builtin_amdgcn_update_dpp(0, (int)bi, 0x4e, 0xf, 0xf, true)
                                            : __builtin_amdgcn_update_dpp(0, (int)bi, 0x141, 0xf, 0xf, true));
      (void)ctl;
      if (better(ov, oi, bv, bi)) { bv = ov; bi = oi; }
    }
    if (t == (int)bi) {
      const unsigned rb = __float_as_uint(rs);
      u32x4 gr = {__float_as_uint(bv), (unsigned)(8 * j) + bi + 1u, __float_as_uint(mean), rb ? rb : 1u};
      __builtin_amdgcn_raw_buffer_store_b128(gr, rsSy, j * 16, 0, 16);
    }
  }
  auto load_role = [&]() {
    const u16* pa = is_gu ? wg + (size_t)(4 * r) * 1792 + f0 + cq * 8 : wd + (size_t)(4 * tt) * 512 + cg * 8;
    const u16* pc = is_gu ? wu + (size_t)(4 * r) * 1792 + f0 + cq * 8 : modb + 2560 + cg * 8;
    const int sa_ = is_gu ? 1792 : 512, sc_ = is_gu ? 1792 : 0;
    #pragma unroll
    for (int i = 0; i < 4; ++i) { w4[i] = *(const u32x4*)(pa + i * sa_); w4[4 + i] = *(const u32x4*)(pc + i * sc_); }
    #pragma unroll
    for (int i = 0; i < 4; ++i) mw[i] = *(const u32x2*)(modb + 1024 + 512 * i + 4 * (r & 127));
  };
  const bool poller = is_gu && rfl(wv) == 0;
  __builtin_amdgcn_sched_barrier(0);
  if ((is_gu || is_dn) && !poller) load_role();
  __builtin_amdgcn_sched_barrier(0);
  #pragma unroll
  for (int k = 0; k < 2; ++k) {
    float ov[4];
    #pragma unroll
    for (int e = 0; e < 4; ++e) {
      const unsigned sw = sb[k][e >> 1], gw = gb[k][e >> 1];
      const float s_ = (e & 1) ? bf_hi(sw) : bf_lo(sw), g_ = (e & 1) ? bf_hi(gw) : bf_lo(gw);
      const float hv = rbf(fmaf((v[4 * k + e] - mean) * rs, u[4 * k + e], s_));
      ov[e] = fmaf(g_, hv, v[4 * k + e]);
    }
    u32x2 ow = {pk2(ov[0], ov[1]), pk2(ov[2], ov[3])};
    *(u32x2*)(smem + c8 * 1024 + (col0 + 16 * k) * 2) = ow;
  }
  __syncthreads();
  __builtin_amdgcn_raw_buffer_store_b128(*(const u32x4*)(smem + t * 16), rsOut, j * 8192 + t * 16, 0, 16);
  if (!is_gu) {
    asm volatile("s_waitcnt vmcnt(0)" ::: "memory");
    __syncthreads();
    if (t == 0) __hip_atomic_store((gu32*)sync + SY_DONE + j, 1u, RLX_AGENT);
  }
  if (is_gu) {
    float* sred = (float*)(smem + 8192);
    unsigned* sbest = (unsigned*)(smem + 8192 + 2048);
    if (t < 64) {
      u32x4 b4;
      for (unsigned spins = 0; spins < SPIN_CAP; ++spins) {
        u32x4 q[4];
        asm volatile("" ::: "memory");
        #pragma unroll
        for (int k = 0; k < 4; ++k) q[k] = __builtin_amdgcn_raw_buffer_load_b128(rsSy, (64 * k + l) * 16, 0, 16);
        bool ok = true;
        #pragma unroll
        for (int k = 0; k < 4; ++k) ok = ok && q[k][1] && q[k][3];
        b4 = q[0];
        #pragma unroll
        for (int k = 1; k < 4; ++k) if (better(__uint_as_float(q[k][0]), q[k][1], __uint_as_float(b4[0]), b4[1])) b4 = q[k];
        if (__all(ok)) break;
        __builtin_amdgcn_s_sleep(1);
      }
      float bv = __uint_as_float(b4[0]); unsigned bi = b4[1];
      argmax64(bv, bi);
      if (b4[1] == bi) { sbest[0] = (bi - 1u) & 2047u; sbest[1] = b4[0]; sbest[2] = b4[2]; sbest[3] = b4[3]; }
    }
    asm volatile("s_waitcnt vmcnt(0)" ::: "memory");
    __syncthreads();
    if (t == 0) __hip_atomic_store((gu32*)sync + SY_DONE + j, 1u, RLX_AGENT);
    const unsigned idx = sbest[0];
    const float smean = __uint_as_float(sbest[2]), srs = __uint_as_float(sbest[3]);
    if (poller) load_role();
    float xs[4];
    {
      const int col = 4 * r;
      const char* qb = (const char*)P + (size_t)(idx >> 4) * 65536 + (col >> 7) * 4096 + ((col >> 5) & 3) * 1024
                     + (16 * ((col >> 2) & 3) + (idx & 15)) * 16 + 8 * ((col >> 4) & 1);
      u32x2 pw[4];
      #pragma unroll
      for (int s = 0; s < 4; ++s) pw[s] = *(const u32x2*)(qb + s * 16384);
      const u32x2 xw = *(const u32x2*)(x + idx * 512 + col);
      __builtin_amdgcn_sched_barrier(0);
      float bs_[4];
      #pragma unroll
      for (int e = 0; e < 2; ++e) {
        float a0 = bf_lo(pw[0][e]), a1 = bf_hi(pw[0][e]);
        #pragma unroll
        for (int s = 1; s < 4; ++s) { a0 = addsel(pw[s][e], klo, a0); a1 = addsel(pw[s][e], khi, a1); }
        const float v0 = fmaf(bf_lo(mw[0][e]), a0, bf_lo(xw[e])), v1 = fmaf(bf_hi(mw[0][e]), a1, bf_hi(xw[e]));
        xs[2 * e] = rbf(fmaf((v0 - smean) * srs, rbf(1.f + bf_lo(mw[2][e])), bf_lo(mw[1][e])));
        xs[2 * e + 1] = rbf(fmaf((v1 - smean) * srs, rbf(1.f + bf_hi(mw[2][e])), bf_hi(mw[1][e])));
        bs_[2 * e] = fmaf(bf_lo(mw[3][e]), xs[2 * e], v0); bs_[2 * e + 1] = fmaf(bf_hi(mw[3][e]), xs[2 * e + 1], v1);
      }
      if (j == 0 && cq < 2) {
        u32x4 gq = {__float_as_uint(cq ? bs_[2] : bs_[0]), 1u, __float_as_uint(cq ? bs_[3] : bs_[1]), 1u};
        __builtin_amdgcn_raw_buffer_store_b128(gq, rsSy, SY_BASE * 4 + (col + 2 * cq) * 8, 0, 16);
        if (t == 0) __hip_atomic_store((gu64*)(sync + SY_HDR), ((unsigned long long)(idx + 1u) << 32) | sbest[1], RLX_AGENT);
      }
    }
    float a[16];
    #pragma unroll
    for (int e = 0; e < 16; ++e) a[e] = 0;
    #pragma unroll
    for (int i = 0; i < 4; ++i)
      #pragma unroll
      for (int e = 0; e < 4; ++e) {
        a[2 * e] += xs[i] * bf_lo(w4[i][e]); a[2 * e + 1] += xs[i] * bf_hi(w4[i][e]);
        a[8 + 2 * e] += xs[i] * bf_lo(w4[4 + i][e]); a[8 + 2 * e + 1] += xs[i] * bf_hi(w4[4 + i][e]);
      }
    {
      float b[8], c[4], d[2];
      #pragma unroll
      for (int i = 0; i < 8; ++i) b[i] = fold32(a[i], a[i + 8]);
      #pragma unroll
      for (int i = 0; i < 4; ++i) c[i] = fold16(b[i], b[i + 4]);
      #pragma unroll
      for (int i = 0; i < 2; ++i) d[i] = fold8(c[i], c[i + 2], l & 8);
      const float e1 = fold4(d[0], d[1], l & 4);
      sred[wv * 64 + (l >> 5) * 32 + cq * 8 + ((l >> 4) & 1) * 4 + ((l >> 3) & 1) * 2 + ((l >> 2) & 1)] = e1;
    }
    __syncthreads();
    if (t < 32) {
      float gs = 0, us = 0;
      #pragma unroll
      for (int w = 0; w < 8; ++w) { gs += sred[64 * w + t]; us += sred[64 * w + 32 + t]; }
      const float av = rbf(rbf(silu_f(rbf(gs))) * rbf(us));
      const float an = dppf<0xb1>(av);
      if ((t & 1) == 0) __hip_atomic_store((gu64*)(sync + SY_ACT) + (f0 + t) / 2, (1ull << 32) | pk2(av, an), RLX_AGENT);
    }
  } else if (is_dn) {
    u32x4 q0, bs; unsigned long long hd;
    for (unsigned spins = 0; spins < SPIN_CAP; ++spins) {
      asm volatile("" ::: "memory");
      q0 = __builtin_amdgcn_raw_buffer_load_b128(rsSy, SY_ACT * 4 + tt * 16, 0, 16);
      hd = __hip_atomic_load((gu64*)(sync + SY_HDR), RLX_AGENT);
      bs = __builtin_amdgcn_raw_buffer_load_b128(rsSy, SY_BASE * 4 + (cg * 8 + 2 * (l & 3)) * 8, 0, 16);
      if (__all(q0[1] && q0[3] && (hd >> 32) && bs[1] && bs[3])) break;
      __builtin_amdgcn_s_sleep(2);
    }
    const unsigned idx = ((unsigned)(hd >> 32) - 1u) & 2047u;
    const unsigned done = __hip_atomic_load((gu32*)sync + SY_DONE + (idx >> 3), RLX_AGENT);
    float y[8];
    #pragma unroll
    for (int e = 0; e < 8; ++e) y[e] = 0;
    #pragma unroll
    for (int i = 0; i < 4; ++i) {
      const unsigned aw = q0[2 * (i >> 1)];
      const float av = t < 448 ? ((i & 1) ? bf_hi(aw) : bf_lo(aw)) : 0.f;
      #pragma unroll
      for (int e = 0; e < 4; ++e) { y[2 * e] += av * bf_lo(w4[i][e]); y[2 * e + 1] += av * bf_hi(w4[i][e]); }
    }
    float* sred = (float*)smem;
    {
      float b[4], c[2];
      #pragma unroll
      for (int i = 0; i < 4; ++i) b[i] = fold32(y[i], y[i + 4]);
      #pragma unroll
      for (int i = 0; i < 2; ++i) c[i] = fold16(b[i], b[i + 2]);
      float d = fold8(c[0], c[1], l & 8);
      d += dppf<0xb1>(d); d += dppf<0x4e>(d); d += dppf<0x141>(d);
      if ((l & 7) == 0) sred[wv * 8 + (l >> 5) * 4 + ((l >> 4) & 1) * 2 + ((l >> 3) & 1)] = d;
    }
    __syncthreads();
    if (t < 4) {
      if (!done)
        for (unsigned spins = 0; spins < SPIN_CAP; ++spins) {
          if (__hip_atomic_load((gu32*)sync + SY_DONE + (idx >> 3), RLX_AGENT)) break;
          __builtin_amdgcn_s_sleep(1);
        }
      const float sg = rbf(1.f / (1.f + __expf(-rbf(__uint_as_float((unsigned)hd)))));
      float y0 = 0, y1 = 0;
      #pragma unroll
      for (int w = 0; w < 8; ++w) { y0 += sred[8 * w + 2 * t]; y1 += sred[8 * w + 2 * t + 1]; }
      const unsigned gw = t == 0 ? w4[4][0] : t == 1 ? w4[4][1] : t == 2 ? w4[4][2] : w4[4][3];
      const float o0 = fmaf(bf_lo(gw), rbf(rbf(y0) * sg), __uint_as_float(bs[0])), o1 = fmaf(bf_hi(gw), rbf(rbf(y1) * sg), __uint_as_float(bs[2]));
      __hip_atomic_store((gu32*)(out + idx * 512 + cg * 8 + 2 * t), pk2(o0, o1), RLX_AGENT);
    }
  }
  asm volatile("Lcode_end:");
}

extern "C" void kernel_launch(void* const* d_in, const int* in_sizes, int n_in, void* d_out, int out_size, void* d_ws, size_t ws_size, hipStream_t stream) {
  const u16* x = (const u16*)d_in[0]; const u16* kc = (const u16*)d_in[1]; const u16* vc = (const u16*)d_in[2];
  const u16* wq = (const u16*)d_in[3]; const u16* wo = (const u16*)d_in[4];
  const u16* wr = (const u16*)d_in[6]; const u16* wg = (const u16*)d_in[7]; const u16* wu = (const u16*)d_in[8]; const u16* wd = (const u16*)d_in[9];
  const u16* c = (const u16*)d_in[10]; const u16* wm = (const u16*)d_in[11];
  char* ws = (char*)d_ws;
  float* stats = (float*)(ws + WS_STAT); u16* modb = (u16*)(ws + WS_MODB); unsigned* sync = (unsigned*)(ws + WS_SYNC);
  u16* hf = (u16*)(ws + WS_H); u16* of = (u16*)(ws + WS_O); u16* pp = (u16*)(ws + WS_P);
  (void)hipFuncSetAttribute((const void*)k_attn, hipFuncAttributeMaxDynamicSharedMemorySize, (int)ATTN_LDS);
  k_mod<<<352, 256, 0, stream>>>(c, wm, modb, sync, wq, x, stats, wr);
  k_ln1<<<512 / LN1_R, 256, 0, stream>>>(x, modb, stats, hf);
  k_attn<<<256, 256, ATTN_LDS, stream>>>(hf, wq, kc, vc, of, wo, wg, wu, wd, pp, sync);
  k_tail<<<256, 512, 0, stream>>>(pp, x, modb, wr, wg, wu, wd, (u16*)d_out, sync);
}
```
